```python
import torch

def _softmax(x, axis=-1):
    return torch.softmax(x, dim=axis)
def _gelu(x):
    return torch.nn.functional.gelu(x, approximate="tanh")
def _silu(x):
    return torch.nn.functional.silu(x)
def _sigmoid(x):
    return torch.sigmoid(x)
def _layernorm(x, axis=-1, eps=1e-5):
    m = x.mean(dim=axis, keepdim=True)
    v = x.var(dim=axis, keepdim=True, unbiased=False)
    return (x - m) / torch.sqrt(v + eps)
def _scatter_add(tgt, idx, src, axis=0):
    return tgt.clone().index_add_(axis, idx, src)
def _segment_sum(src, seg, n):
    out = torch.zeros((n,) + src.shape[1:], dtype=src.dtype)
    return out.index_add_(0, seg, src)
def _pad(x, pw):
    flat = tuple(v for pair in reversed(pw) for v in pair)
    return torch.nn.functional.pad(x, flat)

kernel_name = 'parallel_sum__linear_attn_block__mixer_token_mlp__prompt'

def setup_inputs():

    _w = (lambda a: (lambda t: t.to(torch.bfloat16) if t.is_floating_point() else t)(torch.as_tensor(a)))
    def _A_setup():
        B, S, D = 1, 2048, 640
        return {
            "x": _w((lambda *s: torch.randn(*s, dtype=torch.bfloat16) * 0.05)(B, S, D)),
            "wqkv": _w((lambda *s: torch.randn(*s, dtype=torch.bfloat16) * 0.05)(D, 3 * D)),
            "wo": _w((lambda *s: torch.randn(*s, dtype=torch.bfloat16) * 0.05)(D, D)),
        }
    _A_in = _A_setup()

    def _B_setup():
        B, S, D, F = 1, 2048, 640, 1728
        return {
            "x": _w((lambda *s: torch.randn(*s, dtype=torch.bfloat16) * 0.05)(B, S, D)),
            "w1": _w((lambda *s: torch.randn(*s, dtype=torch.bfloat16) * 0.4)(S, F) * (S ** -0.5)),
            "b1": _w((lambda *s: torch.randn(*s, dtype=torch.bfloat16) * 3.2)(F) * 0.02),
            "w2": _w((lambda *s: torch.randn(*s, dtype=torch.bfloat16) * 0.2)(F, S) * (F ** -0.5)),
            "b2": _w((lambda *s: torch.randn(*s, dtype=torch.bfloat16) * 0.4)(S) * 0.02),
        }
    _B_in = _B_setup()

    return {
        "A_x": _A_in["x"],
        "A_wqkv": _A_in["wqkv"],
        "A_wo": _A_in["wo"],
        "B_x": _B_in["x"],
        "B_w1": _B_in["w1"],
        "B_b1": _B_in["b1"],
        "B_w2": _B_in["w2"],
        "B_b2": _B_in["b2"],
    }

def reference(A_x, A_wqkv, A_wo, B_x, B_w1, B_b1, B_w2, B_b2):
    def _A_ref(x, wqkv, wo):
        B, S, D = x.shape
        H = 32; Dh = D // H
        qkv = (lambda x, *d: x.permute(*d))((x @ wqkv).reshape(B, S, 3, H, Dh), 2, 0, 3, 1, 4)
        q, k, v = qkv[0], qkv[1], qkv[2]
        phi = lambda t: torch.where(t > 0, t + 1, torch.exp(t)) + 1e-6
        pq, pk = phi(q), phi(k)
        kv = torch.einsum("bhsd,bhse->bhde", pk, v)
        z = torch.einsum("bhsd->bhd", pk)
        num = torch.einsum("bhqd,bhde->bhqe", pq, kv)
        den = torch.einsum("bhqd,bhd->bhq", pq, z)[..., None] + 1e-6
        o = num / den
        return (lambda x, *d: x.permute(*d))(o, 0, 2, 1, 3).reshape(B, S, D) @ wo

    def _B_ref(x, w1, b1, w2, b2):
        B, S, D = x.shape
        h = _layernorm(x, axis=-1)
        u = torch.einsum("bsd,sf->bdf", h, w1) + b1
        u = _gelu(u)
        y = torch.einsum("bdf,fs->bds", u, w2) + b2
        return (lambda x, *d: x.permute(*d))(y, 0, 2, 1)

    x = A_x
    xn = _layernorm(x, axis=-1)
    return x + _A_ref(xn, A_wqkv, A_wo) + _B_ref(xn, B_w1, B_b1, B_w2, B_b2)
```

```cpp
#include <hip/hip_runtime.h>
#include <hip/hip_bf16.h>
typedef __hip_bfloat16 bf16;
constexpr int S=2048, D=640, F=1728, H=32, DH=20;
__device__ __forceinline__ float b2f(bf16 v){ return __bfloat162float(v); }

__global__ void ln_k(const bf16* x, float* xn, float* h){
  int s=blockIdx.x; int t=threadIdx.x;
  float v[10]; float sum=0;
  for(int i=0;i<10;i++){ v[i]=b2f(x[s*D+t+64*i]); sum+=v[i]; }
  for(int o=32;o;o>>=1) sum+=__shfl_xor(sum,o);
  float m=sum/D; float q=0;
  for(int i=0;i<10;i++){ float d=v[i]-m; q+=d*d; }
  for(int o=32;o;o>>=1) q+=__shfl_xor(q,o);
  float r=1.f/sqrtf(q/D+1e-5f);
  sum=0;
  for(int i=0;i<10;i++){ v[i]=(v[i]-m)*r; xn[s*D+t+64*i]=v[i]; sum+=v[i]; }
  for(int o=32;o;o>>=1) sum+=__shfl_xor(sum,o);
  m=sum/D; q=0;
  for(int i=0;i<10;i++){ float d=v[i]-m; q+=d*d; }
  for(int o=32;o;o>>=1) q+=__shfl_xor(q,o);
  r=1.f/sqrtf(q/D+1e-5f);
  for(int i=0;i<10;i++){ h[s*D+t+64*i]=(v[i]-m)*r; }
}
__global__ void qkv_k(const float* xn, const bf16* w, float* qkv){
  int n=blockIdx.x*64+threadIdx.x; int s=blockIdx.y;
  float a=0; for(int k=0;k<D;k++) a+=xn[s*D+k]*b2f(w[k*3*D+n]);
  if(n<2*D){ a=(a>0?a+1.f:__expf(a))+1e-6f; }
  qkv[s*3*D+n]=a;
}
__global__ void kv_k(const float* qkv, float* kv, float* z){
  int hh=blockIdx.x; int t=threadIdx.x;
  if(t<400){ int d=t/20,e=t%20; float a=0; for(int s=0;s<S;s++) a+=qkv[s*3*D+D+hh*20+d]*qkv[s*3*D+2*D+hh*20+e]; kv[hh*400+t]=a; }
  else if(t<420){ int d=t-400; float a=0; for(int s=0;s<S;s++) a+=qkv[s*3*D+D+hh*20+d]; z[hh*20+d]=a; }
}
__global__ void o_k(const float* qkv, const float* kv, const float* z, float* o){
  int c=blockIdx.x*64+threadIdx.x; int s=blockIdx.y; int hh=c/20,e=c%20;
  float num=0,den=0; for(int d=0;d<20;d++){ float p=qkv[s*3*D+hh*20+d]; num+=p*kv[hh*400+d*20+e]; den+=p*z[hh*20+d]; }
  o[s*D+c]=num/(den+1e-6f);
}
__global__ void g_k(const float* h, const bf16* w1, const bf16* b1, float* g){
  int f=blockIdx.x*64+threadIdx.x; int d=blockIdx.y;
  float a=0; for(int s=0;s<S;s++) a+=h[s*D+d]*b2f(w1[s*F+f]);
  a+=b2f(b1[f]);
  float u=0.7978845608028654f*(a+0.044715f*a*a*a);
  g[d*F+f]=0.5f*a*(1.f+tanhf(u));
}
__global__ void out_k(const bf16* x, const float* o, const bf16* wo, const float* g, const bf16* w2, const bf16* b2, bf16* out){
  int d=blockIdx.x*64+threadIdx.x; int s=blockIdx.y;
  float a=0; for(int k=0;k<D;k++) a+=o[s*D+k]*b2f(wo[k*D+d]);
  float y=0; for(int f=0;f<F;f++) y+=g[d*F+f]*b2f(w2[f*S+s]);
  out[s*D+d]=__float2bfloat16(b2f(x[s*D+d])+a+y+b2f(b2[s]));
}
extern "C" void kernel_launch(void* const* d_in, const int* in_sizes, int n_in, void* d_out, int out_size, void* d_ws, size_t ws_size, hipStream_t stream){
  const bf16* x=(const bf16*)d_in[0]; const bf16* wqkv=(const bf16*)d_in[1]; const bf16* wo=(const bf16*)d_in[2];
  const bf16* w1=(const bf16*)d_in[4]; const bf16* b1=(const bf16*)d_in[5]; const bf16* w2=(const bf16*)d_in[6]; const bf16* b2=(const bf16*)d_in[7];
  float* ws=(float*)d_ws;
  float* xn=ws; float* h=xn+S*D; float* qkv=h+S*D; float* kv=qkv+S*3*D; float* z=kv+H*400; float* o=z+H*20; float* g=o+S*D;
  ln_k<<<S,64,0,stream>>>(x,xn,h);
  qkv_k<<<dim3(30,S),64,0,stream>>>(xn,wqkv,qkv);
  kv_k<<<H,448,0,stream>>>(qkv,kv,z);
  o_k<<<dim3(10,S),64,0,stream>>>(qkv,kv,z,o);
  g_k<<<dim3(27,D),64,0,stream>>>(h,w1,b1,g);
  out_k<<<dim3(10,S),64,0,stream>>>(x,o,wo,g,w2,b2,(bf16*)d_out);
}
```

```cpp
#define PROBE 0
#include <hip/hip_runtime.h>
#include <hip/hip_bf16.h>
typedef __hip_bfloat16 bf16;
constexpr int S=2048, D=640, F=1728, H=32, DH=20;
__device__ __forceinline__ float b2f(bf16 v){ return __bfloat162float(v); }

__global__ void ln_k(const bf16* x, float* xn, float* h){
  int s=blockIdx.x; int t=threadIdx.x;
  float v[10]; float sum=0;
  for(int i=0;i<10;i++){ v[i]=b2f(x[s*D+t+64*i]); sum+=v[i]; }
  for(int o=32;o;o>>=1) sum+=__shfl_xor(sum,o);
  float m=sum/D; float q=0;
  for(int i=0;i<10;i++){ float d=v[i]-m; q+=d*d; }
  for(int o=32;o;o>>=1) q+=__shfl_xor(q,o);
  float r=1.f/sqrtf(q/D+1e-5f);
  sum=0;
  for(int i=0;i<10;i++){ v[i]=(v[i]-m)*r; xn[s*D+t+64*i]=v[i]; sum+=v[i]; }
  for(int o=32;o;o>>=1) sum+=__shfl_xor(sum,o);
  m=sum/D; q=0;
  for(int i=0;i<10;i++){ float d=v[i]-m; q+=d*d; }
  for(int o=32;o;o>>=1) q+=__shfl_xor(q,o);
  r=1.f/sqrtf(q/D+1e-5f);
  for(int i=0;i<10;i++){ h[s*D+t+64*i]=(v[i]-m)*r; }
}
__global__ void qkv_k(const float* xn, const bf16* w, float* qkv){
  int n=blockIdx.x*64+threadIdx.x; int s=blockIdx.y;
  float a=0; for(int k=0;k<D;k++) a+=xn[s*D+k]*b2f(w[k*3*D+n]);
  if(n<2*D){ a=(a>0?a+1.f:__expf(a))+1e-6f; }
  qkv[s*3*D+n]=a;
}
__global__ void kv_k(const float* qkv, float* kv, float* z){
  int hh=blockIdx.x; int t=threadIdx.x;
  if(t<400){ int d=t/20,e=t%20; float a=0; for(int s=0;s<S;s++) a+=qkv[s*3*D+D+hh*20+d]*qkv[s*3*D+2*D+hh*20+e]; kv[hh*400+t]=a; }
  else if(t<420){ int d=t-400; float a=0; for(int s=0;s<S;s++) a+=qkv[s*3*D+D+hh*20+d]; z[hh*20+d]=a; }
}
__global__ void o_k(const float* qkv, const float* kv, const float* z, float* o){
  int c=blockIdx.x*64+threadIdx.x; int s=blockIdx.y; int hh=c/20,e=c%20;
  float num=0,den=0; for(int d=0;d<20;d++){ float p=qkv[s*3*D+hh*20+d]; num+=p*kv[hh*400+d*20+e]; den+=p*z[hh*20+d]; }
  o[s*D+c]=num/(den+1e-6f);
}
__global__ void g_k(const float* h, const bf16* w1, const bf16* b1, float* g){
  int f=blockIdx.x*64+threadIdx.x; int d=blockIdx.y;
  float a=0; for(int s=0;s<S;s++) a+=h[s*D+d]*b2f(w1[s*F+f]);
  a+=b2f(b1[f]);
  float u=0.7978845608028654f*(a+0.044715f*a*a*a);
  g[d*F+f]=0.5f*a*(1.f+tanhf(u));
}
__global__ void out_k(const bf16* x, const float* o, const bf16* wo, const float* g, const bf16* w2, const bf16* b2, bf16* out){
  int d=blockIdx.x*64+threadIdx.x; int s=blockIdx.y;
  float a=0; for(int k=0;k<D;k++) a+=o[s*D+k]*b2f(wo[k*D+d]);
  float y=0; for(int f=0;f<F;f++) y+=g[d*F+f]*b2f(w2[f*S+s]);
  out[s*D+d]=__float2bfloat16(b2f(x[s*D+d])+a+y+b2f(b2[s]));
}

typedef unsigned v4u __attribute__((ext_vector_type(4)));
typedef unsigned long long u64;
__device__ __forceinline__ v4u rd(const void* p, size_t bytes, int b, int t){
  size_t per=bytes/256; const char* q=(const char*)p+per*b; v4u acc={0,0,0,0};
  #pragma unroll 8
  for(size_t o=t*16;o<per;o+=4096) acc^=*(const v4u*)(q+o);
  return acc;
}
__global__ __launch_bounds__(256) void probe_k(const bf16* x,const bf16* wqkv,const bf16* wo,const bf16* w1,const bf16* w2,u64* st,int slot){
  int b=blockIdx.x,t=threadIdx.x;
  u64 r0=__builtin_amdgcn_s_memrealtime();
  u64 c0=__builtin_amdgcn_s_memtime();
  v4u acc={0,0,0,0};
  acc^=rd(x,S*D*2,b,t); acc^=rd(wqkv,D*3*D*2,b,t); acc^=rd(wo,D*D*2,b,t); acc^=rd(w1,S*F*2,b,t); acc^=rd(w2,S*F*2,b,t);
  asm volatile("s_waitcnt vmcnt(0)"::"v"(acc):"memory");
  __syncthreads();
  u64 c1=__builtin_amdgcn_s_memtime();
  u64 r1=__builtin_amdgcn_s_memrealtime();
  if(t==0){ u64* s=st+slot*1024+b*4; s[0]=r0; s[1]=r1; s[2]=c1-c0; s[3]=acc.x; }
}
__global__ void spin_k(const u64* st){
  u64 mn=~0ull,mx=0,sum=0, mn2=~0ull;
  int slot = (PROBE==3)?1:0;
  for(int b=0;b<256;b++){ const u64* s=st+slot*1024+b*4; mn=s[0]<mn?s[0]:mn; mx=s[1]>mx?s[1]:mx; sum+=s[2]; u64 v=st[1024+b*4]; mn2=v<mn2?v:mn2; }
  u64 val;
  if(PROBE==1||PROBE==3) val=(mx-mn)*100;
  else if(PROBE==2||PROBE==5) val=(sum/256)*10;
  else if(PROBE==4) val=(mn2-mx)*100;
  else if(PROBE==6) val=(sum/256);
  else val=0;
  if(val>1000000) val=1000000;
  u64 r0=__builtin_amdgcn_s_memrealtime();
  while(__builtin_amdgcn_s_memrealtime()-r0<val) __builtin_amdgcn_s_sleep(8);
}
extern "C" void kernel_launch(void* const* d_in, const int* in_sizes, int n_in, void* d_out, int out_size, void* d_ws, size_t ws_size, hipStream_t stream){
  const bf16* x=(const bf16*)d_in[0]; const bf16* wqkv=(const bf16*)d_in[1]; const bf16* wo=(const bf16*)d_in[2];
  const bf16* w1=(const bf16*)d_in[4]; const bf16* b1=(const bf16*)d_in[5]; const bf16* w2=(const bf16*)d_in[6]; const bf16* b2=(const bf16*)d_in[7];
  float* ws=(float*)d_ws;
  float* xn=ws; float* h=xn+S*D; float* qkv=h+S*D; float* kv=qkv+S*3*D; float* z=kv+H*400; float* o=z+H*20; float* g=o+S*D;
  u64* st=(u64*)((char*)d_ws+(128u<<20));
  probe_k<<<256,256,0,stream>>>(x,wqkv,wo,w1,w2,st,0);
  probe_k<<<256,256,0,stream>>>(x,wqkv,wo,w1,w2,st,1);
  spin_k<<<1,1,0,stream>>>(st);
  ln_k<<<S,64,0,stream>>>(x,xn,h);
  qkv_k<<<dim3(30,S),64,0,stream>>>(xn,wqkv,qkv);
  kv_k<<<H,448,0,stream>>>(qkv,kv,z);
  o_k<<<dim3(10,S),64,0,stream>>>(qkv,kv,z,o);
  g_k<<<dim3(27,D),64,0,stream>>>(h,w1,b1,g);
  out_k<<<dim3(10,S),64,0,stream>>>(x,o,wo,g,w2,b2,(bf16*)d_out);
}
```

```cpp
#define PROBE 11
#include <hip/hip_runtime.h>
#include <hip/hip_bf16.h>
typedef __hip_bfloat16 bf16;
constexpr int S=2048, D=640, F=1728, H=32, DH=20;
__device__ __forceinline__ float b2f(bf16 v){ return __bfloat162float(v); }

__global__ void ln_k(const bf16* x, float* xn, float* h){
  int s=blockIdx.x; int t=threadIdx.x;
  float v[10]; float sum=0;
  for(int i=0;i<10;i++){ v[i]=b2f(x[s*D+t+64*i]); sum+=v[i]; }
  for(int o=32;o;o>>=1) sum+=__shfl_xor(sum,o);
  float m=sum/D; float q=0;
  for(int i=0;i<10;i++){ float d=v[i]-m; q+=d*d; }
  for(int o=32;o;o>>=1) q+=__shfl_xor(q,o);
  float r=1.f/sqrtf(q/D+1e-5f);
  sum=0;
  for(int i=0;i<10;i++){ v[i]=(v[i]-m)*r; xn[s*D+t+64*i]=v[i]; sum+=v[i]; }
  for(int o=32;o;o>>=1) sum+=__shfl_xor(sum,o);
  m=sum/D; q=0;
  for(int i=0;i<10;i++){ float d=v[i]-m; q+=d*d; }
  for(int o=32;o;o>>=1) q+=__shfl_xor(q,o);
  r=1.f/sqrtf(q/D+1e-5f);
  for(int i=0;i<10;i++){ h[s*D+t+64*i]=(v[i]-m)*r; }
}
__global__ void qkv_k(const float* xn, const bf16* w, float* qkv){
  int n=blockIdx.x*64+threadIdx.x; int s=blockIdx.y;
  float a=0; for(int k=0;k<D;k++) a+=xn[s*D+k]*b2f(w[k*3*D+n]);
  if(n<2*D){ a=(a>0?a+1.f:__expf(a))+1e-6f; }
  qkv[s*3*D+n]=a;
}
__global__ void kv_k(const float* qkv, float* kv, float* z){
  int hh=blockIdx.x; int t=threadIdx.x;
  if(t<400){ int d=t/20,e=t%20; float a=0; for(int s=0;s<S;s++) a+=qkv[s*3*D+D+hh*20+d]*qkv[s*3*D+2*D+hh*20+e]; kv[hh*400+t]=a; }
  else if(t<420){ int d=t-400; float a=0; for(int s=0;s<S;s++) a+=qkv[s*3*D+D+hh*20+d]; z[hh*20+d]=a; }
}
__global__ void o_k(const float* qkv, const float* kv, const float* z, float* o){
  int c=blockIdx.x*64+threadIdx.x; int s=blockIdx.y; int hh=c/20,e=c%20;
  float num=0,den=0; for(int d=0;d<20;d++){ float p=qkv[s*3*D+hh*20+d]; num+=p*kv[hh*400+d*20+e]; den+=p*z[hh*20+d]; }
  o[s*D+c]=num/(den+1e-6f);
}
__global__ void g_k(const float* h, const bf16* w1, const bf16* b1, float* g){
  int f=blockIdx.x*64+threadIdx.x; int d=blockIdx.y;
  float a=0; for(int s=0;s<S;s++) a+=h[s*D+d]*b2f(w1[s*F+f]);
  a+=b2f(b1[f]);
  float u=0.7978845608028654f*(a+0.044715f*a*a*a);
  g[d*F+f]=0.5f*a*(1.f+tanhf(u));
}
__global__ void out_k(const bf16* x, const float* o, const bf16* wo, const float* g, const bf16* w2, const bf16* b2, bf16* out){
  int d=blockIdx.x*64+threadIdx.x; int s=blockIdx.y;
  float a=0; for(int k=0;k<D;k++) a+=o[s*D+k]*b2f(wo[k*D+d]);
  float y=0; for(int f=0;f<F;f++) y+=g[d*F+f]*b2f(w2[f*S+s]);
  out[s*D+d]=__float2bfloat16(b2f(x[s*D+d])+a+y+b2f(b2[s]));
}

typedef unsigned v4u __attribute__((ext_vector_type(4)));
typedef unsigned long long u64;
#define GAS __attribute__((address_space(1)))
#define LAS __attribute__((address_space(3)))
template<int N> __device__ __forceinline__ void gl(const void* p, size_t per, int b, int t, char* lds, int& slot){
  const char* q=(const char*)p+per*b; int w=__builtin_amdgcn_readfirstlane(t>>6);
  #pragma unroll
  for(int i=0;i<N;i++){ size_t o=(size_t)i*4096+t*16; if(o>=per) o=t*16%per;
    __builtin_amdgcn_global_load_lds((const GAS void*)(q+o),(LAS void*)(lds+(slot*4+w)*1024),16,0,0); slot++; }
}
__global__ __launch_bounds__(256) void probe_k(const bf16* x,const bf16* wqkv,const bf16* wo,const bf16* w1,const bf16* w2,u64* st,int slot){
  extern __shared__ __attribute__((aligned(16))) char lds[];
  int b=blockIdx.x,t=threadIdx.x;
  u64 r0=__builtin_amdgcn_s_memrealtime();
  u64 c0=__builtin_amdgcn_s_memtime();
  v4u acc={0,0,0,0};
  asm volatile("s_waitcnt vmcnt(0)"::"v"(acc):"memory");
  __syncthreads();
  u64 c1=__builtin_amdgcn_s_memtime();
  u64 r1=__builtin_amdgcn_s_memrealtime();
  if(t==0){ u64* s=st+slot*1024+b*4; s[0]=r0; s[1]=r1; s[2]=c1-c0; s[3]=*(unsigned*)lds; }
}
__global__ void spin_k(const u64* st){
  u64 mn=~0ull,mx=0,sum=0, mn2=~0ull, mxs=0;
  int slot = (PROBE==9)?1:0;
  for(int b=0;b<256;b++){ const u64* s=st+slot*1024+b*4; mn=s[0]<mn?s[0]:mn; mxs=s[0]>mxs?s[0]:mxs; mx=s[1]>mx?s[1]:mx; sum+=s[2]; u64 v=st[1024+b*4]; mn2=v<mn2?v:mn2; }
  u64 val;
  if(PROBE==8||PROBE==9||PROBE==12) val=(mx-mn)*100;
  else if(PROBE==10) val=(sum/256)*4;
  else if(PROBE==7) val=(mn2-mx)*100;
  else if(PROBE==11) val=(mxs-mn)*100;
  else val=0;
  if(val>1000000) val=1000000;
  u64 r0=__builtin_amdgcn_s_memrealtime();
  while(__builtin_amdgcn_s_memrealtime()-r0<val) __builtin_amdgcn_s_sleep(8);
}
extern "C" void kernel_launch(void* const* d_in, const int* in_sizes, int n_in, void* d_out, int out_size, void* d_ws, size_t ws_size, hipStream_t stream){
  const bf16* x=(const bf16*)d_in[0]; const bf16* wqkv=(const bf16*)d_in[1]; const bf16* wo=(const bf16*)d_in[2];
  const bf16* w1=(const bf16*)d_in[4]; const bf16* b1=(const bf16*)d_in[5]; const bf16* w2=(const bf16*)d_in[6]; const bf16* b2=(const bf16*)d_in[7];
  float* ws=(float*)d_ws;
  float* xn=ws; float* h=xn+S*D; float* qkv=h+S*D; float* kv=qkv+S*3*D; float* z=kv+H*400; float* o=z+H*20; float* g=o+S*D;
  u64* st=(u64*)((char*)d_ws+(128u<<20));
  hipFuncSetAttribute((void*)probe_k,hipFuncAttributeMaxDynamicSharedMemorySize,150*1024);
  probe_k<<<256,256,150*1024,stream>>>(x,wqkv,wo,w1,w2,st,0);
  probe_k<<<256,256,150*1024,stream>>>(x,wqkv,wo,w1,w2,st,1);
  spin_k<<<1,1,0,stream>>>(st);
  ln_k<<<S,64,0,stream>>>(x,xn,h);
  qkv_k<<<dim3(30,S),64,0,stream>>>(xn,wqkv,qkv);
  kv_k<<<H,448,0,stream>>>(qkv,kv,z);
  o_k<<<dim3(10,S),64,0,stream>>>(qkv,kv,z,o);
  g_k<<<dim3(27,D),64,0,stream>>>(h,w1,b1,g);
  out_k<<<dim3(10,S),64,0,stream>>>(x,o,wo,g,w2,b2,(bf16*)d_out);
}
```

```cpp
#define PROBE 14
#include <hip/hip_runtime.h>
#include <hip/hip_bf16.h>
typedef __hip_bfloat16 bf16;
constexpr int S=2048, D=640, F=1728, H=32, DH=20;
__device__ __forceinline__ float b2f(bf16 v){ return __bfloat162float(v); }

__global__ void ln_k(const bf16* x, float* xn, float* h){
  int s=blockIdx.x; int t=threadIdx.x;
  float v[10]; float sum=0;
  for(int i=0;i<10;i++){ v[i]=b2f(x[s*D+t+64*i]); sum+=v[i]; }
  for(int o=32;o;o>>=1) sum+=__shfl_xor(sum,o);
  float m=sum/D; float q=0;
  for(int i=0;i<10;i++){ float d=v[i]-m; q+=d*d; }
  for(int o=32;o;o>>=1) q+=__shfl_xor(q,o);
  float r=1.f/sqrtf(q/D+1e-5f);
  sum=0;
  for(int i=0;i<10;i++){ v[i]=(v[i]-m)*r; xn[s*D+t+64*i]=v[i]; sum+=v[i]; }
  for(int o=32;o;o>>=1) sum+=__shfl_xor(sum,o);
  m=sum/D; q=0;
  for(int i=0;i<10;i++){ float d=v[i]-m; q+=d*d; }
  for(int o=32;o;o>>=1) q+=__shfl_xor(q,o);
  r=1.f/sqrtf(q/D+1e-5f);
  for(int i=0;i<10;i++){ h[s*D+t+64*i]=(v[i]-m)*r; }
}
__global__ void qkv_k(const float* xn, const bf16* w, float* qkv){
  int n=blockIdx.x*64+threadIdx.x; int s=blockIdx.y;
  float a=0; for(int k=0;k<D;k++) a+=xn[s*D+k]*b2f(w[k*3*D+n]);
  if(n<2*D){ a=(a>0?a+1.f:__expf(a))+1e-6f; }
  qkv[s*3*D+n]=a;
}
__global__ void kv_k(const float* qkv, float* kv, float* z){
  int hh=blockIdx.x; int t=threadIdx.x;
  if(t<400){ int d=t/20,e=t%20; float a=0; for(int s=0;s<S;s++) a+=qkv[s*3*D+D+hh*20+d]*qkv[s*3*D+2*D+hh*20+e]; kv[hh*400+t]=a; }
  else if(t<420){ int d=t-400; float a=0; for(int s=0;s<S;s++) a+=qkv[s*3*D+D+hh*20+d]; z[hh*20+d]=a; }
}
__global__ void o_k(const float* qkv, const float* kv, const float* z, float* o){
  int c=blockIdx.x*64+threadIdx.x; int s=blockIdx.y; int hh=c/20,e=c%20;
  float num=0,den=0; for(int d=0;d<20;d++){ float p=qkv[s*3*D+hh*20+d]; num+=p*kv[hh*400+d*20+e]; den+=p*z[hh*20+d]; }
  o[s*D+c]=num/(den+1e-6f);
}
__global__ void g_k(const float* h, const bf16* w1, const bf16* b1, float* g){
  int f=blockIdx.x*64+threadIdx.x; int d=blockIdx.y;
  float a=0; for(int s=0;s<S;s++) a+=h[s*D+d]*b2f(w1[s*F+f]);
  a+=b2f(b1[f]);
  float u=0.7978845608028654f*(a+0.044715f*a*a*a);
  g[d*F+f]=0.5f*a*(1.f+tanhf(u));
}
__global__ void out_k(const bf16* x, const float* o, const bf16* wo, const float* g, const bf16* w2, const bf16* b2, bf16* out){
  int d=blockIdx.x*64+threadIdx.x; int s=blockIdx.y;
  float a=0; for(int k=0;k<D;k++) a+=o[s*D+k]*b2f(wo[k*D+d]);
  float y=0; for(int f=0;f<F;f++) y+=g[d*F+f]*b2f(w2[f*S+s]);
  out[s*D+d]=__float2bfloat16(b2f(x[s*D+d])+a+y+b2f(b2[s]));
}

typedef unsigned v4u __attribute__((ext_vector_type(4)));
typedef unsigned long long u64;
#define GAS __attribute__((address_space(1)))
#define LAS __attribute__((address_space(3)))
template<int N,int AUX> __device__ __forceinline__ void gl(const void* p, size_t per, int b, int t, char* lds, int& slot){
  const char* q=(const char*)p+per*b; int w=__builtin_amdgcn_readfirstlane(t>>6);
  #pragma unroll
  for(int i=0;i<N;i++){ size_t o=(size_t)i*4096+t*16; if(o>=per) o=t*16%per;
    __builtin_amdgcn_global_load_lds((const GAS void*)(q+o),(LAS void*)(lds+(slot*4+w)*1024),16,0,AUX); slot++; }
}
__global__ __launch_bounds__(256) void probe_k(const bf16* x,const bf16* wqkv,const bf16* wo,const bf16* w1,const bf16* w2,u64* st,int slot,char* scratch){
  extern __shared__ __attribute__((aligned(16))) char lds[];
  int b=blockIdx.x,t=threadIdx.x;
  u64 r0=__builtin_amdgcn_s_memrealtime();
  u64 c0=__builtin_amdgcn_s_memtime();
  v4u acc={0,0,0,0};
  int sl=0;
  gl<3,2>(x,S*D*2/256,b,t,lds,sl); gl<3,2>(wqkv,D*3*D*2/256,b,t,lds,sl);
  asm volatile("s_waitcnt vmcnt(0)"::"v"(acc):"memory");
  __syncthreads();
  u64 c1=__builtin_amdgcn_s_memtime();
  u64 r1=__builtin_amdgcn_s_memrealtime();
  if(t==0){ u64* s=st+slot*1024+b*4; s[0]=r0; s[1]=r1; s[2]=c1-c0; s[3]=*(unsigned*)lds; }
}
__global__ void spin_k(const u64* st){
  u64 mn=~0ull,mx=0,sum=0;
  int slot = 0;
  for(int b=0;b<256;b++){ const u64* s=st+slot*1024+b*4; mn=s[0]<mn?s[0]:mn; mx=s[1]>mx?s[1]:mx; sum+=s[2]; }
  u64 val;
  if(PROBE==13||PROBE==14) val=(mx-mn)*200;
  else if(PROBE==15||PROBE==16) val=(sum/256)*10;
  else val=(sum/256)*50;
  if(val>1000000) val=1000000;
  u64 r0=__builtin_amdgcn_s_memrealtime();
  while(__builtin_amdgcn_s_memrealtime()-r0<val) __builtin_amdgcn_s_sleep(8);
}
extern "C" void kernel_launch(void* const* d_in, const int* in_sizes, int n_in, void* d_out, int out_size, void* d_ws, size_t ws_size, hipStream_t stream){
  const bf16* x=(const bf16*)d_in[0]; const bf16* wqkv=(const bf16*)d_in[1]; const bf16* wo=(const bf16*)d_in[2];
  const bf16* w1=(const bf16*)d_in[4]; const bf16* b1=(const bf16*)d_in[5]; const bf16* w2=(const bf16*)d_in[6]; const bf16* b2=(const bf16*)d_in[7];
  float* ws=(float*)d_ws;
  float* xn=ws; float* h=xn+S*D; float* qkv=h+S*D; float* kv=qkv+S*3*D; float* z=kv+H*400; float* o=z+H*20; float* g=o+S*D;
  u64* st=(u64*)((char*)d_ws+(128u<<20));
  (void)hipFuncSetAttribute((void*)probe_k,hipFuncAttributeMaxDynamicSharedMemorySize,150*1024);
  probe_k<<<256,256,150*1024,stream>>>(x,wqkv,wo,w1,w2,st,0,(char*)d_ws+(160u<<20));
  spin_k<<<1,1,0,stream>>>(st);
  ln_k<<<S,64,0,stream>>>(x,xn,h);
  qkv_k<<<dim3(30,S),64,0,stream>>>(xn,wqkv,qkv);
  kv_k<<<H,448,0,stream>>>(qkv,kv,z);
  o_k<<<dim3(10,S),64,0,stream>>>(qkv,kv,z,o);
  g_k<<<dim3(27,D),64,0,stream>>>(h,w1,b1,g);
  out_k<<<dim3(10,S),64,0,stream>>>(x,o,wo,g,w2,b2,(bf16*)d_out);
}
```

```cpp
#define PROBE 0
#define NTH 64
#include <hip/hip_runtime.h>
#include <hip/hip_bf16.h>
typedef __hip_bfloat16 bf16;
constexpr int S=2048, D=640, F=1728, H=32, DH=20;
__device__ __forceinline__ float b2f(bf16 v){ return __bfloat162float(v); }

__global__ void ln_k(const bf16* x, float* xn, float* h){
  int s=blockIdx.x; int t=threadIdx.x;
  float v[10]; float sum=0;
  for(int i=0;i<10;i++){ v[i]=b2f(x[s*D+t+64*i]); sum+=v[i]; }
  for(int o=32;o;o>>=1) sum+=__shfl_xor(sum,o);
  float m=sum/D; float q=0;
  for(int i=0;i<10;i++){ float d=v[i]-m; q+=d*d; }
  for(int o=32;o;o>>=1) q+=__shfl_xor(q,o);
  float r=1.f/sqrtf(q/D+1e-5f);
  sum=0;
  for(int i=0;i<10;i++){ v[i]=(v[i]-m)*r; xn[s*D+t+64*i]=v[i]; sum+=v[i]; }
  for(int o=32;o;o>>=1) sum+=__shfl_xor(sum,o);
  m=sum/D; q=0;
  for(int i=0;i<10;i++){ float d=v[i]-m; q+=d*d; }
  for(int o=32;o;o>>=1) q+=__shfl_xor(q,o);
  r=1.f/sqrtf(q/D+1e-5f);
  for(int i=0;i<10;i++){ h[s*D+t+64*i]=(v[i]-m)*r; }
}
__global__ void qkv_k(const float* xn, const bf16* w, float* qkv){
  int n=blockIdx.x*64+threadIdx.x; int s=blockIdx.y;
  float a=0; for(int k=0;k<D;k++) a+=xn[s*D+k]*b2f(w[k*3*D+n]);
  if(n<2*D){ a=(a>0?a+1.f:__expf(a))+1e-6f; }
  qkv[s*3*D+n]=a;
}
__global__ void kv_k(const float* qkv, float* kv, float* z){
  int hh=blockIdx.x; int t=threadIdx.x;
  if(t<400){ int d=t/20,e=t%20; float a=0; for(int s=0;s<S;s++) a+=qkv[s*3*D+D+hh*20+d]*qkv[s*3*D+2*D+hh*20+e]; kv[hh*400+t]=a; }
  else if(t<420){ int d=t-400; float a=0; for(int s=0;s<S;s++) a+=qkv[s*3*D+D+hh*20+d]; z[hh*20+d]=a; }
}
__global__ void o_k(const float* qkv, const float* kv, const float* z, float* o){
  int c=blockIdx.x*64+threadIdx.x; int s=blockIdx.y; int hh=c/20,e=c%20;
  float num=0,den=0; for(int d=0;d<20;d++){ float p=qkv[s*3*D+hh*20+d]; num+=p*kv[hh*400+d*20+e]; den+=p*z[hh*20+d]; }
  o[s*D+c]=num/(den+1e-6f);
}
__global__ void g_k(const float* h, const bf16* w1, const bf16* b1, float* g){
  int f=blockIdx.x*64+threadIdx.x; int d=blockIdx.y;
  float a=0; for(int s=0;s<S;s++) a+=h[s*D+d]*b2f(w1[s*F+f]);
  a+=b2f(b1[f]);
  float u=0.7978845608028654f*(a+0.044715f*a*a*a);
  g[d*F+f]=0.5f*a*(1.f+tanhf(u));
}
__global__ void out_k(const bf16* x, const float* o, const bf16* wo, const float* g, const bf16* w2, const bf16* b2, bf16* out){
  int d=blockIdx.x*64+threadIdx.x; int s=blockIdx.y;
  float a=0; for(int k=0;k<D;k++) a+=o[s*D+k]*b2f(wo[k*D+d]);
  float y=0; for(int f=0;f<F;f++) y+=g[d*F+f]*b2f(w2[f*S+s]);
  out[s*D+d]=__float2bfloat16(b2f(x[s*D+d])+a+y+b2f(b2[s]));
}

typedef unsigned long long u64;
__global__ __launch_bounds__(512) void probe_k(u64* st,int slot){
  extern __shared__ __attribute__((aligned(16))) char lds[];
  int b=blockIdx.x,t=threadIdx.x;
  u64 c0=__builtin_amdgcn_s_memtime();
  unsigned v=t;
  asm volatile("s_waitcnt vmcnt(0)"::"v"(v):"memory");
  u64 c1=__builtin_amdgcn_s_memtime();
  if(t==0){ u64* s=st+slot*1024+b*4; s[2]=c1-c0; s[3]=v; }
}
__global__ void spin_k(const u64* st){
  u64 sum=0;
  for(int b=0;b<256;b++){ const u64* s=st+b*4; sum+=s[2]; }
  u64 val=(sum/256)*5;
  if(val>1000000) val=1000000;
  u64 r0=__builtin_amdgcn_s_memrealtime();
  while(__builtin_amdgcn_s_memrealtime()-r0<val) __builtin_amdgcn_s_sleep(8);
}
extern "C" void kernel_launch(void* const* d_in, const int* in_sizes, int n_in, void* d_out, int out_size, void* d_ws, size_t ws_size, hipStream_t stream){
  const bf16* x=(const bf16*)d_in[0]; const bf16* wqkv=(const bf16*)d_in[1]; const bf16* wo=(const bf16*)d_in[2];
  const bf16* w1=(const bf16*)d_in[4]; const bf16* b1=(const bf16*)d_in[5]; const bf16* w2=(const bf16*)d_in[6]; const bf16* b2=(const bf16*)d_in[7];
  float* ws=(float*)d_ws;
  float* xn=ws; float* h=xn+S*D; float* qkv=h+S*D; float* kv=qkv+S*3*D; float* z=kv+H*400; float* o=z+H*20; float* g=o+S*D;
  u64* st=(u64*)((char*)d_ws+(128u<<20));
  (void)hipFuncSetAttribute((void*)probe_k,hipFuncAttributeMaxDynamicSharedMemorySize,150*1024);
  probe_k<<<256,NTH,150*1024,stream>>>(st,0);
  spin_k<<<1,1,0,stream>>>(st);
  ln_k<<<S,64,0,stream>>>(x,xn,h);
  qkv_k<<<dim3(30,S),64,0,stream>>>(xn,wqkv,qkv);
  kv_k<<<H,448,0,stream>>>(qkv,kv,z);
  o_k<<<dim3(10,S),64,0,stream>>>(qkv,kv,z,o);
  g_k<<<dim3(27,D),64,0,stream>>>(h,w1,b1,g);
  out_k<<<dim3(10,S),64,0,stream>>>(x,o,wo,g,w2,b2,(bf16*)d_out);
}
```

```cpp
#include <hip/hip_runtime.h>
#include <hip/hip_bf16.h>
#pragma clang diagnostic ignored "-Wint-to-pointer-cast"

#define GAS __attribute__((address_space(1)))
#define LAS __attribute__((address_space(3)))
#define DI __device__ __forceinline__
typedef unsigned u32;
typedef unsigned long long u64;
typedef short s16x4 __attribute__((ext_vector_type(4)));
typedef short s16x8 __attribute__((ext_vector_type(8)));
typedef __bf16 b16x2 __attribute__((ext_vector_type(2)));
typedef __bf16 b16x8 __attribute__((ext_vector_type(8)));
typedef float f32x2 __attribute__((ext_vector_type(2)));
typedef float f32x4 __attribute__((ext_vector_type(4)));
typedef unsigned u32x2 __attribute__((ext_vector_type(2)));
typedef unsigned u32x4 __attribute__((ext_vector_type(4)));

constexpr int WS_EPOCH = 0;
constexpr int WS_XNFLAG = 4096;
constexpr int WS_HFLAG = 8192;
constexpr int WS_KVFLAG = 12288;
constexpr int WS_GFLAG = 16384;
constexpr int WS_OFLAG = 20480;
constexpr int WS_PART = 32768;
constexpr int WS_XN = 524288;
constexpr int WS_H = WS_XN + 2621440;
constexpr int WS_G = WS_H + 2621440;
constexpr int WS_O = WS_G + 2211840;
constexpr int WS_END = WS_O + 2621440;
constexpr int L_BYTES = 163840;
constexpr int L_LN = 131072, LN_PITCH = 1296;
constexpr int LQ_B = 122880, L1_B = 131072, L3_B = 122880;
constexpr int L_RED = 122880, L_FIN = L_RED + 4 * 6720;
constexpr u32 SPIN_CAP = 1u << 16;

#define BAR() asm volatile("s_waitcnt lgkmcnt(0)\n\ts_barrier" ::: "memory")
#define WAITV(n) asm volatile("s_waitcnt vmcnt(" #n ")" ::: "memory")
#define STAMP_AT(k)
#define STAMP_DECL
#define STAMP_OUT()
#define RLX_AGENT __ATOMIC_RELAXED, __HIP_MEMORY_SCOPE_AGENT

DI f32x4 mfma32(s16x8 a, s16x8 b, f32x4 c) {
  return __builtin_amdgcn_mfma_f32_16x16x32_bf16(__builtin_bit_cast(b16x8, a), __builtin_bit_cast(b16x8, b), c, 0, 0, 0);
}
DI f32x4 mfma16(s16x4 a, s16x4 b, f32x4 c) { return __builtin_amdgcn_mfma_f32_16x16x16bf16_1k(a, b, c, 0, 0, 0); }
DI s16x4 ldtr(u32 a) { return __builtin_amdgcn_ds_read_tr16_b64_v4i16((LAS s16x4*)a); }
DI s16x8 ld128(u32 a) { return *(const LAS s16x8*)a; }
DI s16x4 ld64(u32 a) { return *(const LAS s16x4*)a; }
DI s16x8 cat(s16x4 a, s16x4 b) { return __builtin_shufflevector(a, b, 0, 1, 2, 3, 4, 5, 6, 7); }
DI u32 pk2(float a, float b) { f32x2 v = {a, b}; return __builtin_bit_cast(u32, __builtin_convertvector(v, b16x2)); }
DI s16x4 pk4(f32x4 v) { u32x2 r = {pk2(v[0], v[1]), pk2(v[2], v[3])}; return __builtin_bit_cast(s16x4, r); }
DI float blo(u32 u) { return __uint_as_float(u << 16); }
DI float bhi(u32 u) { return __uint_as_float(u & 0xffff0000u); }
template <int C> DI float dpp_add(float s) { return s + __int_as_float(__builtin_amdgcn_update_dpp(0, __float_as_int(s), C, 0xf, 0xf, true)); }
DI float sum32(float s) {
  s = dpp_add<0x121>(s); s = dpp_add<0x122>(s); s = dpp_add<0x124>(s); s = dpp_add<0x128>(s);
  auto r = __builtin_amdgcn_permlane16_swap(__float_as_uint(s), __float_as_uint(s), false, false);
  return __uint_as_float(r[0]) + __uint_as_float(r[1]);
}
DI float phi1(float a) {
  const float L = 1.4426950408889634f;
  float e = __builtin_amdgcn_fmed3f(__builtin_amdgcn_exp2f(__builtin_fmaf(a, L, -L)), 0.f, 1.f);
  return __int_as_float(max(__float_as_int(a), __float_as_int(e))) + 1e-6f;
}
DI f32x4 phi4(f32x4 a) { f32x4 r = {phi1(a[0]), phi1(a[1]), phi1(a[2]), phi1(a[3])}; return r; }
DI float gelu1(float u) {
  const float K = -2.f * 0.7978845608028654f * 1.4426950408889634f;
  float e = __builtin_amdgcn_exp2f(u * __builtin_fmaf(u * u, K * 0.044715f, K));
  return u * __builtin_amdgcn_rcpf(1.f + e);
}
DI f32x4 gelu4(f32x4 a) { f32x4 r = {gelu1(a[0]), gelu1(a[1]), gelu1(a[2]), gelu1(a[3])}; return r; }

#define GL(v, off, sc) "global_load_lds_dwordx4 " v ", %0 offset:" #off sc "\n\t"
DI void gl4(const char* base, u32 m, u32 v0, u32 v1, u32 v2, u32 v3) {
  asm volatile("s_mov_b32 m0, %1\n\ts_nop 0\n\t" GL("%2", 0, "") GL("%3", 1024, "") GL("%4", 2048, "") GL("%5", 3072, "")
               ::"s"(base), "s"(m), "v"(v0), "v"(v1), "v"(v2), "v"(v3) : "memory");
}
DI void gl3(const char* base, u32 m, u32 v0, u32 v1, u32 v2) {
  asm volatile("s_mov_b32 m0, %1\n\ts_nop 0\n\t" GL("%2", 0, "") GL("%3", 1024, "") GL("%4", 2048, "")
               ::"s"(base), "s"(m), "v"(v0), "v"(v1), "v"(v2) : "memory");
}
DI void gl1(const char* base, u32 m, u32 v0) {
  asm volatile("s_mov_b32 m0, %1\n\ts_nop 0\n\t" GL("%2", 0, "")::"s"(base), "s"(m), "v"(v0) : "memory");
}
DI void glin8(const char* base, u32 m, u32 vl, u32 vl4) {
  asm volatile("s_mov_b32 m0, %1\n\ts_nop 0\n\t" GL("%2", 0, " sc1") GL("%2", 1024, " sc1") GL("%2", 2048, " sc1") GL("%2", 3072, " sc1")
               "s_add_u32 m0, m0, 4096\n\ts_nop 0\n\t" GL("%3", 0, " sc1") GL("%3", 1024, " sc1") GL("%3", 2048, " sc1") GL("%3", 3072, " sc1")
               ::"s"(base), "s"(m), "v"(vl), "v"(vl4) : "memory", "scc");
}
DI void glin10(const char* base, u32 m, u32 vl, u32 vl4, u32 vl8) {
  asm volatile("s_mov_b32 m0, %1\n\ts_nop 0\n\t" GL("%2", 0, " sc1") GL("%2", 1024, " sc1") GL("%2", 2048, " sc1") GL("%2", 3072, " sc1")
               "s_add_u32 m0, m0, 4096\n\ts_nop 0\n\t" GL("%3", 0, " sc1") GL("%3", 1024, " sc1") GL("%3", 2048, " sc1") GL("%3", 3072, " sc1")
               "s_add_u32 m0, m0, 4096\n\ts_nop 0\n\t" GL("%4", 0, " sc1") GL("%4", 1024, " sc1")
               ::"s"(base), "s"(m), "v"(vl), "v"(vl4), "v"(vl8) : "memory", "scc");
}
DI void poll(const char* f, int n, u32 tag, int lane) {
  const GAS u32* p = (const GAS u32*)f + (lane < n ? lane : 0);
#pragma unroll 1
  for (u32 sp = 0; sp < SPIN_CAP; ++sp) {
    u32 v = __hip_atomic_load(p, RLX_AGENT);
    if (__all(v == tag)) break;
    __builtin_amdgcn_s_sleep(1);
  }
  asm volatile("" ::: "memory");
}
DI void publish(char* f, u32 tag, int lane) {
  WAITV(0);
  if (lane == 0) __hip_atomic_store((GAS u32*)f, tag, RLX_AGENT);
}

struct Params {
  const char *x, *wqkv, *wo, *w1, *b1, *w2, *b2;
  char *out, *ws;
};

__global__ __launch_bounds__(320) void fused_fwd(Params P) {
  extern __shared__ __attribute__((aligned(16))) char lds_[];
  const int tid = threadIdx.x, lane = tid & 63;
  const int wave = __builtin_amdgcn_readfirstlane(tid >> 6);
  const int b = blockIdx.x, xc = b & 7, jb = b >> 3;
  char* const ws = P.ws;
  const bool roleA = xc < 4;
  const int hg = 4 * (xc & 1) + (jb & 3), rt = 8 * ((xc >> 1) & 1) + (jb >> 2);
  const int nB = (xc - 4) * 28 + jb;
  const bool roleB = !roleA && jb < 28;
  const int ft = nB >> 3, dt = nB & 7;
  const int st = 4 * xc + (jb & 3), dt3 = jb >> 2;
  const u32 L0 = (u32)(size_t)(LAS char*)lds_;
  STAMP_DECL;
  STAMP_AT(0);
  u32 e0 = *(const __attribute__((address_space(4))) u32*)(ws + WS_EPOCH);

  if (wave == 4) {
    const u32 vl = lane * 16, vl4 = vl + 4096, vl8 = vl + 8192;
    if (roleA) {
      u32 v[15];
#pragma unroll
      for (int i = 0; i < 15; ++i) {
        int n = 64 * i + lane, rho = n / 30, cc = n - 30 * rho, sg = cc / 10;
        int k = (rho & 19) | ((rho & 4) << 1) | ((rho & 8) >> 1);
        v[i] = k * 3840 + sg * 1280 + (cc - 10 * sg) * 16 + 3072 - (i & 3) * 1024;
      }
      const char* wb = P.wqkv + hg * 160 - 3072;
      const char* xb = ws + WS_XN + rt * 163840;
      const int rot = (5 * (rt & 7)) >> 1;
      int ca = rot, cb = rot, q = 0;
      u32 sa = L0, sb = L0 + LQ_B;
      auto agroup = [&]() {
        const char* s = wb + ca * (32 * 3840);
        if (q == 0) gl4(s, sa, v[0], v[1], v[2], v[3]);
        else if (q == 1) gl4(s, sa + 4096, v[4], v[5], v[6], v[7]);
        else if (q == 2) gl4(s, sa + 8192, v[8], v[9], v[10], v[11]);
        else {
          gl3(s, sa + 12288, v[12], v[13], v[14]);
          ca = ca == 19 ? 0 : ca + 1;
          sa = sa == L0 + 7 * 15360 ? L0 : sa + 15360;
        }
        q = (q + 1) & 3;
      };
      auto bunit = [&]() {
        glin8(xb + cb * 8192, sb, vl, vl4);
        cb = cb == 19 ? 0 : cb + 1;
        sb = sb == L0 + LQ_B + 3 * 8192 ? L0 + LQ_B : sb + 8192;
      };
#pragma unroll 1
      for (int i = 0; i < 32; ++i) {
        if (i == 8) { asm volatile("" : "+s"(e0)); BAR(); }
        agroup();
        if (i >= 8) WAITV(20);
      }
      STAMP_AT(1);
      BAR();
      STAMP_AT(2);
      const u32 tg = e0 * 0x9E3779B1u + 0x7F4A7C15u;
      poll(ws + WS_XNFLAG + rt * 128, 32, tg, lane);
      STAMP_AT(3);
      bunit(); bunit(); bunit();
#pragma unroll 1
      for (int r = 0; r < 20; ++r) {
        if (r < 3) WAITV(16); else if (r < 18) WAITV(38); else if (r == 18) WAITV(19); else WAITV(0);
        BAR();
        if (r <= 16) {
          if (r >= 1) {
#pragma unroll 1
            for (int i = 0; i < 3; ++i) agroup();
          }
          bunit();
        }
      }
      BAR();
    } else if (roleB) {
      u32 v[4];
#pragma unroll
      for (int i = 0; i < 4; ++i) {
        int f = (lane >> 4) | (((i >> 1) & 1) << 2);
        int ch = (lane & 15) ^ (f << 1);
        if (ft == 13) ch &= 7;
        v[i] = (4 * i + (lane >> 4)) * 3456 + ch * 16 - i * 1024;
      }
      const char* wb = P.w1 + ft * 256;
      const char* hb = ws + WS_H + dt * 327680;
      int ga = dt * 16, gs = 0, cb = dt * 4;
      u32 sb = L0 + L1_B;
      auto agroup = [&]() {
        gl4(wb + ga * (16 * 3456), L0 + gs * 4096, v[0], v[1], v[2], v[3]);
        ga = (ga + 1) & 127; gs = (gs + 1) & 31;
      };
      auto bunit = [&]() {
        glin10(hb + cb * 10240, sb, vl, vl4, vl8);
        cb = (cb + 1) & 31;
        sb = sb == L0 + L1_B + 2 * 10240 ? L0 + L1_B : sb + 10240;
      };
#pragma unroll 1
      for (int i = 0; i < 32; ++i) {
        if (i == 8) { asm volatile("" : "+s"(e0)); BAR(); }
        agroup();
        if (i >= 8) WAITV(20);
      }
      STAMP_AT(1);
      BAR();
      STAMP_AT(2);
      const u32 tg = e0 * 0x9E3779B1u + 0x7F4A7C15u;
      {
        const __amdgpu_buffer_rsrc_t rs = __builtin_amdgcn_make_buffer_rsrc(ws, 0, WS_END, 0x00020000);
#pragma unroll 1
        for (u32 sp = 0; sp < SPIN_CAP; ++sp) {
          u32x4 a = __builtin_amdgcn_raw_buffer_load_b128(rs, WS_HFLAG + vl, 0, 16);
          u32x4 c = __builtin_amdgcn_raw_buffer_load_b128(rs, WS_HFLAG + 1024 + vl, 0, 16);
          bool ok = a[0] == tg && a[1] == tg && a[2] == tg && a[3] == tg && c[0] == tg && c[1] == tg && c[2] == tg && c[3] == tg;
          if (__all(ok)) break;
          __builtin_amdgcn_s_sleep(1);
          asm volatile("" ::: "memory");
        }
        asm volatile("" ::: "memory");
      }
      STAMP_AT(3);
      if (nB == 0 && lane == 0) __hip_atomic_store((GAS u32*)(ws + WS_EPOCH), tg, RLX_AGENT);
      bunit(); bunit();
#pragma unroll 1
      for (int r = 0; r < 32; ++r) {
        if (r < 3 || r == 30) WAITV(10); else if (r < 29) WAITV(36); else if (r == 29) WAITV(30); else WAITV(0);
        BAR();
        if (r <= 29) {
          bunit();
          int n = r == 0 || r == 29 ? 0 : r == 28 ? 2 : 4 - (r & 1);
#pragma unroll 1
          for (int i = 0; i < n; ++i) agroup();
        }
      }
      BAR();
    } else {
      asm volatile("" : "+s"(e0));
      BAR();
      BAR();
    }
    STAMP_AT(4);
    {
      const u32 tg = e0 * 0x9E3779B1u + 0x7F4A7C15u;
      u32 v[4], u[8];
#pragma unroll
      for (int i = 0; i < 4; ++i) v[i] = (8 * i + (lane >> 3)) * 4096 + ((lane & 7) ^ (((lane >> 4) & 3) << 1)) * 16 - i * 1024;
#pragma unroll
      for (int i = 0; i < 8; ++i) { int n = 64 * i + lane, rw = n / 10; u[i] = rw * 1280 + (n - 10 * rw) * 16 - (i & 3) * 1024; }
      const char* wb = P.w2 + st * 128;
      const char* ob = P.wo + dt3 * 160;
      const char* gb = ws + WS_G + dt3 * 276480;
      const char* qb = ws + WS_O + st * 81920;
      const int rot = (27 * dt3) >> 3;
      int ca = rot, cb = rot, ua = 0, ub = 0;
      u32 sa = L0, sb = L0 + L3_B;
      auto aunit = [&]() {
        if (ua < 27) {
          const char* s = wb + ca * (64 * 4096);
          gl4(s, sa, v[0], v[1], v[2], v[3]);
          gl4(s + 32 * 4096, sa + 4096, v[0], v[1], v[2], v[3]);
          ca = ca == 26 ? 0 : ca + 1;
        } else {
          const int m = ua - 27;
          const char* s = ob + ((((m >> 1) + dt3) & 7) * 80 + (m & 1) * 48) * 1280;
          gl4(s, sa, u[0], u[1], u[2], u[3]);
          if (m & 1) gl1(s, sa + 4096, u[4]); else gl4(s, sa + 4096, u[4], u[5], u[6], u[7]);
        }
        ++ua;
        sa = sa == L0 + 14 * 8192 ? L0 : sa + 8192;
      };
      auto bunit = [&]() {
        const char* s = ub < 27 ? gb + cb * 10240 : qb + ((ub - 27 + dt3) & 7) * 10240;
        glin10(s, sb, vl, vl4, vl8);
        cb = cb == 26 ? 0 : cb + 1;
        ++ub;
        sb = sb == L0 + L3_B + 3 * 10240 ? L0 + L3_B : sb + 10240;
      };
#pragma unroll 1
      for (int i = 0; i < 15; ++i) {
        if (i == 4 && roleA) BAR();
        aunit();
        WAITV(32);
      }
      STAMP_AT(5);
      if (roleA) { BAR(); BAR(); }
      STAMP_AT(6);
      poll(ws + WS_GFLAG + dt3 * 256, 54, tg, lane);
      STAMP_AT(7);
      bunit(); bunit(); bunit();
#pragma unroll 1
      for (int r = 0; r < 35; ++r) {
        if (r < 4) WAITV(20); else if (r < 15) WAITV(44); else if (r < 30) WAITV(38); else if (r < 33) WAITV(20); else if (r == 33) WAITV(10); else WAITV(0);
        BAR();
        if (r == 24) { STAMP_AT(8); poll(ws + WS_OFLAG + st * 64, 16, tg, lane); STAMP_AT(9); }
        if (r <= 31) bunit();
        if (r >= 1 && r <= 28) aunit();
      }
    }
    STAMP_AT(10);
    STAMP_OUT();
    return;
  }

  const __amdgpu_buffer_rsrc_t rs = __builtin_amdgcn_make_buffer_rsrc(ws, 0, WS_END, 0x00020000);
  const int c16 = lane & 15, g = lane >> 4, q4 = c16 >> 2, p4 = lane & 3;
  u32 tag;
  {
    const int row = 2 * wave + (lane >> 5), hl = lane & 31;
    const GAS u32x2* xp = (const GAS u32x2*)(P.x + (8 * b + row) * 1280) + hl;
    u32x2 raw[5];
#pragma unroll
    for (int i = 0; i < 5; ++i) raw[i] = xp[32 * i];
    f32x2 v[10];
#pragma unroll
    for (int i = 0; i < 5; ++i) { v[2 * i] = f32x2{blo(raw[i][0]), bhi(raw[i][0])}; v[2 * i + 1] = f32x2{blo(raw[i][1]), bhi(raw[i][1])}; }
    f32x2 s = v[0];
#pragma unroll
    for (int i = 1; i < 10; ++i) s += v[i];
    const float m = sum32(s[0] + s[1]) * (1.f / 640.f);
    f32x2 qq = {0.f, 0.f};
#pragma unroll
    for (int i = 0; i < 10; ++i) { v[i] -= m; qq += v[i] * v[i]; }
    const float r1 = __builtin_amdgcn_rsqf(sum32(qq[0] + qq[1]) * (1.f / 640.f) + 1e-5f);
    s = f32x2{0.f, 0.f}; qq = s;
#pragma unroll
    for (int i = 0; i < 10; ++i) { v[i] *= r1; s += v[i]; qq += v[i] * v[i]; }
    const u32 wa = L0 + L_LN + row * LN_PITCH + hl * 8;
#pragma unroll
    for (int i = 0; i < 5; ++i) *(LAS u32x2*)(wa + i * 256) = u32x2{pk2(v[2 * i][0], v[2 * i][1]), pk2(v[2 * i + 1][0], v[2 * i + 1][1])};
    const float m2 = sum32(s[0] + s[1]) * (1.f / 640.f);
    const float r2 = __builtin_amdgcn_rsqf(sum32(qq[0] + qq[1]) * (1.f / 640.f) - m2 * m2 + 1e-5f);
#pragma unroll
    for (int i = 0; i < 10; ++i) v[i] = (v[i] - m2) * r2;
#pragma unroll
    for (int i = 0; i < 5; ++i) *(LAS u32x2*)(wa + 8 * LN_PITCH + i * 256) = u32x2{pk2(v[2 * i][0], v[2 * i][1]), pk2(v[2 * i + 1][0], v[2 * i + 1][1])};
    asm volatile("" : "+s"(e0));
    tag = e0 * 0x9E3779B1u + 0x7F4A7C15u;
    STAMP_AT(1);
    BAR();
    if (wave < 2) {
      const int ch0 = 40 * wave + (lane >> 3);
      const u32 ra = L0 + L_LN + (lane & 7) * LN_PITCH + ch0 * 16;
      const int go = WS_XN + (b >> 4) * 163840 + ((b >> 1) & 7) * 1024 + (b & 1) * 128 + (ch0 >> 2) * 8192 + (ch0 & 3) * 256 + (lane & 7) * 16;
#pragma unroll
      for (int i = 0; i < 5; ++i)
        __builtin_amdgcn_raw_buffer_store_b128(__builtin_bit_cast(u32x4, ld128(ra + i * 128)), rs, go, i * 16384, 16);
      publish(ws + WS_XNFLAG + b * 8 + wave * 4, tag, lane);
    } else {
      const u32 ra = L0 + L_LN + (8 + q4) * LN_PITCH + (20 * (wave - 2) + g) * 32 + p4 * 8;
#pragma unroll
      for (int i = 0; i < 5; ++i) {
        const int jj = 20 * (wave - 2) + 4 * i + g, d5 = jj / 5;
        const int go = WS_H + d5 * 327680 + (jj - 5 * d5) * 1024 + (b >> 2) * 5120 + (b & 3) * 256 + c16 * 16;
        __builtin_amdgcn_raw_buffer_store_b128(__builtin_bit_cast(u32x4, cat(ldtr(ra + i * 128), ldtr(ra + i * 128 + 4 * LN_PITCH))), rs, go, 0, 16);
      }
      publish(ws + WS_HFLAG + b * 8 + (wave - 2) * 4, tag, lane);
    }
    STAMP_AT(2);
    BAR();
  }

  if (roleA) {
    f32x4 aq[5][2], ak[5][2], av[5][2];
#pragma unroll
    for (int j = 0; j < 5; ++j)
#pragma unroll
      for (int i = 0; i < 2; ++i) { aq[j][i] = f32x4{1.f, 1.f, 1.f, 1.f}; ak[j][i] = aq[j][i]; av[j][i] = f32x4{0.f, 0.f, 0.f, 0.f}; }
    const u32 wa0 = L0 + ((g >> 1) * 16 + (g & 1) * 4 + q4) * 480 + p4 * 8;
    const u32 xa0 = L0 + LQ_B + wave * 2048 + lane * 16;
    s16x8 xp[2] = {}, wv[5] = {};
#pragma unroll 1
    for (int r = 0; r < 20; ++r) {
      const u32 wa = wa0 + (r & 7) * 15360, xa = xa0 + (r & 3) * 8192;
      BAR();
      s16x8 xn[2], wq[5], wk[5];
      xn[0] = ld128(xa); xn[1] = ld128(xa + 1024);
#pragma unroll
      for (int j = 0; j < 5; ++j) wq[j] = cat(ldtr(wa + j * 32), ldtr(wa + 3840 + j * 32));
#pragma unroll
      for (int j = 0; j < 5; ++j) { av[j][0] = mfma32(xp[0], wv[j], av[j][0]); av[j][1] = mfma32(xp[1], wv[j], av[j][1]); }
      __builtin_amdgcn_sched_group_barrier(0x100, 2, 0);
#pragma unroll
      for (int j = 0; j < 10; ++j) { __builtin_amdgcn_sched_group_barrier(0x8, 1, 0); __builtin_amdgcn_sched_group_barrier(0x100, 1, 0); }
      __builtin_amdgcn_sched_barrier(0);
#pragma unroll
      for (int j = 0; j < 5; ++j) wk[j] = cat(ldtr(wa + 160 + j * 32), ldtr(wa + 160 + 3840 + j * 32));
#pragma unroll
      for (int j = 0; j < 5; ++j) { aq[j][0] = mfma32(wq[j], xn[0], aq[j][0]); aq[j][1] = mfma32(wq[j], xn[1], aq[j][1]); }
#pragma unroll
      for (int j = 0; j < 10; ++j) { __builtin_amdgcn_sched_group_barrier(0x8, 1, 0); __builtin_amdgcn_sched_group_barrier(0x100, 1, 0); }
      __builtin_amdgcn_sched_barrier(0);
#pragma unroll
      for (int j = 0; j < 5; ++j) wv[j] = cat(ldtr(wa + 320 + j * 32), ldtr(wa + 320 + 3840 + j * 32));
#pragma unroll
      for (int j = 0; j < 5; ++j) { ak[j][0] = mfma32(xn[0], wk[j], ak[j][0]); ak[j][1] = mfma32(xn[1], wk[j], ak[j][1]); }
#pragma unroll
      for (int j = 0; j < 5; ++j) { __builtin_amdgcn_sched_group_barrier(0x8, 1, 0); __builtin_amdgcn_sched_group_barrier(0x100, 2, 0); }
      __builtin_amdgcn_sched_group_barrier(0x8, 5, 0);
      __builtin_amdgcn_sched_barrier(0);
      xp[0] = xn[0]; xp[1] = xn[1];
    }
    BAR();
    STAMP_AT(3);
#pragma unroll
    for (int j = 0; j < 5; ++j) { av[j][0] = mfma32(xp[0], wv[j], av[j][0]); av[j][1] = mfma32(xp[1], wv[j], av[j][1]); }
    {
      s16x8 pk[5], pv[5];
#pragma unroll
      for (int j = 0; j < 5; ++j) { pk[j] = cat(pk4(phi4(ak[j][0])), pk4(phi4(ak[j][1]))); pv[j] = cat(pk4(av[j][0]), pk4(av[j][1])); }
      const s16x8 ones = {0x3f80, 0x3f80, 0x3f80, 0x3f80, 0x3f80, 0x3f80, 0x3f80, 0x3f80};
      const f32x4 z4 = {0.f, 0.f, 0.f, 0.f};
      const u32 rb = L0 + L_RED + wave * 6720;
#pragma unroll
      for (int je = 0; je < 5; ++je) {
        const int e = 16 * je + c16, he = e / 20;
#pragma unroll
        for (int jd = je - 1; jd <= je + 1; ++jd) {
          if (jd < 0 || jd > 4) continue;
          const f32x4 t = mfma32(pk[jd], pv[je], z4);
          const int dl = 4 * jd + g - 5 * he;
          if (dl >= 0 && dl < 5) *(LAS f32x4*)(rb + (e * 5 + dl) * 16) = t;
        }
        const f32x4 t = mfma32(pk[je], ones, z4);
        if (c16 == 0) *(LAS f32x4*)(rb + (400 + 4 * je + g) * 16) = t;
      }
    }
    STAMP_AT(4);
    BAR();
    const int t2 = tid < 210 ? tid : 209;
    {
      f32x4 s0 = {0.f, 0.f, 0.f, 0.f}, s1 = s0;
#pragma unroll
      for (int w = 0; w < 4; ++w) {
        s0 += *(const LAS f32x4*)(L0 + L_RED + w * 6720 + t2 * 32);
        s1 += *(const LAS f32x4*)(L0 + L_RED + w * 6720 + t2 * 32 + 16);
      }
      if (tid < 210) __builtin_amdgcn_raw_buffer_store_b128(__builtin_bit_cast(u32x4, cat(pk4(s0), pk4(s1))), rs, WS_PART + (hg * 16 + rt) * 3456 + tid * 16, 0, 16);
      publish(ws + WS_KVFLAG + hg * 256 + rt * 16 + wave * 4, tag, lane);
    }
    STAMP_AT(5);
    s16x4 pq[5][2];
#pragma unroll
    for (int j = 0; j < 5; ++j) { pq[j][0] = pk4(phi4(aq[j][0])); pq[j][1] = pk4(phi4(aq[j][1])); }
    poll(ws + WS_KVFLAG + hg * 256, 64, tag, lane);
    STAMP_AT(6);
    {
      u32x4 pr[16];
#pragma unroll
      for (int i = 0; i < 16; ++i) pr[i] = __builtin_amdgcn_raw_buffer_load_b128(rs, WS_PART + hg * 16 * 3456 + t2 * 16, i * 3456, 16);
      f32x4 s0 = {0.f, 0.f, 0.f, 0.f}, s1 = s0;
#pragma unroll
      for (int i = 0; i < 16; ++i) {
        s0 += f32x4{blo(pr[i][0]), bhi(pr[i][0]), blo(pr[i][1]), bhi(pr[i][1])};
        s1 += f32x4{blo(pr[i][2]), bhi(pr[i][2]), blo(pr[i][3]), bhi(pr[i][3])};
      }
      s16x8 fin = cat(pk4(s0), pk4(s1));
      if (tid >= 210) fin = s16x8{0, 0, 0, 0, 0, 0, 0, 0};
      if (tid <= 210) *(LAS s16x8*)(L0 + L_FIN + tid * 16) = fin;
    }
    STAMP_AT(7);
    BAR();
    {
      s16x4 kq[5][3], zq[5];
#pragma unroll
      for (int je = 0; je < 5; ++je) {
        const int e = 16 * je + c16, he = e / 20;
#pragma unroll
        for (int x = 0; x < 3; ++x) {
          const int jd = je - 1 + x;
          if (jd < 0 || jd > 4) { kq[je][x] = s16x4{0, 0, 0, 0}; continue; }
          const int dl = 4 * jd + g - 5 * he;
          kq[je][x] = ld64(L0 + L_FIN + (dl >= 0 && dl < 5 ? e * 5 + dl : 420) * 8);
        }
        const int dq = 4 * je + g;
        zq[je] = ld64(L0 + L_FIN + (dq / 5 == (c16 & 3) ? 400 + dq : 420) * 8);
      }
      BAR();
      const s16x4 zz = {0, 0, 0, 0};
      const int ob = WS_O + (2 * rt + (wave >> 1)) * 81920 + hg * 10240 + (wave & 1) * 5120 + lane * 16;
      s16x4 last[2];
#pragma unroll
      for (int i = 0; i < 2; ++i) {
        const s16x8 p01 = cat(pq[0][i], pq[1][i]), p23 = cat(pq[2][i], pq[3][i]);
        f32x4 den = {1e-6f, 1e-6f, 1e-6f, 1e-6f};
        den = mfma32(cat(zq[0], zq[1]), p01, den);
        den = mfma32(cat(zq[2], zq[3]), p23, den);
        den = mfma16(zq[4], pq[4][i], den);
        const f32x4 z4 = {0.f, 0.f, 0.f, 0.f};
        f32x4 o[5];
        o[0] = mfma32(cat(kq[0][1], kq[0][2]), p01, z4);
        o[1] = mfma32(cat(kq[1][2], zz), p23, mfma32(cat(kq[1][0], kq[1][1]), p01, z4));
        o[2] = mfma32(cat(kq[2][1], kq[2][2]), p23, mfma32(cat(zz, kq[2][0]), p01, z4));
        o[3] = mfma16(kq[3][2], pq[4][i], mfma32(cat(kq[3][0], kq[3][1]), p23, z4));
        o[4] = mfma16(kq[4][1], pq[4][i], mfma32(cat(zz, kq[4][0]), p23, z4));
        const float r0 = __builtin_amdgcn_rcpf(den[0]), r1 = __builtin_amdgcn_rcpf(den[1]), r2 = __builtin_amdgcn_rcpf(den[2]), r3 = __builtin_amdgcn_rcpf(den[3]);
        o[0] *= r0;
        o[1] *= g < 1 ? r0 : r1;
        o[2] *= g < 2 ? r1 : r2;
        o[3] *= g < 3 ? r2 : r3;
        o[4] *= r3;
        __builtin_amdgcn_raw_buffer_store_b128(__builtin_bit_cast(u32x4, cat(pk4(o[0]), pk4(o[1]))), rs, ob, i * 2048, 16);
        __builtin_amdgcn_raw_buffer_store_b128(__builtin_bit_cast(u32x4, cat(pk4(o[2]), pk4(o[3]))), rs, ob, i * 2048 + 1024, 16);
        last[i] = pk4(o[4]);
      }
      __builtin_amdgcn_raw_buffer_store_b128(__builtin_bit_cast(u32x4, cat(last[0], last[1])), rs, ob, 4096, 16);
      publish(ws + WS_OFLAG + (2 * rt + (wave >> 1)) * 64 + (wave & 1) * 32 + hg * 4, tag, lane);
    }
  } else if (roleB) {
    const bool live = ft < 13 || wave < 2;
    f32x4 acc[2][5];
#pragma unroll
    for (int t = 0; t < 2; ++t) {
      const u32x2 bb = *(const GAS u32x2*)(P.b1 + (live ? (ft * 128 + wave * 32 + t * 16 + g * 4) * 2 : 0));
      const f32x4 bi = {blo(bb[0]), bhi(bb[0]), blo(bb[1]), bhi(bb[1])};
#pragma unroll
      for (int j = 0; j < 5; ++j) acc[t][j] = bi;
    }
    const u32 aa0 = L0 + (8 * g + q4) * 256 + ((2 * wave) ^ (q4 | ((g & 1) << 2))) * 32 + p4 * 8;
    u32 ha = L0 + L1_B + lane * 16;
    s16x8 a1[2] = {}, h1[5] = {};
#pragma unroll 1
    for (int r = 0; r < 32; ++r) {
      const u32 aa = aa0 + (r & 7) * 16384, ab = aa ^ 32;
      BAR();
      s16x8 a0[2], h0[5];
      a0[0] = cat(ldtr(aa), ldtr(aa + 1024)); a0[1] = cat(ldtr(ab), ldtr(ab + 1024));
#pragma unroll
      for (int j = 0; j < 5; ++j) h0[j] = ld128(ha + j * 1024);
#pragma unroll
      for (int j = 0; j < 5; ++j) { acc[0][j] = mfma32(a1[0], h1[j], acc[0][j]); acc[1][j] = mfma32(a1[1], h1[j], acc[1][j]); }
#pragma unroll
      for (int j = 0; j < 9; ++j) { __builtin_amdgcn_sched_group_barrier(0x100, 1, 0); __builtin_amdgcn_sched_group_barrier(0x8, 1, 0); }
      __builtin_amdgcn_sched_barrier(0);
      a1[0] = cat(ldtr(aa + 8192), ldtr(aa + 9216)); a1[1] = cat(ldtr(ab + 8192), ldtr(ab + 9216));
#pragma unroll
      for (int j = 0; j < 5; ++j) h1[j] = ld128(ha + 5120 + j * 1024);
#pragma unroll
      for (int j = 0; j < 5; ++j) { acc[0][j] = mfma32(a0[0], h0[j], acc[0][j]); acc[1][j] = mfma32(a0[1], h0[j], acc[1][j]); }
#pragma unroll
      for (int j = 0; j < 4; ++j) { __builtin_amdgcn_sched_group_barrier(0x100, 2, 0); __builtin_amdgcn_sched_group_barrier(0x8, 1, 0); }
      __builtin_amdgcn_sched_group_barrier(0x100, 1, 0);
      __builtin_amdgcn_sched_group_barrier(0x8, 6, 0);
      __builtin_amdgcn_sched_barrier(0);
      ha = ha >= L0 + L1_B + 2 * 10240 ? ha - 2 * 10240 : ha + 10240;
    }
    BAR();
    STAMP_AT(3);
#pragma unroll
    for (int j = 0; j < 5; ++j) { acc[0][j] = mfma32(a1[0], h1[j], acc[0][j]); acc[1][j] = mfma32(a1[1], h1[j], acc[1][j]); }
    if (live) {
      const int go = WS_G + dt * 276480 + (ft * 4 + wave) * 5120 + lane * 16;
#pragma unroll
      for (int j = 0; j < 5; ++j)
        __builtin_amdgcn_raw_buffer_store_b128(__builtin_bit_cast(u32x4, cat(pk4(gelu4(acc[0][j])), pk4(gelu4(acc[1][j])))), rs, go, j * 1024, 16);
      publish(ws + WS_GFLAG + dt * 256 + (ft * 4 + wave) * 4, tag, lane);
    }
  }

  STAMP_AT(8);
  {
    const int srow = st * 64 + wave * 16 + c16;
    const int xo = srow * 1280 + dt3 * 160 + g * 8;
    f32x4 acc[5];
    {
      const float bs = blo(*(const GAS unsigned short*)(P.b2 + srow * 2));
#pragma unroll
      for (int j = 0; j < 5; ++j) {
        const u32x2 xx = *(const GAS u32x2*)(P.x + xo + j * 32);
        acc[j] = f32x4{blo(xx[0]), bhi(xx[0]), blo(xx[1]), bhi(xx[1])} + bs;
      }
    }
    const u32 aa0 = (4 * g + q4) * 128 + (wave ^ ((2 * g + (q4 >> 1)) & 3)) * 32 + p4 * 8;
    const u32 ga0 = L0 + L3_B + lane * 16;
    u32 sa = L0;
    s16x8 a1 = {}, g1[5] = {};
#pragma unroll 1
    for (int r = 0; r < 27; ++r) {
      const u32 aa = aa0 + sa, ga = ga0 + (r & 3) * 10240;
      BAR();
      s16x8 a0, g0[5];
      a0 = cat(ldtr(aa), ldtr(aa + 2048));
#pragma unroll
      for (int j = 0; j < 5; ++j) g0[j] = ld128(ga + j * 1024);
#pragma unroll
      for (int j = 0; j < 5; ++j) acc[j] = mfma32(g1[j], a1, acc[j]);
      __builtin_amdgcn_sched_group_barrier(0x100, 7, 0);
      __builtin_amdgcn_sched_group_barrier(0x8, 5, 0);
      __builtin_amdgcn_sched_barrier(0);
      a1 = cat(ldtr(aa + 4096), ldtr(aa + 6144));
#pragma unroll
      for (int j = 0; j < 5; ++j) g1[j] = ld128(ga + 5120 + j * 1024);
#pragma unroll
      for (int j = 0; j < 5; ++j) acc[j] = mfma32(g0[j], a0, acc[j]);
      __builtin_amdgcn_sched_group_barrier(0x100, 7, 0);
      __builtin_amdgcn_sched_group_barrier(0x8, 5, 0);
      __builtin_amdgcn_sched_barrier(0);
      sa = sa == L0 + 14 * 8192 ? L0 : sa + 8192;
    }
#pragma unroll
    for (int j = 0; j < 5; ++j) acc[j] = mfma32(g1[j], a1, acc[j]);
    STAMP_AT(9);
    const u32 wa0 = (4 * g + q4) * 160 + p4 * 8;
    const u32 oa0 = ga0 + (wave >> 1) * 5120;
#pragma unroll 1
    for (int r = 27; r < 35; ++r) {
      const u32 wa = wa0 + sa;
      sa = sa == L0 + 14 * 8192 ? L0 : sa + 8192;
      const u32 wb = wa0 + sa;
      sa = sa == L0 + 14 * 8192 ? L0 : sa + 8192;
      const u32 oa = oa0 + (r & 3) * 10240;
      BAR();
      const s16x8 o0 = ld128(oa + (wave & 1) * 2048), o1 = ld128(oa + (wave & 1) * 2048 + 1024);
      const s16x4 o2 = ld64(oa + 4096 + (wave & 1) * 8);
#pragma unroll
      for (int j = 0; j < 5; ++j) {
        acc[j] = mfma32(cat(ldtr(wa + j * 32), ldtr(wa + 2560 + j * 32)), o0, acc[j]);
        acc[j] = mfma32(cat(ldtr(wa + 5120 + j * 32), ldtr(wb + j * 32)), o1, acc[j]);
        acc[j] = mfma16(ldtr(wb + 2560 + j * 32), o2, acc[j]);
      }
    }
#pragma unroll
    for (int j = 0; j < 5; ++j) *(GAS s16x4*)(P.out + xo + j * 32) = pk4(acc[j]);
  }
}

extern "C" void kernel_launch(void* const* d_in, const int* in_sizes, int n_in, void* d_out, int out_size, void* d_ws,
                              size_t ws_size, hipStream_t stream) {
  Params p;
  p.x = (const char*)d_in[0];
  p.wqkv = (const char*)d_in[1];
  p.wo = (const char*)d_in[2];
  p.w1 = (const char*)d_in[4];
  p.b1 = (const char*)d_in[5];
  p.w2 = (const char*)d_in[6];
  p.b2 = (const char*)d_in[7];
  p.out = (char*)d_out;
  p.ws = (char*)d_ws;
  (void)hipFuncSetAttribute((const void*)fused_fwd, hipFuncAttributeMaxDynamicSharedMemorySize, L_BYTES);
  fused_fwd<<<256, 320, L_BYTES, stream>>>(p);
}
```

```cpp
#include <hip/hip_runtime.h>
#include <hip/hip_bf16.h>
#pragma clang diagnostic ignored "-Wint-to-pointer-cast"

#define GAS __attribute__((address_space(1)))
#define LAS __attribute__((address_space(3)))
#define DI __device__ __forceinline__
typedef unsigned u32;
typedef unsigned long long u64;
typedef short s16x4 __attribute__((ext_vector_type(4)));
typedef short s16x8 __attribute__((ext_vector_type(8)));
typedef __bf16 b16x2 __attribute__((ext_vector_type(2)));
typedef __bf16 b16x8 __attribute__((ext_vector_type(8)));
typedef float f32x2 __attribute__((ext_vector_type(2)));
typedef float f32x4 __attribute__((ext_vector_type(4)));
typedef float f32x16 __attribute__((ext_vector_type(16)));
typedef unsigned u32x2 __attribute__((ext_vector_type(2)));
typedef unsigned u32x4 __attribute__((ext_vector_type(4)));

constexpr int WS_EPOCH = 0;
constexpr int WS_XNFLAG = 4096;
constexpr int WS_HFLAG = 8192;
constexpr int WS_KVFLAG = 12288;
constexpr int WS_GFLAG = 16384;
constexpr int WS_OFLAG = 20480;
constexpr int WS_PART = 32768;
constexpr int WS_XN = 524288;
constexpr int WS_H = WS_XN + 2621440;
constexpr int WS_G = WS_H + 2621440;
constexpr int WS_O = WS_G + 2211840;
constexpr int WS_WO = WS_O + 2621440;
constexpr int WS_END = WS_WO + 819200;
constexpr int OOB = 0x40000000;
constexpr int L_BYTES = 163840;
constexpr int LN_PITCH = 1296;
constexpr int LQ_B = 122880, L1_B = 131072, L3_B = 122880;
constexpr int L_KV = 114688, L_FIN = L_KV + 32 * LN_PITCH;
constexpr u32 SPIN_CAP = 1u << 16;

#define BAR() do { asm volatile("" ::: "memory"); __builtin_amdgcn_s_waitcnt(0xC07F); asm volatile("s_barrier" ::: "memory"); } while (0)
#define WAITV(n) asm volatile("s_waitcnt vmcnt(" #n ")" ::: "memory")
#define STAMP_AT(k)
#define STAMP_DECL
#define ACC_TO(k)
#define STAMP_OUT()
#define ACC1(k)
#define ACC3(k)
#define LW1(n) do { ACC1(13); WAITV(n); ACC1(11); } while (0)
#define LB1() do { BAR(); ACC1(12); } while (0)
#define LW3(n) do { ACC3(13); WAITV(n); ACC3(11); } while (0)
#define LB3() do { BAR(); ACC3(12); } while (0)
#define RLX_AGENT __ATOMIC_RELAXED, __HIP_MEMORY_SCOPE_AGENT

DI f32x4 mfma32(s16x8 a, s16x8 b, f32x4 c) {
  return __builtin_amdgcn_mfma_f32_16x16x32_bf16(__builtin_bit_cast(b16x8, a), __builtin_bit_cast(b16x8, b), c, 0, 0, 0);
}
DI s16x4 ldtr(u32 a) { return __builtin_amdgcn_ds_read_tr16_b64_v4i16((LAS s16x4*)a); }
DI s16x8 ld128(u32 a) { return *(const LAS s16x8*)a; }
DI s16x4 ld64(u32 a) { return *(const LAS s16x4*)a; }
DI s16x8 cat(s16x4 a, s16x4 b) { return __builtin_shufflevector(a, b, 0, 1, 2, 3, 4, 5, 6, 7); }
DI u32 pk2(float a, float b) { f32x2 v = {a, b}; return __builtin_bit_cast(u32, __builtin_convertvector(v, b16x2)); }
DI s16x4 pk4(f32x4 v) { u32x2 r = {pk2(v[0], v[1]), pk2(v[2], v[3])}; return __builtin_bit_cast(s16x4, r); }
DI float blo(u32 u) { return __uint_as_float(u << 16); }
DI float bhi(u32 u) { return __uint_as_float(u & 0xffff0000u); }
template <int C> DI float dpp_add(float s) { return s + __int_as_float(__builtin_amdgcn_update_dpp(0, __float_as_int(s), C, 0xf, 0xf, true)); }
DI float sum32(float s) {
  s = dpp_add<0x121>(s); s = dpp_add<0x122>(s); s = dpp_add<0x124>(s); s = dpp_add<0x128>(s);
  auto r = __builtin_amdgcn_permlane16_swap(__float_as_uint(s), __float_as_uint(s), false, false);
  return __uint_as_float(r[0]) + __uint_as_float(r[1]);
}
DI u32 phi2(float a0, float a1) {
  const float L = 1.4426950408889634f;
  const f32x2 a = {a0, a1};
  const f32x2 y = __builtin_elementwise_fma(a, f32x2{L, L}, f32x2{-L, -L});
  const float e0 = __builtin_amdgcn_fmed3f(__builtin_amdgcn_exp2f(y[0]), 0.f, 1.f), e1 = __builtin_amdgcn_fmed3f(__builtin_amdgcn_exp2f(y[1]), 0.f, 1.f);
  f32x2 r = {__int_as_float(max(__float_as_int(a0), __float_as_int(e0))), __int_as_float(max(__float_as_int(a1), __float_as_int(e1)))};
  r += 1e-6f;
  return pk2(r[0], r[1]);
}
DI s16x4 phi4(f32x4 a) { u32x2 r = {phi2(a[0], a[1]), phi2(a[2], a[3])}; return __builtin_bit_cast(s16x4, r); }
DI u32 gelu2(float u0, float u1) {
  const float K = -2.f * 0.7978845608028654f * 1.4426950408889634f, K3 = K * 0.044715f;
  const f32x2 u = {u0, u1};
  const f32x2 y = u * __builtin_elementwise_fma(u * u, f32x2{K3, K3}, f32x2{K, K});
  f32x2 d = {__builtin_amdgcn_exp2f(y[0]), __builtin_amdgcn_exp2f(y[1])};
  d += 1.f;
  const f32x2 r = u * f32x2{__builtin_amdgcn_rcpf(d[0]), __builtin_amdgcn_rcpf(d[1])};
  return pk2(r[0], r[1]);
}
DI s16x4 gelu4(f32x4 a) { u32x2 r = {gelu2(a[0], a[1]), gelu2(a[2], a[3])}; return __builtin_bit_cast(s16x4, r); }
DI void st128(__amdgpu_buffer_rsrc_t rs, int off, s16x8 v) { __builtin_amdgcn_raw_buffer_store_b128(__builtin_bit_cast(u32x4, v), rs, off, 0, 16); }
DI void st64(__amdgpu_buffer_rsrc_t rs, int off, s16x4 v) { __builtin_amdgcn_raw_buffer_store_b64(__builtin_bit_cast(u32x2, v), rs, off, 0, 16); }
#define SG(m, n) __builtin_amdgcn_sched_group_barrier(m, n, 0)
#define SB() __builtin_amdgcn_sched_barrier(0)
DI u32 mix(u32 e) { return e * 0x9E3779B1u + 0x7F4A7C15u; }

#define GL(v, off, sc) "global_load_lds_dwordx4 " v ", %0 offset:" #off sc "\n\t"
DI void gl4(const char* base, u32 m, u32 v0, u32 v1, u32 v2, u32 v3) {
  asm volatile("s_mov_b32 m0, %1\n\ts_nop 0\n\t" GL("%2", 0, "") GL("%3", 1024, "") GL("%4", 2048, "") GL("%5", 3072, "")
               ::"s"(base), "s"(m), "v"(v0), "v"(v1), "v"(v2), "v"(v3) : "memory");
}
DI void gl4w(const char* base, u32 m, u32 v0, u32 v1, u32 v2, u32 v3) {
  asm volatile("s_mov_b32 m0, %1\n\ts_mov_b32 exec_hi, 0x0fffffff\n\t" GL("%2", 0, "") GL("%3", 960, "") GL("%4", 1920, "") GL("%5", 2880, "")
               "s_mov_b32 exec_hi, -1"
               ::"s"(base), "s"(m), "v"(v0), "v"(v1), "v"(v2), "v"(v3) : "memory");
}
DI void gl1(const char* base, u32 m, u32 v0) {
  asm volatile("s_mov_b32 m0, %1\n\ts_nop 0\n\t" GL("%2", 0, "")::"s"(base), "s"(m), "v"(v0) : "memory");
}
DI void glin8(const char* base, u32 m, u32 vl, u32 vl4) {
  asm volatile("s_mov_b32 m0, %1\n\ts_nop 0\n\t" GL("%2", 0, " sc1") GL("%2", 1024, " sc1") GL("%2", 2048, " sc1") GL("%2", 3072, " sc1")
               "s_add_u32 m0, m0, 4096\n\ts_nop 0\n\t" GL("%3", 0, " sc1") GL("%3", 1024, " sc1") GL("%3", 2048, " sc1") GL("%3", 3072, " sc1")
               ::"s"(base), "s"(m), "v"(vl), "v"(vl4) : "memory", "scc");
}
DI void glin10(const char* base, u32 m, u32 vl, u32 vl4, u32 vl8) {
  asm volatile("s_mov_b32 m0, %1\n\ts_nop 0\n\t" GL("%2", 0, " sc1") GL("%2", 1024, " sc1") GL("%2", 2048, " sc1") GL("%2", 3072, " sc1")
               "s_add_u32 m0, m0, 4096\n\ts_nop 0\n\t" GL("%3", 0, " sc1") GL("%3", 1024, " sc1") GL("%3", 2048, " sc1") GL("%3", 3072, " sc1")
               "s_add_u32 m0, m0, 4096\n\ts_nop 0\n\t" GL("%4", 0, " sc1") GL("%4", 1024, " sc1")
               ::"s"(base), "s"(m), "v"(vl), "v"(vl4), "v"(vl8) : "memory", "scc");
}
DI void touch(const char* base, u32 m, u32 v) {
  asm volatile("s_mov_b32 m0, %1\n\ts_nop 0\n\tglobal_load_lds_dword %2, %0" ::"s"(base), "s"(m), "v"(v) : "memory");
}
DI void poll(const char* f, int idx, u32 tag) {
  const GAS u32* p = (const GAS u32*)f + idx;
#pragma unroll 1
  for (u32 sp = 0; sp < SPIN_CAP; ++sp) {
    u32 v = __hip_atomic_load(p, RLX_AGENT);
    if (__all(v == tag)) break;
    __builtin_amdgcn_s_sleep(1);
  }
  asm volatile("" ::: "memory");
}
DI void poll512(__amdgpu_buffer_rsrc_t rs, int off, u32 tag, bool rows62) {
#pragma unroll 1
  for (u32 sp = 0; sp < SPIN_CAP; ++sp) {
    u32x4 a = __builtin_amdgcn_raw_buffer_load_b128(rs, off, 0, 16);
    u32x4 c = __builtin_amdgcn_raw_buffer_load_b128(rs, off + 1024, 0, 16);
    a = (a ^ tag) | (c ^ tag);
    if (__all((a[0] | a[1] | (rows62 ? 0u : a[2] | a[3])) == 0)) break;
    __builtin_amdgcn_s_sleep(1);
    asm volatile("" ::: "memory");
  }
  asm volatile("" ::: "memory");
}
DI void publish(char* f, u32 tag, int lane) {
  WAITV(0);
  if (lane == 0) __hip_atomic_store((GAS u32*)f, tag, RLX_AGENT);
}

struct Params {
  const char *x, *wqkv, *wo, *w1, *b1, *w2, *b2;
  char *out, *ws;
};
#define CODE_LINES 14

__global__ __launch_bounds__(512) void fused_fwd(Params P) {
  extern __shared__ __attribute__((aligned(16))) char lds_[];
  const int tid = threadIdx.x, lane = tid & 63;
  const int wave = __builtin_amdgcn_readfirstlane(tid >> 6);
  const int b = blockIdx.x, xc = b & 7, jb = b >> 3;
  char* const ws = P.ws;
  const u32 L0 = (u32)(size_t)(LAS char*)lds_;
  const u32 LDUM = L0 + L_BYTES - 256;
  const int c16 = lane & 15, g = lane >> 4, q4 = c16 >> 2, p4 = lane & 3;
  const __amdgpu_buffer_rsrc_t rs = __builtin_amdgcn_make_buffer_rsrc(ws, 0, WS_END, 0x00020000);
  STAMP_DECL;
  STAMP_AT(0);
  u32 e0 = *(const __attribute__((address_space(4))) u32*)(ws + WS_EPOCH);
  const bool roleA = b < 128;
  const int ib = jb - 16;
  const bool roleB = !roleA && (xc < 6 || ib < 8);

  if (!roleA) {
    if (wave < 3) {
      const u32 t = tid < 180 ? tid : 179, rw = t / 9u, k = t - 9 * rw, sg = k / 3u;
      touch(P.wqkv + ((xc >> 2) * 320 + ib * 20) * 3840 + ((320 * (xc & 3)) & ~127), LDUM, rw * 3840 + sg * 896 + k * 128);
    } else if (wave == 6) {
      u64 pc;
      asm volatile("s_getpc_b64 %0" : "=s"(pc));
      touch((const char*)(pc & ~255ull) + ib * (CODE_LINES * 128), LDUM, (lane < CODE_LINES ? lane : 0) * 128);
    }
    if (wave > 4) return;
    asm volatile("" : "+s"(e0));
  } else {
    const int row = 2 * wave + (lane >> 5), hl = lane & 31;
    const GAS u32x2* xp = (const GAS u32x2*)(P.x + b * (16 * 1280) + (u32)(row * 1280 + hl * 8));
    u32x2 raw[5];
#pragma unroll
    for (int i = 0; i < 5; ++i) raw[i] = xp[32 * i];
    f32x2 v[10];
#pragma unroll
    for (int i = 0; i < 5; ++i) { v[2 * i] = f32x2{blo(raw[i][0]), bhi(raw[i][0])}; v[2 * i + 1] = f32x2{blo(raw[i][1]), bhi(raw[i][1])}; }
    f32x2 s = v[0];
#pragma unroll
    for (int i = 1; i < 10; ++i) s += v[i];
    const float m = sum32(s[0] + s[1]) * (1.f / 640.f);
    f32x2 qq = {0.f, 0.f};
#pragma unroll
    for (int i = 0; i < 10; ++i) { v[i] -= m; qq += v[i] * v[i]; }
    const float r1 = __builtin_amdgcn_rsqf(sum32(qq[0] + qq[1]) * (1.f / 640.f) + 1e-5f);
    s = f32x2{0.f, 0.f}; qq = s;
#pragma unroll
    for (int i = 0; i < 10; ++i) { v[i] *= r1; asm("" : "+v"(v[i])); s += v[i]; qq += v[i] * v[i]; }
    const u32 wa = L0 + row * LN_PITCH + hl * 8;
#pragma unroll
    for (int i = 0; i < 5; ++i) *(LAS u32x2*)(wa + i * 256) = u32x2{pk2(v[2 * i][0], v[2 * i][1]), pk2(v[2 * i + 1][0], v[2 * i + 1][1])};
    const float m2 = sum32(s[0] + s[1]) * (1.f / 640.f);
    const float r2 = __builtin_amdgcn_rsqf(sum32(qq[0] + qq[1]) * (1.f / 640.f) - m2 * m2 + 1e-5f);
    const float mr = -m2 * r2;
#pragma unroll
    for (int i = 0; i < 10; ++i) v[i] = __builtin_elementwise_fma(v[i], f32x2{r2, r2}, f32x2{mr, mr});
#pragma unroll
    for (int i = 0; i < 5; ++i) *(LAS u32x2*)(wa + 16 * LN_PITCH + i * 256) = u32x2{pk2(v[2 * i][0], v[2 * i][1]), pk2(v[2 * i + 1][0], v[2 * i + 1][1])};
    asm volatile("" : "+s"(e0));
    STAMP_AT(1);
    BAR();
    if (wave < 4) {
      const int dl = 2 * wave + (g >> 1);
      const u32 ra = L0 + (16 + 8 * (g & 1) + q4) * LN_PITCH + dl * 160 + p4 * 8, rb = ra + p4 * 8;
      s16x8 f[5];
#pragma unroll
      for (int j = 0; j < 4; ++j) f[j] = cat(ldtr(rb + (j >> 1) * 64 + (j & 1) * 8), ldtr(rb + (j >> 1) * 64 + (j & 1) * 8 + 4 * LN_PITCH));
      f[4] = cat(ldtr(ra + 128), ldtr(ra + 128 + 4 * LN_PITCH));
      const int go = WS_H + (b >> 1) * 5120 + (b & 1) * 512 + dl * 327680 + (lane & 31) * 16;
      SB();
#pragma unroll
      for (int j = 0; j < 5; ++j) st128(rs, go + j * 1024, f[j]);
      BAR();
      publish(ws + WS_HFLAG + b * 16 + wave * 4, mix(e0), lane);
      STAMP_AT(2);
    } else if (wave > 4) {
      const int ks = wave == 7 ? 13 : 7 * (wave - 5);
      const u32 ra = L0 + c16 * LN_PITCH + ks * 64 + g * 16;
      s16x8 f[7];
#pragma unroll
      for (int i = 0; i < 7; ++i) f[i] = ld128(ra + i * 64);
      const int go = WS_XN + (b >> 3) * 163840 + (b & 7) * 1024 + ks * 8192 + lane * 16;
      SB();
#pragma unroll
      for (int i = 0; i < 7; ++i) st128(rs, go + i * 8192, f[i]);
      BAR();
      publish(ws + WS_XNFLAG + b * 16 + (wave - 5) * 4, mix(e0), lane);
      return;
    }
  }
  const u32 tag = mix(e0);
  const int hg = 2 * (xc & 3) + (jb & 1), rt = 8 * (xc >> 2) + (jb >> 1);
  const int ft = xc < 6 ? 2 * xc + (ib >> 3) : 6 + xc, dt = ib & 7;
  const int sp = (xc - 6) * 8 + ib - 8;
  const int st = 4 * xc + (jb & 3), dt3 = jb >> 2;

  if (wave == 4) {
    __builtin_amdgcn_s_setprio(3);
    const u32 vl = lane * 16, vl4 = vl + 4096, vl8 = vl + 8192;
    if (roleB) {
      u32 v[4];
#pragma unroll
      for (int i = 0; i < 4; ++i) {
        int f = g | (((i >> 1) & 1) << 2);
        int ch = c16 ^ (f << 1);
        if (ft == 13) ch &= 7;
        v[i] = (4 * i + g) * 3456 + ch * 16 - i * 1024;
      }
      const char* wb = P.w1 + ft * 256;
      const char* hb = ws + WS_H + dt * 327680;
      u32 oa = dt * (256 * 3456), gs = 0, ob = dt * 40960;
      u32 sb = L0 + L1_B;
      auto agroup = [&]() {
        gl4(wb + oa, L0 + gs, v[0], v[1], v[2], v[3]);
        oa = oa == 127 * 55296 ? 0 : oa + 55296;
        gs = (gs + 4096) & 131071;
      };
      auto bunit = [&]() {
        glin10(hb + ob, sb, vl, vl4, vl8);
        ob = ob == 31 * 10240 ? 0 : ob + 10240;
        sb = sb == L0 + L1_B + 2 * 10240 ? L0 + L1_B : sb + 10240;
      };
      STAMP_AT(1);
#pragma unroll 1
      for (int i = 0; i < 16; ++i) { agroup(); agroup(); WAITV(32); }
      STAMP_AT(2);
      poll512(rs, WS_HFLAG + vl, tag, false);
      STAMP_AT(3); ACC1(-1);
      bunit(); bunit();
      LW1(10); LB1(); bunit();
      LW1(10); LB1(); bunit(); agroup(); agroup(); agroup();
      LW1(22);
#pragma unroll 1
      for (int r = 2; r < 28; r += 2) {
        LB1(); bunit(); agroup(); agroup(); agroup(); agroup();
        LW1(36); LB1(); bunit(); agroup(); agroup(); agroup();
        LW1(36);
      }
      LB1(); bunit(); agroup(); agroup();
      LW1(30); LB1(); bunit();
      LW1(18); LB1();
      LW1(0); LB1();
      BAR();
    } else if (roleA) {
      const u32 hi = lane >= 30, cc = lane - 30 * hi, sg = (cc >= 10) + (cc >= 20);
      const u32 v0 = hi * 3840 + sg * 1120 + cc * 16, v1 = v0 + 2 * 3840 - 960, v2 = v0 + 8 * 3840 - 1920, v3 = v0 + 10 * 3840 - 2880;
      const char* wb = P.wqkv + hg * 160;
      const char* xb = ws + WS_XN + rt * 163840;
      const u32 rot = (5 * (rt & 7)) >> 1;
      u32 oa = rot * 122880, ob = rot * 8192, st4 = 4 * 3840;
      u32 sa = L0, sb = L0 + LQ_B;
      auto agroup = [&]() {
        gl4w(wb + oa, sa, v0, v1, v2, v3);
        oa += st4; st4 ^= (4 * 3840) ^ (12 * 3840);
        oa = oa == 640 * 3840 ? 0 : oa;
        sa = sa == L0 + 31 * 3840 ? L0 : sa + 3840;
      };
      auto bunit = [&]() {
        glin8(xb + ob, sb, vl, vl4);
        ob = ob == 19 * 8192 ? 0 : ob + 8192;
        sb = sb == L0 + LQ_B + 3 * 8192 ? L0 + LQ_B : sb + 8192;
      };
      BAR();
      STAMP_AT(14);
#pragma unroll 1
      for (int i = 0; i < 12; ++i) { agroup(); agroup(); WAITV(12); }
      STAMP_AT(2);
      poll(ws + WS_XNFLAG + rt * 128, (lane & 31) - (p4 == 3), tag);
      STAMP_AT(3); ACC1(-1);
      bunit(); bunit(); bunit();
      LW1(16);
#pragma unroll 1
      for (int r = 0; r < 16; r += 2) {
        LB1(); agroup(); agroup(); agroup(); agroup(); bunit();
        LW1(32); LB1(); agroup(); agroup(); agroup(); bunit();
        LW1(38);
      }
      LB1(); bunit();
      LW1(28); LB1();
      {
        const char* tb = P.w2 + xc * 512 + jb * (108 * 4096);
        const u32 tv = (lane >> 2) * 4096 + p4 * 128;
#pragma unroll 1
        for (int i = 0; i < 7; ++i) touch(tb + (i == 6 ? 92 : 16 * i) * 4096, LDUM, tv);
      }
      LW1(15); LB1(); LW1(7); LB1();
      BAR();
    } else {
      const char* wb = P.wo + (sp >> 2) * (160 * 1280) + (sp & 3) * 320;
#pragma unroll 1
      for (int i = 0; i < 50; ++i) {
        const u32 n = 64 * i + lane, rw = n / 20u;
        gl1(wb, L0 + i * 1024, rw * 960 + n * 16);
      }
      WAITV(0);
      BAR();
      BAR();
    }
    STAMP_AT(4);
    {
      u32 v[4];
#pragma unroll
      for (int i = 0; i < 4; ++i) v[i] = (8 * i + (lane >> 3)) * 4096 + ((lane & 7) ^ (g << 1)) * 16 - i * 1024;
      const char* wb = P.w2 + st * 128;
      const char* bb = ws + WS_G + dt3 * 276480;
      const u32 rot = (27 * dt3) >> 3;
      u32 oa = rot * 262144, ob = rot * 10240, obl = 26 * 10240;
      u32 sa = L0, sb = L0 + L3_B;
      auto ahalf = [&]() {
        gl4(wb + oa, sa, v[0], v[1], v[2], v[3]);
        oa = oa == 53 * 131072 ? 0 : oa + 131072;
        sa = sa == L0 + 29 * 4096 ? L0 : sa + 4096;
      };
      auto bunit = [&]() {
        glin10(bb + ob, sb, vl, vl4, vl8);
        ob = ob == obl ? 0 : ob + 10240;
        sb = sb == L0 + L3_B + 3 * 10240 ? L0 + L3_B : sb + 10240;
      };
      if (roleA) { BAR(); BAR(); }
#pragma unroll 1
      for (int i = 0; i < 15; ++i) {
        if (i == 14 && roleA) { STAMP_AT(5); BAR(); BAR(); }
        ahalf(); ahalf();
        WAITV(32);
      }
      STAMP_AT(6);
      poll(ws + WS_GFLAG + dt3 * 256, lane < 62 ? lane : 0, tag);
      STAMP_AT(7); ACC3(-1);
      bunit(); bunit(); bunit();
      LW3(20); LB3(); bunit();
      LW3(20); LB3(); bunit(); ahalf();
      LW3(24);
#pragma unroll 1
      for (int r = 2; r < 19; ++r) { LB3(); bunit(); ahalf(); LW3(28); }
      u32 of;
      asm volatile("global_load_dword %0, %1, %2 sc1" : "=&v"(of) : "v"(c16 * 4), "s"(ws + WS_OFLAG + st * 64) : "memory");
#pragma unroll 1
      for (int r = 19; r < 23; ++r) { LB3(); bunit(); ahalf(); if (r > 20) ahalf(); LW3(32); }
      STAMP_AT(8);
      asm volatile("" : "+v"(of));
      if (!__all(of == tag)) poll(ws + WS_OFLAG + st * 64, c16, tag);
      STAMP_AT(9);
      const char* ab = ws + WS_O + st * 81920;
      oa = dt3 * 8192;
#pragma unroll 1
      for (int r = 23; r < 33; ++r) {
        LB3(); bunit();
        if (r == 23) { bb = ws + WS_WO + dt3 * 102400; ob = dt3 * 10240; obl = 9 * 10240; }
        glin8(ab + oa, sa, vl, vl4);
        oa = oa == 9 * 8192 ? 0 : oa + 8192;
        sa = sa == L0 + 14 * 8192 ? L0 : sa + 8192;
        LW3(44);
      }
      LB3(); bunit();
      LW3(36); LB3();
      LW3(18); LB3();
      LW3(0); LB3();
    }
    STAMP_AT(10);
    STAMP_OUT();
    return;
  }

  const int hw = wave >> 1, ty = wave & 1;
  if (roleA) {
    f32x4 aq[5][2], akv[5][4];
    {
      const float i2 = ty ? 0.f : 1.f;
#pragma unroll
      for (int j = 0; j < 5; ++j) {
        const float iv = j < 2 ? 1.f : j == 2 ? i2 : 0.f;
        aq[j][0] = aq[j][1] = f32x4{1.f, 1.f, 1.f, 1.f};
#pragma unroll
        for (int i = 0; i < 4; ++i) akv[j][i] = f32x4{iv, iv, iv, iv};
      }
    }
    const u32 wa0 = L0 + ((g >> 1) * 16 + (g & 1) * 4 + q4) * 480 + p4 * 8;
    const u32 xa0 = L0 + LQ_B + wave * 2048 + lane * 16;
    s16x8 xA[4] = {}, xB[4], wq[5] = {};
    auto round = [&](int r, s16x8(&xo)[4], s16x8(&xn)[4]) {
      const u32 wa = wa0 + (r & 7) * 15360, wk = wa + ty * 96, w2 = wa + ty * 160, xa = xa0 + (r & 3) * 8192, xb = xa ^ 2048;
      BAR();
      s16x8 w[5];
      xn[0] = ld128(xa); w[0] = cat(ldtr(wk + 160), ldtr(wk + 160 + 3840));
      xn[1] = ld128(xa + 1024); xn[2] = ld128(xb); xn[3] = ld128(xb + 1024);
      w[1] = cat(ldtr(wk + 192), ldtr(wk + 192 + 3840));
      w[2] = cat(ldtr(w2 + 224), ldtr(w2 + 224 + 3840));
#pragma unroll
      for (int j = 0; j < 5; ++j) { aq[j][0] = mfma32(wq[j], xo[0], aq[j][0]); aq[j][1] = mfma32(wq[j], xo[1], aq[j][1]); }
#pragma unroll
      for (int j = 0; j < 10; ++j) { SG(0x100, 1); SG(0x8, 1); }
      SB();
      w[3] = cat(ldtr(wk + 320), ldtr(wk + 320 + 3840));
      w[4] = cat(ldtr(wk + 352), ldtr(wk + 352 + 3840));
#pragma unroll
      for (int j = 0; j < 5; ++j) wq[j] = cat(ldtr(wa + j * 32), ldtr(wa + 3840 + j * 32));
#pragma unroll
      for (int j = 0; j < 5; ++j)
#pragma unroll
        for (int i = 0; i < 4; ++i) akv[j][i] = mfma32(xn[i], w[j], akv[j][i]);
#pragma unroll
      for (int j = 0; j < 14; ++j) { SG(0x8, 1); SG(0x100, 1); }
      SG(0x8, 6);
      SB();
    };
#pragma unroll 1
    for (int r = 0; r < 20; r += 2) { round(r, xA, xB); round(r + 1, xB, xA); }
    BAR();
    STAMP_AT(3);
#pragma unroll
    for (int j = 0; j < 5; ++j) { aq[j][0] = mfma32(wq[j], xA[0], aq[j][0]); aq[j][1] = mfma32(wq[j], xA[1], aq[j][1]); }
    {
      const u32 va = L0 + L_KV + (8 * hw + g) * LN_PITCH + c16 * 16 + ty * 768;
#pragma unroll
      for (int pr = 0; pr < 2; ++pr) {
        const u32 vp = va + (pr ^ ty) * (4 * LN_PITCH);
        *(LAS s16x8*)(vp + 16 * LN_PITCH) = cat(pk4(akv[3][2 * pr]), pk4(akv[3][2 * pr + 1]));
        *(LAS s16x8*)(vp + 16 * LN_PITCH + 256) = cat(pk4(akv[4][2 * pr]), pk4(akv[4][2 * pr + 1]));
        *(LAS s16x8*)(vp) = cat(phi4(akv[0][2 * pr]), phi4(akv[0][2 * pr + 1]));
        *(LAS s16x8*)(vp + 256) = cat(phi4(akv[1][2 * pr]), phi4(akv[1][2 * pr + 1]));
      }
      if (ty) {
#pragma unroll
        for (int pr = 0; pr < 2; ++pr) *(LAS s16x8*)(va + (pr ^ 1) * (4 * LN_PITCH) + 16 * LN_PITCH - 256) = cat(pk4(akv[2][2 * pr]), pk4(akv[2][2 * pr + 1]));
        *(LAS s16x8*)(L0 + L_KV + (16 + c16) * LN_PITCH + 1280) = s16x8{0x3f80, 0x3f80, 0x3f80, 0x3f80, 0x3f80, 0x3f80, 0x3f80, 0x3f80};
      } else {
#pragma unroll
        for (int pr = 0; pr < 2; ++pr) *(LAS s16x8*)(va + pr * (4 * LN_PITCH) + 512) = cat(phi4(akv[2][2 * pr]), phi4(akv[2][2 * pr + 1]));
      }
    }
    STAMP_AT(4);
    BAR();
    const int pb = WS_PART + (hg * 4 + wave) * (16 * 896);
    {
      const int c32 = lane & 31, hh = lane >> 5;
      const u32 ka = L0 + L_KV + hh * LN_PITCH + (20 * wave + (c32 < 20 ? c32 : 19)) * 16;
      const u32 vb = L0 + L_KV + (16 + hh) * LN_PITCH + (c32 < 20 ? 20 * wave + c32 : 80) * 16;
      f32x16 d = {};
#pragma unroll
      for (int i = 0; i < 8; ++i)
        d = __builtin_amdgcn_mfma_f32_32x32x16_bf16(__builtin_bit_cast(b16x8, ld128(ka + i * 2 * LN_PITCH)), __builtin_bit_cast(b16x8, ld128(vb + i * 2 * LN_PITCH)), d, 0, 0, 0);
      const u32x4 lo = {pk2(d[0], d[1]), pk2(d[2], d[3]), pk2(d[4], d[5]), pk2(d[6], d[7])};
      const u32x2 hi = {pk2(d[8], d[9]), pk2(d[10], d[11])};
      st128(rs, c32 <= 20 ? pb + rt * 896 + (hh * 21 + c32) * 16 : OOB, __builtin_bit_cast(s16x8, lo));
      st64(rs, lane <= 20 ? pb + rt * 896 + 672 + lane * 8 : OOB, __builtin_bit_cast(s16x4, hi));
    }
    BAR();
    s16x4 pq[5][2];
#pragma unroll
    for (int j = 0; j < 3; ++j) { pq[j][0] = phi4(aq[j][0]); pq[j][1] = phi4(aq[j][1]); asm volatile("" : "+v"(pq[j][0]), "+v"(pq[j][1])); }
    publish(ws + WS_KVFLAG + (hg * 4 + wave) * 64 + rt * 4, tag, lane);
    STAMP_AT(5);
#pragma unroll
    for (int j = 3; j < 5; ++j) { pq[j][0] = phi4(aq[j][0]); pq[j][1] = phi4(aq[j][1]); asm volatile("" : "+v"(pq[j][0]), "+v"(pq[j][1])); }
    u32 qa[5][3], za[5];
#pragma unroll
    for (int je = 0; je < 5; ++je) {
      const int e = 16 * je + c16, he = e / 20, el = e - 20 * he;
#pragma unroll
      for (int x = 0; x < 3; ++x) {
        const int dl = 4 * (je - 1 + x) + g - 5 * he;
        const int qi = dl < 4 ? ((dl & 1) * 21 + el) * 2 + (dl >> 1) : 84 + el;
        qa[je][x] = L0 + L_FIN + (dl >= 0 && dl < 5 ? he * 848 + qi * 8 : 3392);
        if (je - 1 + x >= 0 && je - 1 + x < 5) asm volatile("" : "+v"(qa[je][x]));
      }
      const int dl = 4 * je + g - 5 * p4;
      const int qi = dl < 4 ? ((dl & 1) * 21 + 20) * 2 + (dl >> 1) : 104;
      za[je] = L0 + L_FIN + (dl >= 0 && dl < 5 ? p4 * 848 + qi * 8 : 3392);
      asm volatile("" : "+v"(za[je]));
    }
    poll(ws + WS_KVFLAG + (hg * 4 + wave) * 64, c16, tag);
    STAMP_AT(6);
    {
      u32x4 pr[16];
#pragma unroll
      for (int i = 0; i < 16; ++i) pr[i] = __builtin_amdgcn_raw_buffer_load_b128(rs, lane < 53 ? pb + lane * 16 : OOB, i * 896, 16);
      f32x4 s0 = {0.f, 0.f, 0.f, 0.f}, s1 = s0;
#pragma unroll
      for (int i = 0; i < 16; ++i) {
        s0 += f32x4{blo(pr[i][0]), bhi(pr[i][0]), blo(pr[i][1]), bhi(pr[i][1])};
        s1 += f32x4{blo(pr[i][2]), bhi(pr[i][2]), blo(pr[i][3]), bhi(pr[i][3])};
      }
      *(LAS s16x8*)(L0 + L_FIN + (lane < 53 ? wave * 848 + lane * 16 : 3392)) = cat(pk4(s0), pk4(s1));
    }
    STAMP_AT(7);
    BAR();
    {
      s16x4 kq[5][3], zq[5];
#pragma unroll
      for (int je = 0; je < 5; ++je) {
        zq[je] = ld64(za[je]);
#pragma unroll
        for (int x = 0; x < 3; ++x)
          if (je - 1 + x >= 0 && je - 1 + x < 5) kq[je][x] = ld64(qa[je][x]);
      }
      BAR();
      const int ob = WS_O + (2 * rt + hw) * 81920 + hg * 10240 + ty * 1024 + lane * 16;
      s16x4 ot[5][2];
      const s16x4 zz = {0, 0, 0, 0};
#pragma unroll
      for (int i = 0; i < 2; ++i) {
        const s16x8 p01 = cat(pq[0][i], pq[1][i]), p23 = cat(pq[2][i], pq[3][i]), p34 = cat(pq[3][i], pq[4][i]);
        f32x4 den = {1e-6f, 1e-6f, 1e-6f, 1e-6f};
        den = mfma32(cat(zq[0], zq[1]), p01, den);
        den = mfma32(cat(zq[2], zq[3]), p23, den);
        den = mfma32(cat(zz, zq[4]), p34, den);
        const f32x4 z4 = {0.f, 0.f, 0.f, 0.f};
        f32x4 o[5];
        o[0] = mfma32(cat(kq[0][1], kq[0][2]), p01, z4);
        o[1] = mfma32(cat(kq[1][2], zz), p23, mfma32(cat(kq[1][0], kq[1][1]), p01, z4));
        o[2] = mfma32(cat(zz, kq[2][0]), p01, mfma32(cat(kq[2][1], kq[2][2]), p23, z4));
        o[3] = mfma32(cat(zz, kq[3][2]), p34, mfma32(cat(kq[3][0], kq[3][1]), p23, z4));
        o[4] = mfma32(cat(kq[4][0], kq[4][1]), p34, z4);
        const float r0 = __builtin_amdgcn_rcpf(den[0]), r1 = __builtin_amdgcn_rcpf(den[1]), r2 = __builtin_amdgcn_rcpf(den[2]), r3 = __builtin_amdgcn_rcpf(den[3]);
        o[0] *= r0;
        o[1] *= g < 1 ? r0 : r1;
        o[2] *= g < 2 ? r1 : r2;
        o[3] *= g < 3 ? r2 : r3;
        o[4] *= r3;
#pragma unroll
        for (int je = 0; je < 5; ++je) ot[je][i] = pk4(o[je]);
      }
#pragma unroll
      for (int je = 0; je < 5; ++je) st128(rs, ob + je * 2048, cat(ot[je][0], ot[je][1]));
    }
  } else if (roleB) {
    const bool live = ft < 13 || wave < 2;
    f32x4 acc[2][5];
#pragma unroll
    for (int t = 0; t < 2; ++t) {
      const u32x2 bb = *(const GAS u32x2*)(P.b1 + (live ? ft * 256 + wave * 64 : 0) + (u32)(t * 32 + g * 8));
      const f32x4 bi = {blo(bb[0]), bhi(bb[0]), blo(bb[1]), bhi(bb[1])};
#pragma unroll
      for (int j = 0; j < 5; ++j) acc[t][j] = bi;
    }
    const u32 aa0 = L0 + (8 * g + q4) * 256 + ((2 * wave) ^ (q4 | ((g & 1) << 2))) * 32 + p4 * 8;
    const u32 ha0 = L0 + L1_B + lane * 16;
    u32 hs = 0;
    s16x8 a1[2] = {}, h1[5] = {};
#pragma unroll 1
    for (int r = 0; r < 32; ++r) {
      const u32 aa = aa0 + (r & 7) * 16384, ab = aa ^ 32, ha = ha0 + hs;
      BAR();
      s16x8 a0[2], h0[5];
      a0[0] = cat(ldtr(aa), ldtr(aa + 1024)); a0[1] = cat(ldtr(ab), ldtr(ab + 1024));
#pragma unroll
      for (int j = 0; j < 5; ++j) h0[j] = ld128(ha + j * 1024);
#pragma unroll
      for (int j = 0; j < 5; ++j) { acc[0][j] = mfma32(a1[0], h1[j], acc[0][j]); acc[1][j] = mfma32(a1[1], h1[j], acc[1][j]); }
#pragma unroll
      for (int j = 0; j < 9; ++j) { SG(0x100, 1); SG(0x8, 1); }
      SB();
      a1[0] = cat(ldtr(aa + 8192), ldtr(aa + 9216)); a1[1] = cat(ldtr(ab + 8192), ldtr(ab + 9216));
#pragma unroll
      for (int j = 0; j < 5; ++j) h1[j] = ld128(ha + 5120 + j * 1024);
#pragma unroll
      for (int j = 0; j < 5; ++j) { acc[0][j] = mfma32(a0[0], h0[j], acc[0][j]); acc[1][j] = mfma32(a0[1], h0[j], acc[1][j]); }
#pragma unroll
      for (int j = 0; j < 4; ++j) { SG(0x100, 2); SG(0x8, 1); }
      SG(0x100, 1); SG(0x8, 6);
      SB();
      hs = hs == 20480 ? 0 : hs + 10240;
    }
    BAR();
    STAMP_AT(3);
#pragma unroll
    for (int j = 0; j < 5; ++j) { acc[0][j] = mfma32(a1[0], h1[j], acc[0][j]); acc[1][j] = mfma32(a1[1], h1[j], acc[1][j]); }
    if (live) {
      const int go = WS_G + dt * 276480 + (ft * 4 + wave) * 5120 + lane * 16;
#pragma unroll
      for (int j = 0; j < 5; ++j) st128(rs, go + j * 1024, cat(gelu4(acc[0][j]), gelu4(acc[1][j])));
      STAMP_AT(4);
      publish(ws + WS_GFLAG + dt * 256 + (ft * 4 + wave) * 4, tag, lane);
    }
  } else {
    BAR();
    const u32 ra = L0 + (4 * g + q4) * 320 + hw * 160 + p4 * 8, rb = ra + p4 * 8;
    const u32 a0 = rb + ty * 64, a1 = a0 + 8, a2 = ty ? ra + 128 : rb + 64;
    const int d8 = 2 * (sp & 3) + hw;
    const int go = WS_WO + d8 * 102400 + (sp >> 2) * 25600 + ty * 2048 + lane * 16;
#pragma unroll 1
    for (int k = 0; k < 5; ++k) {
      const u32 o = k * 10240;
      const s16x8 f0 = cat(ldtr(a0 + o), ldtr(a0 + o + 5120)), f1 = cat(ldtr(a1 + o), ldtr(a1 + o + 5120)), f2 = cat(ldtr(a2 + o), ldtr(a2 + o + 5120));
      st128(rs, go + k * 5120, f0);
      st128(rs, go + k * 5120 + 1024, f1);
      st128(rs, go + k * 5120 + 2048, f2);
      asm volatile("s_nop 1" ::: "memory");
    }
    publish(ws + WS_GFLAG + d8 * 256 + (54 + (sp >> 2) * 2 + ty) * 4, tag, lane);
    BAR();
    if (sp == 0 && wave == 0) {
      poll512(rs, WS_HFLAG + lane * 16, tag, false);
      poll512(rs, WS_GFLAG + lane * 16, tag, c16 == 15);
      if (lane == 0) __hip_atomic_store((GAS u32*)(ws + WS_EPOCH), tag, RLX_AGENT);
    }
  }

  STAMP_AT(8);
  {
    const int srow = st * 64 + wave * 16 + c16;
    const u32 xo = srow * 1280 + dt3 * 160 + g * 16;
    const u32x4 x01 = *(const GAS u32x4*)(P.x + xo), x23 = *(const GAS u32x4*)(P.x + xo + 64);
    const u32x2 x4 = *(const GAS u32x2*)(P.x + (xo + 128 - g * 8));
    const u32 bs = *(const GAS unsigned short*)(P.b2 + (u32)(srow * 2));
    f32x4 acc[2][5] = {};
    const u32 aa0 = L0 + (32 * ty + 4 * g + q4) * 128 + (wave ^ ((2 * g + (q4 >> 1)) & 3)) * 32 + p4 * 8;
    const u32 ga0 = L0 + L3_B + ty * 5120 + lane * 16;
    const u32 oa0 = L0 + ty * 4096 + hw * 1024 + lane * 16 + ty * 8;
    u32 sa = 0, sb = 0;
    struct FA { s16x8 a[2], g[5]; };
    auto lda = [&](FA& f, bool tr) {
      const u32 aa = (tr ? aa0 : oa0) + sa, ab = aa ^ (tr ? 32 : 8), ga = ga0 + sb;
      sa = sa == 14 * 8192 ? 0 : sa + 8192;
      sb = sb == 30720 ? 0 : sb + 10240;
      BAR();
      if (tr) { f.a[1] = cat(ldtr(ab), ldtr(ab + 2048)); f.a[0] = cat(ldtr(aa), ldtr(aa + 2048)); }
      else { f.a[1] = cat(ld64(ab), ld64(ab + 2048)); f.a[0] = cat(ld64(aa), ld64(aa + 2048)); }
#pragma unroll
      for (int j = 0; j < 5; ++j) f.g[j] = ld128(ga + j * 1024);
    };
    auto mma = [&](const FA& f) {
#pragma unroll
      for (int j = 0; j < 5; ++j) acc[1][j] = mfma32(f.g[j], f.a[1], acc[1][j]);
#pragma unroll
      for (int j = 0; j < 5; ++j) acc[0][j] = mfma32(f.g[j], f.a[0], acc[0][j]);
    };
    auto pat = [&]() {
#pragma unroll
      for (int j = 0; j < 5; ++j) { SG(0x100, 2); SG(0x8, 1); }
      SG(0x8, 5);
      SB();
    };
    FA f0, f1;
    lda(f0, true); SB();
    {
      const float bf = blo(bs);
      acc[0][0] = f32x4{blo(x01[0]), bhi(x01[0]), blo(x01[1]), bhi(x01[1])} + bf;
      acc[0][1] = f32x4{blo(x01[2]), bhi(x01[2]), blo(x01[3]), bhi(x01[3])} + bf;
      acc[0][2] = f32x4{blo(x23[0]), bhi(x23[0]), blo(x23[1]), bhi(x23[1])} + bf;
      acc[0][3] = f32x4{blo(x23[2]), bhi(x23[2]), blo(x23[3]), bhi(x23[3])} + bf;
      acc[0][4] = f32x4{blo(x4[0]), bhi(x4[0]), blo(x4[1]), bhi(x4[1])} + bf;
      if (roleA) publish(ws + WS_OFLAG + (2 * rt + hw) * 64 + ty * 32 + hg * 4, tag, lane);
      SB();
    }
#pragma unroll 1
    for (int r = 1; r < 27; r += 2) { lda(f1, true); mma(f0); pat(); lda(f0, true); mma(f1); pat(); }
    STAMP_AT(9);
#pragma unroll 1
    for (int r = 27; r < 37; r += 2) { lda(f1, false); mma(f0); pat(); lda(f0, false); mma(f1); pat(); }
    mma(f0);
    const u32 ea = L0 + L3_B + 20480 + lane * 16;
#pragma unroll
    for (int j = 0; j < 5; ++j) *(LAS f32x4*)(ea + wave * 5120 + j * 1024) = acc[1][j];
    SG(0x8, 8); SG(0x200, 2); SG(0x8, 2); SG(0x200, 3);
    SB();
    BAR();
#pragma unroll
    for (int j = 0; j < 5; ++j) acc[0][j] += *(const LAS f32x4*)(ea + (wave ^ 1) * 5120 + j * 1024);
    *(GAS s16x8*)(P.out + xo) = cat(pk4(acc[0][0]), pk4(acc[0][1]));
    *(GAS s16x8*)(P.out + xo + 64) = cat(pk4(acc[0][2]), pk4(acc[0][3]));
    *(GAS s16x4*)(P.out + (xo + 128 - g * 8)) = pk4(acc[0][4]);
  }
}

extern "C" void kernel_launch(void* const* d_in, const int* in_sizes, int n_in, void* d_out, int out_size, void* d_ws,
                              size_t ws_size, hipStream_t stream) {
  Params p;
  p.x = (const char*)d_in[0];
  p.wqkv = (const char*)d_in[1];
  p.wo = (const char*)d_in[2];
  p.w1 = (const char*)d_in[4];
  p.b1 = (const char*)d_in[5];
  p.w2 = (const char*)d_in[6];
  p.b2 = (const char*)d_in[7];
  p.out = (char*)d_out;
  p.ws = (char*)d_ws;
  (void)hipFuncSetAttribute((const void*)fused_fwd, hipFuncAttributeMaxDynamicSharedMemorySize, L_BYTES);
  fused_fwd<<<256, 512, L_BYTES, stream>>>(p);
}
```

```cpp
#include <hip/hip_runtime.h>
#include <hip/hip_bf16.h>
#pragma clang diagnostic ignored "-Wint-to-pointer-cast"

#define GAS __attribute__((address_space(1)))
#define LAS __attribute__((address_space(3)))
#define DI __device__ __forceinline__
typedef unsigned u32;
typedef unsigned long long u64;
typedef short s16x4 __attribute__((ext_vector_type(4)));
typedef short s16x8 __attribute__((ext_vector_type(8)));
typedef __bf16 b16x2 __attribute__((ext_vector_type(2)));
typedef __bf16 b16x8 __attribute__((ext_vector_type(8)));
typedef float f32x2 __attribute__((ext_vector_type(2)));
typedef float f32x4 __attribute__((ext_vector_type(4)));
typedef float f32x16 __attribute__((ext_vector_type(16)));
typedef unsigned u32x2 __attribute__((ext_vector_type(2)));
typedef unsigned u32x4 __attribute__((ext_vector_type(4)));

constexpr int WS_EPOCH = 0;
constexpr int WS_LNFLAG = 196608;
constexpr int WS_GFLAG = 32768;
constexpr int WS_KVFLAG = 65536;
constexpr int WS_OFLAG = 131072;
constexpr int WS_PART = 262144;
constexpr int WS_XN = 786432;
constexpr int WS_H = WS_XN + 2621440;
constexpr int WS_G = WS_H + 2621440;
constexpr int WS_O = WS_G + 2211840;
constexpr int WS_WO = WS_O + 2621440;
constexpr int WS_END = WS_WO + 819200;
constexpr int OOB = 0x40000000;
constexpr int L_BYTES = 163840;
constexpr int L_LN = 131072, LN_PITCH = 1296;
#define NB3 5
constexpr int LA3 = NB3 - 1, NA3 = 18 - NB3, H3 = 2 * (NA3 - LA3 - 2);
constexpr int LQ_B = 122880, L1_B = 114688, L3_B = NA3 * 8192;
constexpr int L_KV = L3_B, L_FIN = L_KV + 32 * LN_PITCH;
constexpr int a3(int t) { return t < 1 || t > 35 - LA3 ? 0 : t <= H3 ? 4 : 8; }
constexpr int w3(int r) { int n = a3(r - LA3); for (int t = r - LA3 + 1; t < r; ++t) n += a3(t) + (t <= 36 - LA3 ? 10 : 0); return n; }
#define NP3 5
#define PRE_A 4
#define PRE_B 6
#define NP_A 16
#define TH_A0 16
#define TH_B0 36
#define TH_P3 8
#define SLP 2
constexpr u32 SPIN_CAP = 1u << 16;

#define BAR() do { asm volatile("" ::: "memory"); __builtin_amdgcn_s_waitcnt(0xC07F); asm volatile("s_barrier" ::: "memory"); } while (0)
#define WAITV(n) asm volatile("s_waitcnt vmcnt(" #n ")" ::: "memory")
#define WAITX(n) asm volatile("s_waitcnt vmcnt(%0)" ::"i"((n) > 63 ? 63 : (n)) : "memory")
#define STAMP_AT(k)
#define STAMP_DECL
#define ACC_TO(k)
#define STAMP_OUT()
#define ACC1(k)
#define ACC3(k)
#define LW1(n) do { ACC1(13); WAITX(n); ACC1(11); } while (0)
#define LB1() do { BAR(); ACC1(12); } while (0)
#define LW3(n) do { ACC3(13); WAITX(n); ACC3(11); } while (0)
#define LB3() do { BAR(); ACC3(12); } while (0)
#define RLX_AGENT __ATOMIC_RELAXED, __HIP_MEMORY_SCOPE_AGENT

DI f32x4 mfma32(s16x8 a, s16x8 b, f32x4 c) {
  return __builtin_amdgcn_mfma_f32_16x16x32_bf16(__builtin_bit_cast(b16x8, a), __builtin_bit_cast(b16x8, b), c, 0, 0, 0);
}
DI s16x4 ldtr(u32 a) { return __builtin_amdgcn_ds_read_tr16_b64_v4i16((LAS s16x4*)a); }
DI s16x8 ld128(u32 a) { return *(const LAS s16x8*)a; }
DI s16x4 ld64(u32 a) { return *(const LAS s16x4*)a; }
DI s16x8 cat(s16x4 a, s16x4 b) { return __builtin_shufflevector(a, b, 0, 1, 2, 3, 4, 5, 6, 7); }
DI u32 pk2(float a, float b) { f32x2 v = {a, b}; return __builtin_bit_cast(u32, __builtin_convertvector(v, b16x2)); }
DI s16x4 pk4(f32x4 v) { u32x2 r = {pk2(v[0], v[1]), pk2(v[2], v[3])}; return __builtin_bit_cast(s16x4, r); }
DI float blo(u32 u) { return __uint_as_float(u << 16); }
DI float bhi(u32 u) { return __uint_as_float(u & 0xffff0000u); }
template <int C> DI float dpp_add(float s) { return s + __int_as_float(__builtin_amdgcn_update_dpp(0, __float_as_int(s), C, 0xf, 0xf, true)); }
DI float sum32(float s) {
  s = dpp_add<0x121>(s); s = dpp_add<0x122>(s); s = dpp_add<0x124>(s); s = dpp_add<0x128>(s);
  auto r = __builtin_amdgcn_permlane16_swap(__float_as_uint(s), __float_as_uint(s), false, false);
  return __uint_as_float(r[0]) + __uint_as_float(r[1]);
}
DI u32 phi2(float a0, float a1) {
  const float L = 1.4426950408889634f;
  const f32x2 a = {a0, a1};
  const f32x2 y = __builtin_elementwise_fma(a, f32x2{L, L}, f32x2{-L, -L});
  const float e0 = __builtin_amdgcn_fmed3f(__builtin_amdgcn_exp2f(y[0]), 0.f, 1.f), e1 = __builtin_amdgcn_fmed3f(__builtin_amdgcn_exp2f(y[1]), 0.f, 1.f);
  f32x2 r = {__int_as_float(max(__float_as_int(a0), __float_as_int(e0))), __int_as_float(max(__float_as_int(a1), __float_as_int(e1)))};
  r += 1e-6f;
  return pk2(r[0], r[1]);
}
DI s16x4 phi4(f32x4 a) { u32x2 r = {phi2(a[0], a[1]), phi2(a[2], a[3])}; return __builtin_bit_cast(s16x4, r); }
DI u32 gelu2(float u0, float u1) {
  const float K = -2.f * 0.7978845608028654f * 1.4426950408889634f, K3 = K * 0.044715f;
  const f32x2 u = {u0, u1};
  const f32x2 y = u * __builtin_elementwise_fma(u * u, f32x2{K3, K3}, f32x2{K, K});
  f32x2 d = {__builtin_amdgcn_exp2f(y[0]), __builtin_amdgcn_exp2f(y[1])};
  d += 1.f;
  const f32x2 r = u * f32x2{__builtin_amdgcn_rcpf(d[0]), __builtin_amdgcn_rcpf(d[1])};
  return pk2(r[0], r[1]);
}
DI s16x4 gelu4(f32x4 a) { u32x2 r = {gelu2(a[0], a[1]), gelu2(a[2], a[3])}; return __builtin_bit_cast(s16x4, r); }
DI void st128(__amdgpu_buffer_rsrc_t rs, int off, s16x8 v) { __builtin_amdgcn_raw_buffer_store_b128(__builtin_bit_cast(u32x4, v), rs, off, 0, 16); }
DI void st64(__amdgpu_buffer_rsrc_t rs, int off, s16x4 v) { __builtin_amdgcn_raw_buffer_store_b64(__builtin_bit_cast(u32x2, v), rs, off, 0, 16); }
#define SG(m, n) __builtin_amdgcn_sched_group_barrier(m, n, 0)
#define SB() __builtin_amdgcn_sched_barrier(0)
DI u32 mix(u32 e) { return e * 0x9E3779B1u + 0x7F4A7C15u; }

#define GL(v, off, sc) "global_load_lds_dwordx4 " v ", %0 offset:" #off sc "\n\t"
DI void gl4(const char* base, u32 m, u32 v0, u32 v1, u32 v2, u32 v3) {
  asm volatile("s_mov_b32 m0, %1\n\ts_nop 0\n\t" GL("%2", 0, "") GL("%3", 1024, "") GL("%4", 2048, "") GL("%5", 3072, "")
               ::"s"(base), "s"(m), "v"(v0), "v"(v1), "v"(v2), "v"(v3) : "memory");
}
DI void gl4w(const char* base, u32 m, u32 v0, u32 v1, u32 v2, u32 v3) {
  asm volatile("s_mov_b32 m0, %1\n\ts_mov_b32 exec_hi, 0x0fffffff\n\t" GL("%2", 0, "") GL("%3", 960, "") GL("%4", 1920, "") GL("%5", 2880, "")
               "s_mov_b32 exec_hi, -1"
               ::"s"(base), "s"(m), "v"(v0), "v"(v1), "v"(v2), "v"(v3) : "memory");
}
DI void gl1(const char* base, u32 m, u32 v0) {
  asm volatile("s_mov_b32 m0, %1\n\ts_nop 0\n\t" GL("%2", 0, "")::"s"(base), "s"(m), "v"(v0) : "memory");
}
DI void glin8(const char* base, u32 m, u32 vl, u32 vl4) {
  asm volatile("s_mov_b32 m0, %1\n\ts_nop 0\n\t" GL("%2", 0, " sc1") GL("%2", 1024, " sc1") GL("%2", 2048, " sc1") GL("%2", 3072, " sc1")
               "s_add_u32 m0, m0, 4096\n\ts_nop 0\n\t" GL("%3", 0, " sc1") GL("%3", 1024, " sc1") GL("%3", 2048, " sc1") GL("%3", 3072, " sc1")
               ::"s"(base), "s"(m), "v"(vl), "v"(vl4) : "memory", "scc");
}
DI void glin10(const char* base, u32 m, u32 vl, u32 vl4, u32 vl8) {
  asm volatile("s_mov_b32 m0, %1\n\ts_nop 0\n\t" GL("%2", 0, " sc1") GL("%2", 1024, " sc1") GL("%2", 2048, " sc1") GL("%2", 3072, " sc1")
               "s_add_u32 m0, m0, 4096\n\ts_nop 0\n\t" GL("%3", 0, " sc1") GL("%3", 1024, " sc1") GL("%3", 2048, " sc1") GL("%3", 3072, " sc1")
               "s_add_u32 m0, m0, 4096\n\ts_nop 0\n\t" GL("%4", 0, " sc1") GL("%4", 1024, " sc1")
               ::"s"(base), "s"(m), "v"(vl), "v"(vl4), "v"(vl8) : "memory", "scc");
}
DI void touch(const char* base, u32 m, u32 v) {
  asm volatile("s_mov_b32 m0, %1\n\ts_nop 0\n\tglobal_load_lds_dword %2, %0" ::"s"(base), "s"(m), "v"(v) : "memory");
}
DI void poll(const char* f, int idx, u32 tag) {
  const GAS u32* p = (const GAS u32*)(f + (u32)(idx * 128));
#pragma unroll 1
  for (u32 sp = 0; sp < SPIN_CAP; ++sp) {
    u32 v = __hip_atomic_load(p, RLX_AGENT);
    if (__all(v == tag)) break;
    __builtin_amdgcn_s_sleep(SLP);
  }
  asm volatile("" ::: "memory");
}
DI u32 peek(const char* f, int idx) {
  u32 v;
  asm volatile("global_load_dword %0, %1, %2 sc1" : "=&v"(v) : "v"(idx * 128), "s"(f) : "memory");
  return v;
}
DI void publish(char* f, u32 tag, int lane) {
  WAITV(0);
  if (lane == 0) __hip_atomic_store((GAS u32*)f, tag, RLX_AGENT);
}
DI void publish_last(char* f, u32 tag, int lane, u32 cnt, u32 n) {
  WAITV(0);
  if (lane == 0 && __hip_atomic_fetch_add((LAS u32*)cnt, 1u, __ATOMIC_RELAXED, __HIP_MEMORY_SCOPE_WORKGROUP) == n - 1)
    __hip_atomic_store((GAS u32*)f, tag, RLX_AGENT);
}

struct Params {
  const char *x, *wqkv, *wo, *w1, *b1, *w2, *b2;
  char *out, *ws;
};
#define CODE_LINES 230

#define KEEP_RAW() asm volatile("" ::"v"(raw[0]), "v"(raw[1]), "v"(raw[2]), "v"(raw[3]), "v"(raw[4]))
__global__ __launch_bounds__(320) void fused_fwd(Params P) {
  extern __shared__ __attribute__((aligned(16))) char lds_[];
  const int tid = threadIdx.x, lane = tid & 63;
  const int wave = __builtin_amdgcn_readfirstlane(tid >> 6);
  const int b = blockIdx.x, xc = b & 7, jb = b >> 3;
  char* const ws = P.ws;
  const u32 L0 = (u32)(size_t)(LAS char*)lds_;
  const u32 LDUM = L0 + L_BYTES - 256, LCNT = L0 + L_BYTES - 512;
  const int c16 = lane & 15, g = lane >> 4, q4 = c16 >> 2, p4 = lane & 3;
  STAMP_DECL;
  STAMP_AT(0);
  {
    u64 pc;
    asm volatile("s_getpc_b64 %0" : "=s"(pc));
    touch((const char*)(pc & ~255ull), LDUM, min((jb * 5 + wave) * 2 + (lane & 1), CODE_LINES - 1) * 128);
  }
  const int row = 2 * (wave & 3) + (lane >> 5), hl = lane & 31;
  u32x2 raw[5];
  asm volatile("global_load_dwordx2 %0, %5, %6\n\tglobal_load_dwordx2 %1, %5, %6 offset:256\n\tglobal_load_dwordx2 %2, %5, %6 offset:512\n\t"
               "global_load_dwordx2 %3, %5, %6 offset:768\n\tglobal_load_dwordx2 %4, %5, %6 offset:1024"
               : "=&v"(raw[0]), "=&v"(raw[1]), "=&v"(raw[2]), "=&v"(raw[3]), "=&v"(raw[4])
               : "v"(row * 1280 + hl * 8), "s"(P.x + b * (8 * 1280)) : "memory");
  u32 e0 = *(const __attribute__((address_space(4))) u32*)(ws + WS_EPOCH);
  const bool roleA = b < 128;
  const int ib = jb - 16;
  const bool roleB = !roleA && (xc < 6 || ib < 8);
  const int hg = 2 * (xc & 3) + (jb & 1), rt = 8 * (xc >> 2) + (jb >> 1);
  const int ft = xc < 6 ? 2 * xc + (ib >> 3) : 6 + xc, dt = ib & 7;
  const int sp = (xc - 6) * 8 + ib - 8;
  const int st = 4 * xc + (jb & 3), dt3 = jb >> 2;

  if (__builtin_expect(wave == 4, 1)) {
    __builtin_amdgcn_s_setprio(3);
    const u32 vl = lane * 16, vl4 = vl + 4096, vl8 = vl + 8192;
    if (roleB) {
      u32 v[4];
#pragma unroll
      for (int i = 0; i < 4; ++i) {
        int f = g | (((i >> 1) & 1) << 2);
        int ch = c16 ^ (f << 1);
        if (ft == 13) ch &= 7;
        v[i] = (4 * i + g) * 3456 + ch * 16 - i * 1024;
      }
      const char* wb = P.w1 + ft * 256;
      const char* hb = ws + WS_H + dt * 327680;
      u32 oa = dt * (256 * 3456), gs = 0, ob = dt * 40960;
      u32 sb = L0 + L1_B;
      auto agroup = [&]() {
        gl4(wb + oa, L0 + gs, v[0], v[1], v[2], v[3]);
        oa = oa == 127 * 55296 ? 0 : oa + 55296;
        gs = gs == 27 * 4096 ? 0 : gs + 4096;
      };
      auto bunit = [&]() {
        glin10(hb + ob, sb, vl, vl4, vl8);
        ob = ob == 31 * 10240 ? 0 : ob + 10240;
        sb = sb == L0 + L1_B + 3 * 10240 ? L0 + L1_B : sb + 10240;
      };
      STAMP_AT(1);
#pragma unroll 1
      for (int i = 0; i < PRE_B; i += 2) { agroup(); agroup(); }
      asm volatile("" : "+s"(e0));
      BAR(); BAR(); STAMP_AT(14);
#pragma unroll 1
      for (int i = PRE_B; i < 28; i += 2) { agroup(); WAITX(TH_B0); agroup(); WAITX(TH_B0); }
      KEEP_RAW();
      STAMP_AT(2);
      const u32 tg = mix(e0);
      poll(ws + WS_LNFLAG, dt * 32 + (lane & 31), tg);
      STAMP_AT(3); ACC1(-1);
      bunit();
      u32 f0 = peek(ws + WS_LNFLAG, lane), f1 = peek(ws + WS_LNFLAG + 8192, lane), f2 = peek(ws + WS_LNFLAG + 16384, lane), f3 = peek(ws + WS_LNFLAG + 24576, lane);
      bunit(); bunit();
      LW1(24); LB1(); bunit();
      LW1(20);
      asm volatile("" : "+v"(f0), "+v"(f1), "+v"(f2), "+v"(f3));
      if (!__all(f0 == tg && f1 == tg && f2 == tg && f3 == tg)) {
#pragma unroll 1
        for (int i = 0; i < 4; ++i) poll(ws + WS_LNFLAG, i * 64 + lane, tg);
      }
      if (b == 128 && lane == 0) __hip_atomic_store((GAS u32*)(ws + WS_EPOCH), tg, RLX_AGENT);
#pragma unroll 1
      for (int r = 1; r < 5; ++r) { LB1(); agroup(); agroup(); agroup(); agroup(); bunit(); LW1(36); }
#pragma unroll 1
      for (int r = 5; r < 29; r += 2) {
        LB1(); agroup(); agroup(); agroup(); agroup(); bunit();
        LW1(48); LB1(); agroup(); agroup(); agroup(); bunit();
        LW1(48);
      }
      LB1(); LW1(22); LB1(); LW1(0); LB1();
      BAR();
    } else if (roleA) {
      const u32 hi = lane >= 30, cc = lane - 30 * hi, sg = (cc >= 10) + (cc >= 20);
      const u32 v0 = hi * 3840 + sg * 1120 + cc * 16, v1 = v0 + 2 * 3840 - 960, v2 = v0 + 8 * 3840 - 1920, v3 = v0 + 10 * 3840 - 2880;
      const char* wb = P.wqkv + hg * 160;
      const char* xb = ws + WS_XN + rt * 163840;
      const u32 rot = (5 * (2 * (rt & 7) + (rt >> 3))) >> 2;
      u32 oa = rot * 122880, ob = rot * 8192, st4 = 4 * 3840;
      u32 sa = L0, sb = L0 + LQ_B;
      auto agroup = [&]() {
        gl4w(wb + oa, sa, v0, v1, v2, v3);
        oa += st4; st4 ^= (4 * 3840) ^ (12 * 3840);
        oa = oa == 640 * 3840 ? 0 : oa;
        sa = sa == L0 + 31 * 3840 ? L0 : sa + 3840;
      };
      auto bunit = [&]() {
        glin8(xb + ob, sb, vl, vl4);
        ob = ob == 19 * 8192 ? 0 : ob + 8192;
        sb = sb == L0 + LQ_B + 3 * 8192 ? L0 + LQ_B : sb + 8192;
      };
      STAMP_AT(1);
#pragma unroll 1
      for (int i = 0; i < PRE_A; i += 2) { agroup(); agroup(); }
      asm volatile("" : "+s"(e0));
      BAR(); BAR(); STAMP_AT(14);
#pragma unroll 1
      for (int i = PRE_A; i < NP_A; i += 2) { agroup(); WAITX(TH_A0); agroup(); WAITX(TH_A0); }
      KEEP_RAW();
      STAMP_AT(2);
      poll(ws + WS_LNFLAG, rt * 16 + c16, mix(e0));
      STAMP_AT(3); ACC1(-1);
      bunit(); bunit(); bunit();
      LW1(16);
#pragma unroll 1
      for (int r = 0; r < 16; r += 2) {
        LB1(); agroup(); agroup(); agroup(); agroup(); bunit();
        LW1(32); LB1(); agroup(); agroup(); agroup(); if (NP_A == 16) agroup(); bunit();
        LW1(38);
      }
      LB1(); bunit();
      LW1(28); LB1();
      {
        const char* tb = P.w2 + xc * 512 + jb * (108 * 4096);
        const u32 tv = (lane >> 2) * 4096 + p4 * 128;
#pragma unroll 1
        for (int i = 0; i < 7; ++i) touch(tb + (i == 6 ? 92 : 16 * i) * 4096, LDUM, tv);
      }
      LW1(15); LB1(); LW1(7); LB1();
      BAR();
    } else {
      asm volatile("" : "+s"(e0));
      WAITV(0);
      BAR(); BAR();
      KEEP_RAW();
      const char* wb = P.wo + (sp >> 2) * (160 * 1280) + (sp & 3) * 320;
#pragma unroll 1
      for (int i = 0; i < 50; ++i) {
        const u32 n = 64 * i + lane, rw = n / 20u;
        gl1(wb, L0 + i * 1024, rw * 960 + n * 16);
        WAITV(16);
      }
      WAITV(0);
      BAR();
      BAR();
    }
    const u32 tag = mix(e0);
    STAMP_AT(4);
    {
      u32 v[4];
#pragma unroll
      for (int i = 0; i < 4; ++i) v[i] = (8 * i + (lane >> 3)) * 4096 + ((lane & 7) ^ (g << 1)) * 16 - i * 1024;
      const char* wb = P.w2 + st * 128;
      const char* bb = ws + WS_G + dt3 * 276480;
      const u32 rot = (27 * dt3) >> 3;
      u32 oa = rot * 262144, ob = rot * 10240, obl = 26 * 10240;
      u32 sa = L0, sb = L0 + L3_B;
      auto ahalf = [&]() {
        gl4(wb + oa, sa, v[0], v[1], v[2], v[3]);
        oa = oa == 53 * 131072 ? 0 : oa + 131072;
        sa = sa == L0 + L3_B - 4096 ? L0 : sa + 4096;
      };
      auto bunit = [&]() {
        glin10(bb + ob, sb, vl, vl4, vl8);
        ob = ob == obl ? 0 : ob + 10240;
        sb = sb == L0 + L3_B + LA3 * 10240 ? L0 + L3_B : sb + 10240;
      };
      if (roleA) { BAR(); BAR(); }
#pragma unroll 1
      for (int i = 0; i < NP3; ++i) { ahalf(); WAITX(TH_P3); ahalf(); WAITX(TH_P3); }
      if (roleB) BAR();
      STAMP_AT(15);
#pragma unroll 1
      for (int i = NP3; i < NA3; ++i) { ahalf(); ahalf(); WAITV(40); if (roleA) WAITX(TH_P3); }
      STAMP_AT(5);
      if (roleA) { BAR(); BAR(); }
      STAMP_AT(6);
      poll(ws + WS_GFLAG + dt3 * 4096, lane < 22 ? lane : 0, tag);
      STAMP_AT(7); ACC3(-1);
#pragma unroll 1
      for (int i = 0; i < LA3; ++i) bunit();
      LW3(w3(0)); LB3(); bunit();
      LW3(w3(1));
#pragma unroll 1
      for (int r = 1; r < LA3; ++r) { LB3(); bunit(); ahalf(); LW3(w3(2)); }
#pragma unroll 1
      for (int r = LA3; r <= H3; ++r) { LB3(); bunit(); ahalf(); LW3(w3(LA3 + 1)); }
      u32 of = 0;
#pragma unroll 1
      for (int r = H3 + 1; r < 26 - LA3; ++r) {
        if (r == 21 - LA3) of = peek(ws + WS_OFLAG + st * 2048, c16);
        LB3(); bunit(); ahalf(); ahalf(); LW3(w3(H3 + 2));
      }
      STAMP_AT(8);
      asm volatile("" : "+v"(of));
      if (!__all(of == tag)) poll(ws + WS_OFLAG + st * 2048, c16, tag);
      STAMP_AT(9);
      const char* ab = ws + WS_O + st * 81920;
      oa = dt3 * 8192;
#pragma unroll 1
      for (int r = 26 - LA3; r < 36 - LA3; ++r) {
        LB3(); bunit();
        if (r == 26 - LA3) { bb = ws + WS_WO + dt3 * 102400; ob = dt3 * 10240; obl = 9 * 10240; }
        glin8(ab + oa, sa, vl, vl4);
        oa = oa == 9 * 8192 ? 0 : oa + 8192;
        sa = sa == L0 + L3_B - 8192 ? L0 : sa + 8192;
        LW3(w3(27 - LA3));
      }
      LB3(); bunit();
      if (LA3 > 4) { LW3(w3(32)); LB3(); }
      if (LA3 > 3) { LW3(w3(33)); LB3(); }
      LW3(w3(34)); LB3();
      LW3(w3(35)); LB3();
      LW3(0); LB3();
    }
    STAMP_AT(10);
    STAMP_OUT();
    return;
  }

  const int hw = wave >> 1, ty = wave & 1;
  const __amdgpu_buffer_rsrc_t rs = __builtin_amdgcn_make_buffer_rsrc(ws, 0, WS_END, 0x00020000);
  {
    u32 ra, rb; int go, gstep;
    if (hw == 0) {
      const int ch0 = 40 * wave + (lane >> 3);
      ra = rb = L0 + L_LN + (lane & 7) * LN_PITCH + ch0 * 16;
      go = WS_XN + (b >> 4) * 163840 + ((b >> 1) & 7) * 1024 + (b & 1) * 128 + (ch0 >> 2) * 8192 + (ch0 & 3) * 256 + (lane & 7) * 16;
      gstep = 16384;
    } else {
      const int dl = 4 * ty + g;
      ra = L0 + L_LN + (8 + q4) * LN_PITCH + dl * 160 + p4 * 8; rb = ra + p4 * 8;
      go = WS_H + (b >> 2) * 5120 + (b & 3) * 256 + dl * 327680 + c16 * 16;
      gstep = 1024;
    }
    int g1 = go + gstep, g2 = go + 2 * gstep, g3 = go + 3 * gstep, g4 = go + 4 * gstep;
    u32 wa = L0 + L_LN + row * LN_PITCH + hl * 8;
    *(LAS u32*)LCNT = 0;
    asm volatile("" : "+v"(ra), "+v"(rb), "+v"(go), "+v"(g1), "+v"(g2), "+v"(g3), "+v"(g4), "+v"(wa), "+s"(e0));
    f32x2 v[10];
    {
      float u[20];
#define UNP(n, o, i) "s_waitcnt vmcnt(" #n ")\n\tv_lshlrev_b32 %0, 16, %4\n\tv_and_b32 %1, 0xffff0000, %4\n\tv_lshlrev_b32 %2, 16, %5\n\tv_and_b32 %3, 0xffff0000, %5" \
      : "=&v"(u[o]), "=&v"(u[o + 1]), "=&v"(u[o + 2]), "=&v"(u[o + 3]) : "v"(raw[i][0]), "v"(raw[i][1]) : "memory"
      asm volatile(UNP(4, 0, 0)); asm volatile(UNP(3, 4, 1)); asm volatile(UNP(2, 8, 2)); asm volatile(UNP(1, 12, 3)); asm volatile(UNP(0, 16, 4));
#pragma unroll
      for (int i = 0; i < 10; ++i) v[i] = f32x2{u[2 * i], u[2 * i + 1]};
    }
    f32x2 s = v[0] + v[1], s2 = v[2] + v[3];
#pragma unroll
    for (int i = 4; i < 10; i += 2) { s += v[i]; s2 += v[i + 1]; }
    s += s2;
    const float m = sum32(s[0] + s[1]) * (1.f / 640.f);
#pragma unroll
    for (int i = 0; i < 10; ++i) v[i] -= m;
    f32x2 qq = v[0] * v[0], q2 = v[1] * v[1];
#pragma unroll
    for (int i = 2; i < 10; i += 2) { qq += v[i] * v[i]; q2 += v[i + 1] * v[i + 1]; }
    qq += q2;
    const float r1 = __builtin_amdgcn_rsqf(sum32(qq[0] + qq[1]) * (1.f / 640.f) + 1e-5f);
#pragma unroll
    for (int i = 0; i < 10; ++i) { v[i] *= r1; asm("" : "+v"(v[i])); }
    s = v[0]; qq = v[0] * v[0];
#pragma unroll
    for (int i = 1; i < 10; ++i) { s += v[i]; qq += v[i] * v[i]; }
#pragma unroll
    for (int i = 0; i < 5; ++i) *(LAS u32x2*)(wa + i * 256) = u32x2{pk2(v[2 * i][0], v[2 * i][1]), pk2(v[2 * i + 1][0], v[2 * i + 1][1])};
    const float m2 = sum32(s[0] + s[1]) * (1.f / 640.f);
    const float r2 = __builtin_amdgcn_rsqf(sum32(qq[0] + qq[1]) * (1.f / 640.f) - m2 * m2 + 1e-5f);
    const float mr = -m2 * r2;
#pragma unroll
    for (int i = 0; i < 10; ++i) v[i] = __builtin_elementwise_fma(v[i], f32x2{r2, r2}, f32x2{mr, mr});
#pragma unroll
    for (int i = 0; i < 5; ++i) *(LAS u32x2*)(wa + 8 * LN_PITCH + i * 256) = u32x2{pk2(v[2 * i][0], v[2 * i][1]), pk2(v[2 * i + 1][0], v[2 * i + 1][1])};
    STAMP_AT(1);
    BAR();
    s16x8 f[5];
    if (hw == 0) {
#pragma unroll
      for (int i = 0; i < 5; ++i) f[i] = ld128(ra + i * 128);
    } else {
#pragma unroll
      for (int j = 0; j < 4; ++j) f[j] = cat(ldtr(rb + (j >> 1) * 64 + (j & 1) * 8), ldtr(rb + (j >> 1) * 64 + (j & 1) * 8 + 4 * LN_PITCH));
      f[4] = cat(ldtr(ra + 128), ldtr(ra + 128 + 4 * LN_PITCH));
    }
    st128(rs, go, f[0]); st128(rs, g1, f[1]); st128(rs, g2, f[2]); st128(rs, g3, f[3]); st128(rs, g4, f[4]);
    BAR();
    publish_last(ws + WS_LNFLAG + b * 128, mix(e0), lane, LCNT, 4);
    STAMP_AT(2);
  }
  const u32 tag = mix(e0);
  if (roleA) {
    f32x4 aq[5][2], akv[5][4];
    {
      const float i2 = ty ? 0.f : 1.f;
#pragma unroll
      for (int j = 0; j < 5; ++j) {
        const float iv = j < 2 ? 1.f : j == 2 ? i2 : 0.f;
        aq[j][0] = aq[j][1] = f32x4{1.f, 1.f, 1.f, 1.f};
#pragma unroll
        for (int i = 0; i < 4; ++i) akv[j][i] = f32x4{iv, iv, iv, iv};
      }
    }
    const u32 wa0 = L0 + ((g >> 1) * 16 + (g & 1) * 4 + q4) * 480 + p4 * 8;
    const u32 xa0 = L0 + LQ_B + wave * 2048 + lane * 16;
    s16x8 xA[4] = {}, xB[4], wq[5] = {};
    auto round = [&](int r, s16x8(&xo)[4], s16x8(&xn)[4]) {
      const u32 wa = wa0 + (r & 7) * 15360, wk = wa + ty * 96, w2 = wa + ty * 160, xa = xa0 + (r & 3) * 8192, xb = xa ^ 2048;
      BAR();
      s16x8 w[5];
      xn[0] = ld128(xa); w[0] = cat(ldtr(wk + 160), ldtr(wk + 160 + 3840));
      xn[1] = ld128(xa + 1024); xn[2] = ld128(xb); xn[3] = ld128(xb + 1024);
      w[1] = cat(ldtr(wk + 192), ldtr(wk + 192 + 3840));
      w[2] = cat(ldtr(w2 + 224), ldtr(w2 + 224 + 3840));
#pragma unroll
      for (int j = 0; j < 5; ++j) { aq[j][0] = mfma32(wq[j], xo[0], aq[j][0]); aq[j][1] = mfma32(wq[j], xo[1], aq[j][1]); }
#pragma unroll
      for (int j = 0; j < 10; ++j) { SG(0x100, 1); SG(0x8, 1); }
      SB();
      w[3] = cat(ldtr(wk + 320), ldtr(wk + 320 + 3840));
      w[4] = cat(ldtr(wk + 352), ldtr(wk + 352 + 3840));
#pragma unroll
      for (int j = 0; j < 5; ++j) wq[j] = cat(ldtr(wa + j * 32), ldtr(wa + 3840 + j * 32));
#pragma unroll
      for (int j = 0; j < 5; ++j)
#pragma unroll
        for (int i = 0; i < 4; ++i) akv[j][i] = mfma32(xn[i], w[j], akv[j][i]);
#pragma unroll
      for (int j = 0; j < 14; ++j) { SG(0x8, 1); SG(0x100, 1); }
      SG(0x8, 6);
      SB();
    };
#pragma unroll 1
    for (int r = 0; r < 20; r += 2) { round(r, xA, xB); round(r + 1, xB, xA); }
    BAR();
    STAMP_AT(3);
#pragma unroll
    for (int j = 0; j < 5; ++j) { aq[j][0] = mfma32(wq[j], xA[0], aq[j][0]); aq[j][1] = mfma32(wq[j], xA[1], aq[j][1]); }
    {
      const u32 va = L0 + L_KV + (8 * hw + g) * LN_PITCH + c16 * 16 + ty * 768;
#pragma unroll
      for (int pr = 0; pr < 2; ++pr) {
        const u32 vp = va + (pr ^ ty) * (4 * LN_PITCH);
        *(LAS s16x8*)(vp + 16 * LN_PITCH) = cat(pk4(akv[3][2 * pr]), pk4(akv[3][2 * pr + 1]));
        *(LAS s16x8*)(vp + 16 * LN_PITCH + 256) = cat(pk4(akv[4][2 * pr]), pk4(akv[4][2 * pr + 1]));
        *(LAS s16x8*)(vp) = cat(phi4(akv[0][2 * pr]), phi4(akv[0][2 * pr + 1]));
        *(LAS s16x8*)(vp + 256) = cat(phi4(akv[1][2 * pr]), phi4(akv[1][2 * pr + 1]));
      }
      if (ty) {
#pragma unroll
        for (int pr = 0; pr < 2; ++pr) *(LAS s16x8*)(va + (pr ^ 1) * (4 * LN_PITCH) + 16 * LN_PITCH - 256) = cat(pk4(akv[2][2 * pr]), pk4(akv[2][2 * pr + 1]));
        *(LAS s16x8*)(L0 + L_KV + (16 + c16) * LN_PITCH + 1280) = s16x8{0x3f80, 0x3f80, 0x3f80, 0x3f80, 0x3f80, 0x3f80, 0x3f80, 0x3f80};
      } else {
#pragma unroll
        for (int pr = 0; pr < 2; ++pr) *(LAS s16x8*)(va + pr * (4 * LN_PITCH) + 512) = cat(phi4(akv[2][2 * pr]), phi4(akv[2][2 * pr + 1]));
      }
    }
    STAMP_AT(4);
    BAR();
    const int pb = WS_PART + (hg * 4 + wave) * (16 * 896);
    {
      const int c32 = lane & 31, hh = lane >> 5;
      const u32 ka = L0 + L_KV + hh * LN_PITCH + (20 * wave + (c32 < 20 ? c32 : 19)) * 16;
      const u32 vb = L0 + L_KV + (16 + hh) * LN_PITCH + (c32 < 20 ? 20 * wave + c32 : 80) * 16;
      f32x16 d = {};
#pragma unroll
      for (int i = 0; i < 8; ++i)
        d = __builtin_amdgcn_mfma_f32_32x32x16_bf16(__builtin_bit_cast(b16x8, ld128(ka + i * 2 * LN_PITCH)), __builtin_bit_cast(b16x8, ld128(vb + i * 2 * LN_PITCH)), d, 0, 0, 0);
      const u32x4 lo = {pk2(d[0], d[1]), pk2(d[2], d[3]), pk2(d[4], d[5]), pk2(d[6], d[7])};
      const u32x2 hi = {pk2(d[8], d[9]), pk2(d[10], d[11])};
      st128(rs, c32 <= 20 ? pb + rt * 896 + (hh * 21 + c32) * 16 : OOB, __builtin_bit_cast(s16x8, lo));
      st64(rs, lane <= 20 ? pb + rt * 896 + 672 + lane * 8 : OOB, __builtin_bit_cast(s16x4, hi));
    }
    BAR();
    s16x4 pq[5][2];
#pragma unroll
    for (int j = 0; j < 3; ++j) { pq[j][0] = phi4(aq[j][0]); pq[j][1] = phi4(aq[j][1]); asm volatile("" : "+v"(pq[j][0]), "+v"(pq[j][1])); }
    publish(ws + WS_KVFLAG + (hg * 4 + wave) * 2048 + rt * 128, tag, lane);
    STAMP_AT(5);
#pragma unroll
    for (int j = 3; j < 5; ++j) { pq[j][0] = phi4(aq[j][0]); pq[j][1] = phi4(aq[j][1]); asm volatile("" : "+v"(pq[j][0]), "+v"(pq[j][1])); }
    u32 qa[5][3], za[5];
#pragma unroll
    for (int je = 0; je < 5; ++je) {
      const int e = 16 * je + c16, he = e / 20, el = e - 20 * he;
#pragma unroll
      for (int x = 0; x < 3; ++x) {
        const int dl = 4 * (je - 1 + x) + g - 5 * he;
        const int qi = dl < 4 ? ((dl & 1) * 21 + el) * 2 + (dl >> 1) : 84 + el;
        qa[je][x] = L0 + L_FIN + (dl >= 0 && dl < 5 ? he * 848 + qi * 8 : 3392);
        if (je - 1 + x >= 0 && je - 1 + x < 5) asm volatile("" : "+v"(qa[je][x]));
      }
      const int dl = 4 * je + g - 5 * p4;
      const int qi = dl < 4 ? ((dl & 1) * 21 + 20) * 2 + (dl >> 1) : 104;
      za[je] = L0 + L_FIN + (dl >= 0 && dl < 5 ? p4 * 848 + qi * 8 : 3392);
      asm volatile("" : "+v"(za[je]));
    }
    poll(ws + WS_KVFLAG + (hg * 4 + wave) * 2048, c16, tag);
    STAMP_AT(6);
    {
      u32x4 pr[16];
#pragma unroll
      for (int i = 0; i < 16; ++i) pr[i] = __builtin_amdgcn_raw_buffer_load_b128(rs, lane < 53 ? pb + lane * 16 : OOB, i * 896, 16);
      f32x4 s0 = {0.f, 0.f, 0.f, 0.f}, s1 = s0;
#pragma unroll
      for (int i = 0; i < 16; ++i) {
        s0 += f32x4{blo(pr[i][0]), bhi(pr[i][0]), blo(pr[i][1]), bhi(pr[i][1])};
        s1 += f32x4{blo(pr[i][2]), bhi(pr[i][2]), blo(pr[i][3]), bhi(pr[i][3])};
      }
      *(LAS s16x8*)(L0 + L_FIN + (lane < 53 ? wave * 848 + lane * 16 : 3392)) = cat(pk4(s0), pk4(s1));
    }
    STAMP_AT(7);
    BAR();
    {
      s16x4 kq[5][3], zq[5];
#pragma unroll
      for (int je = 0; je < 5; ++je) {
        zq[je] = ld64(za[je]);
#pragma unroll
        for (int x = 0; x < 3; ++x)
          if (je - 1 + x >= 0 && je - 1 + x < 5) kq[je][x] = ld64(qa[je][x]);
      }
      BAR();
      const int ob = WS_O + (2 * rt + hw) * 81920 + hg * 10240 + ty * 1024 + lane * 16;
      s16x4 ot[5][2];
      const s16x4 zz = {0, 0, 0, 0};
#pragma unroll
      for (int i = 0; i < 2; ++i) {
        const s16x8 p01 = cat(pq[0][i], pq[1][i]), p23 = cat(pq[2][i], pq[3][i]), p34 = cat(pq[3][i], pq[4][i]);
        f32x4 den = {1e-6f, 1e-6f, 1e-6f, 1e-6f};
        den = mfma32(cat(zq[0], zq[1]), p01, den);
        den = mfma32(cat(zq[2], zq[3]), p23, den);
        den = mfma32(cat(zz, zq[4]), p34, den);
        const f32x4 z4 = {0.f, 0.f, 0.f, 0.f};
        f32x4 o[5];
        o[0] = mfma32(cat(kq[0][1], kq[0][2]), p01, z4);
        o[1] = mfma32(cat(kq[1][2], zz), p23, mfma32(cat(kq[1][0], kq[1][1]), p01, z4));
        o[2] = mfma32(cat(zz, kq[2][0]), p01, mfma32(cat(kq[2][1], kq[2][2]), p23, z4));
        o[3] = mfma32(cat(zz, kq[3][2]), p34, mfma32(cat(kq[3][0], kq[3][1]), p23, z4));
        o[4] = mfma32(cat(kq[4][0], kq[4][1]), p34, z4);
        const float r0 = __builtin_amdgcn_rcpf(den[0]), r1 = __builtin_amdgcn_rcpf(den[1]), r2 = __builtin_amdgcn_rcpf(den[2]), r3 = __builtin_amdgcn_rcpf(den[3]);
        o[0] *= r0;
        o[1] *= g < 1 ? r0 : r1;
        o[2] *= g < 2 ? r1 : r2;
        o[3] *= g < 3 ? r2 : r3;
        o[4] *= r3;
#pragma unroll
        for (int je = 0; je < 5; ++je) ot[je][i] = pk4(o[je]);
      }
#pragma unroll
      for (int je = 0; je < 5; ++je) st128(rs, ob + je * 2048, cat(ot[je][0], ot[je][1]));
    }
  } else if (roleB) {
    const bool live = ft < 13 || wave < 2;
    f32x4 acc[2][5];
#pragma unroll
    for (int t = 0; t < 2; ++t) {
      const u32x2 bb = *(const GAS u32x2*)(P.b1 + (live ? ft * 256 + wave * 64 : 0) + (u32)(t * 32 + g * 8));
      const f32x4 bi = {blo(bb[0]), bhi(bb[0]), blo(bb[1]), bhi(bb[1])};
#pragma unroll
      for (int j = 0; j < 5; ++j) acc[t][j] = bi;
    }
    const u32 aa0 = L0 + (8 * g + q4) * 256 + ((2 * wave) ^ (q4 | ((g & 1) << 2))) * 32 + p4 * 8;
    const u32 ha0 = L0 + L1_B + lane * 16;
    u32 as = 0;
    s16x8 a1[2] = {}, h1[5] = {};
#pragma unroll 1
    for (int r = 0; r < 32; ++r) {
      const u32 aa = aa0 + as, ab = aa ^ 32, ha = ha0 + (r & 3) * 10240;
      BAR();
      s16x8 a0[2], h0[5];
      a0[0] = cat(ldtr(aa), ldtr(aa + 1024)); a0[1] = cat(ldtr(ab), ldtr(ab + 1024));
#pragma unroll
      for (int j = 0; j < 5; ++j) h0[j] = ld128(ha + j * 1024);
#pragma unroll
      for (int j = 0; j < 5; ++j) { acc[0][j] = mfma32(a1[0], h1[j], acc[0][j]); acc[1][j] = mfma32(a1[1], h1[j], acc[1][j]); }
#pragma unroll
      for (int j = 0; j < 9; ++j) { SG(0x100, 1); SG(0x8, 1); }
      SB();
      a1[0] = cat(ldtr(aa + 8192), ldtr(aa + 9216)); a1[1] = cat(ldtr(ab + 8192), ldtr(ab + 9216));
#pragma unroll
      for (int j = 0; j < 5; ++j) h1[j] = ld128(ha + 5120 + j * 1024);
#pragma unroll
      for (int j = 0; j < 5; ++j) { acc[0][j] = mfma32(a0[0], h0[j], acc[0][j]); acc[1][j] = mfma32(a0[1], h0[j], acc[1][j]); }
#pragma unroll
      for (int j = 0; j < 4; ++j) { SG(0x100, 2); SG(0x8, 1); }
      SG(0x100, 1); SG(0x8, 6);
      SB();
      as = as == 6 * 16384 ? 0 : as + 16384;
    }
    BAR();
    STAMP_AT(3);
#pragma unroll
    for (int j = 0; j < 5; ++j) { acc[0][j] = mfma32(a1[0], h1[j], acc[0][j]); acc[1][j] = mfma32(a1[1], h1[j], acc[1][j]); }
    if (live) {
      const int go = WS_G + dt * 276480 + (ft * 4 + wave) * 5120 + lane * 16;
#pragma unroll
      for (int j = 0; j < 5; ++j) st128(rs, go + j * 1024, cat(gelu4(acc[0][j]), gelu4(acc[1][j])));
      STAMP_AT(4);
    }
    publish_last(ws + WS_GFLAG + dt * 4096 + ft * 128, tag, lane, LCNT, 8);
    BAR();
  } else {
    BAR();
    const u32 ra = L0 + (4 * g + q4) * 320 + hw * 160 + p4 * 8, rb = ra + p4 * 8;
    const u32 a0 = rb + ty * 64, a1 = a0 + 8, a2 = ty ? ra + 128 : rb + 64;
    const int d8 = 2 * (sp & 3) + hw;
    const int go = WS_WO + d8 * 102400 + (sp >> 2) * 25600 + ty * 2048 + lane * 16;
#pragma unroll 1
    for (int k = 0; k < 5; ++k) {
      const u32 o = k * 10240;
      const s16x8 f0 = cat(ldtr(a0 + o), ldtr(a0 + o + 5120)), f1 = cat(ldtr(a1 + o), ldtr(a1 + o + 5120)), f2 = cat(ldtr(a2 + o), ldtr(a2 + o + 5120));
      st128(rs, go + k * 5120, f0);
      st128(rs, go + k * 5120 + 1024, f1);
      st128(rs, go + k * 5120 + 2048, f2);
      asm volatile("s_nop 1" ::: "memory");
    }
    publish(ws + WS_GFLAG + d8 * 4096 + (14 + (sp >> 2) * 2 + ty) * 128, tag, lane);
    BAR();
  }

  STAMP_AT(8);
  {
    const int srow = st * 64 + wave * 16 + c16;
    const u32 xo = srow * 1280 + dt3 * 160 + g * 16;
    const u32x4 x01 = *(const GAS u32x4*)(P.x + xo), x23 = *(const GAS u32x4*)(P.x + xo + 64);
    const u32x2 x4 = *(const GAS u32x2*)(P.x + (xo + 128 - g * 8));
    const u32 bs = *(const GAS unsigned short*)(P.b2 + (u32)(srow * 2));
    f32x4 acc[2][5] = {};
    const u32 aa0 = L0 + (32 * ty + 4 * g + q4) * 128 + (wave ^ ((2 * g + (q4 >> 1)) & 3)) * 32 + p4 * 8;
    const u32 ga0 = L0 + L3_B + ty * 5120 + lane * 16;
    const u32 oa0 = L0 + ty * 4096 + hw * 1024 + lane * 16 + ty * 8;
    u32 sa = 0, sb = 0;
    struct FA { s16x8 a[2], g[5]; };
    auto lda = [&](FA& f, bool tr) {
      const u32 aa = (tr ? aa0 : oa0) + sa, ab = aa ^ (tr ? 32 : 8), ga = ga0 + sb;
      sa = sa == L3_B - 8192 ? 0 : sa + 8192;
      sb = sb == LA3 * 10240 ? 0 : sb + 10240;
      BAR();
      if (tr) { f.a[1] = cat(ldtr(ab), ldtr(ab + 2048)); f.a[0] = cat(ldtr(aa), ldtr(aa + 2048)); }
      else { f.a[1] = cat(ld64(ab), ld64(ab + 2048)); f.a[0] = cat(ld64(aa), ld64(aa + 2048)); }
#pragma unroll
      for (int j = 0; j < 5; ++j) f.g[j] = ld128(ga + j * 1024);
    };
    auto mma = [&](const FA& f) {
#pragma unroll
      for (int j = 0; j < 5; ++j) acc[1][j] = mfma32(f.g[j], f.a[1], acc[1][j]);
#pragma unroll
      for (int j = 0; j < 5; ++j) acc[0][j] = mfma32(f.g[j], f.a[0], acc[0][j]);
    };
    auto pat = [&]() {
#pragma unroll
      for (int j = 0; j < 5; ++j) { SG(0x100, 2); SG(0x8, 1); }
      SG(0x8, 5);
      SB();
    };
    FA f0, f1;
    lda(f0, true); SB();
    {
      const float bf = blo(bs);
      acc[0][0] = f32x4{blo(x01[0]), bhi(x01[0]), blo(x01[1]), bhi(x01[1])} + bf;
      acc[0][1] = f32x4{blo(x01[2]), bhi(x01[2]), blo(x01[3]), bhi(x01[3])} + bf;
      acc[0][2] = f32x4{blo(x23[0]), bhi(x23[0]), blo(x23[1]), bhi(x23[1])} + bf;
      acc[0][3] = f32x4{blo(x23[2]), bhi(x23[2]), blo(x23[3]), bhi(x23[3])} + bf;
      acc[0][4] = f32x4{blo(x4[0]), bhi(x4[0]), blo(x4[1]), bhi(x4[1])} + bf;
      if (roleA) publish(ws + WS_OFLAG + (2 * rt + hw) * 2048 + (ty * 8 + hg) * 128, tag, lane);
      SB();
    }
#pragma unroll 1
    for (int r = 1; r < 27; r += 2) { lda(f1, true); mma(f0); pat(); lda(f0, true); mma(f1); pat(); }
    STAMP_AT(9);
#pragma unroll 1
    for (int r = 27; r < 37; r += 2) { lda(f1, false); mma(f0); pat(); lda(f0, false); mma(f1); pat(); }
    mma(f0);
    const u32 ea = L0 + L3_B + 20480 + lane * 16;
    static_assert(36 % NB3 < 2, "");
#pragma unroll
    for (int j = 0; j < 5; ++j) *(LAS f32x4*)(ea + wave * 5120 + j * 1024) = acc[1][j];
    SG(0x8, 8); SG(0x200, 2); SG(0x8, 2); SG(0x200, 3);
    SB();
    BAR();
#pragma unroll
    for (int j = 0; j < 5; ++j) acc[0][j] += *(const LAS f32x4*)(ea + (wave ^ 1) * 5120 + j * 1024);
    *(GAS s16x8*)(P.out + xo) = cat(pk4(acc[0][0]), pk4(acc[0][1]));
    *(GAS s16x8*)(P.out + xo + 64) = cat(pk4(acc[0][2]), pk4(acc[0][3]));
    *(GAS s16x4*)(P.out + (xo + 128 - g * 8)) = pk4(acc[0][4]);
  }
}

extern "C" void kernel_launch(void* const* d_in, const int* in_sizes, int n_in, void* d_out, int out_size, void* d_ws,
                              size_t ws_size, hipStream_t stream) {
  Params p;
  p.x = (const char*)d_in[0];
  p.wqkv = (const char*)d_in[1];
  p.wo = (const char*)d_in[2];
  p.w1 = (const char*)d_in[4];
  p.b1 = (const char*)d_in[5];
  p.w2 = (const char*)d_in[6];
  p.b2 = (const char*)d_in[7];
  p.out = (char*)d_out;
  p.ws = (char*)d_ws;
  (void)hipFuncSetAttribute((const void*)fused_fwd, hipFuncAttributeMaxDynamicSharedMemorySize, L_BYTES);
  fused_fwd<<<256, 320, L_BYTES, stream>>>(p);
}
```

```cpp
#include <hip/hip_runtime.h>
#include <hip/hip_bf16.h>
#pragma clang diagnostic ignored "-Wint-to-pointer-cast"

#define GAS __attribute__((address_space(1)))
#define LAS __attribute__((address_space(3)))
#define DI __device__ __forceinline__
typedef unsigned u32;
typedef unsigned long long u64;
typedef short s16x4 __attribute__((ext_vector_type(4)));
typedef short s16x8 __attribute__((ext_vector_type(8)));
typedef __bf16 b16x2 __attribute__((ext_vector_type(2)));
typedef __bf16 b16x8 __attribute__((ext_vector_type(8)));
typedef float f32x2 __attribute__((ext_vector_type(2)));
typedef float f32x4 __attribute__((ext_vector_type(4)));
typedef float f32x16 __attribute__((ext_vector_type(16)));
typedef unsigned u32x2 __attribute__((ext_vector_type(2)));
typedef unsigned u32x4 __attribute__((ext_vector_type(4)));

constexpr int WS_EPOCH = 0;
constexpr int WS_ALLFLAG = 1024;
constexpr int WS_LNFLAG = 196608;
constexpr int WS_GFLAG = 720896;
constexpr int WS_KVFLAG = 65536;
constexpr int WS_OFLAG = 131072;
constexpr int WS_PART = 262144;
constexpr int WS_XN = 786432;
constexpr int WS_H = WS_XN + 2621440;
constexpr int WS_G = WS_H + 2621440;
constexpr int WS_O = WS_G + 2211840;
constexpr int WS_WO = WS_O + 2621440;
constexpr int WS_END = WS_WO + 819200;
constexpr int OOB = 0x40000000;
constexpr int L_BYTES = 163840;
constexpr int L_LN = 131072, LN_PITCH = 1296;
constexpr int NB3 = 5;
constexpr int NB1 = 4, NA1 = 7;
constexpr int LA3 = NB3 - 1, NA3 = 18 - NB3, H3 = 2 * (NA3 - LA3 - 2);
constexpr int LQ_B = 122880, L1_B = NA1 * 16384, L3_B = NA3 * 8192;
constexpr int L_KV = L3_B, L_FIN = L3_B + NB3 * 10240;
constexpr int a3(int t) { return t < 1 || t > 35 - LA3 ? 0 : t <= H3 ? 4 : 8; }
constexpr int w3(int r) { int n = a3(r - LA3); for (int t = r - LA3 + 1; t < r; ++t) n += a3(t) + (t <= 36 - LA3 ? 10 : 0); return n; }
#define APRE 14
#define MAP3 2
#define NPK_A 8
#define ALN 0
#define LPK 22
#define NTCH 7
#define EARLY 16
#define PUBR 3
#define TH_PA 16
#define TH_P3 8
#define SLP 2
#define PRE_A 2
#define MID_A 2
#define PRE_B 3
#define MID_B 3
#define XP 1
constexpr u32 SPIN_CAP = 1u << 16;

#define BAR() do { asm volatile("" ::: "memory"); __builtin_amdgcn_s_waitcnt(0xC07F); asm volatile("s_barrier" ::: "memory"); } while (0)
#define WAITV(n) asm volatile("s_waitcnt vmcnt(" #n ")" ::: "memory")
#define WAITX(n) asm volatile("s_waitcnt vmcnt(%0)" ::"i"((n) > 63 ? 63 : (n)) : "memory")
#define STAMP_AT(k)
#define STAMP_DECL
#define ACC_TO(k)
#define STAMP_OUT()
#define ACC0(k)
#define ACC1(k)
#define ACC3(k)
#define LW1(n) do { ACC1(13); WAITX(n); ACC1(11); } while (0)
#define LBAR() asm volatile("s_barrier" ::: "memory")
#define LB1() do { LBAR(); ACC1(12); } while (0)
#define LW3(n) do { ACC3(13); WAITX(n); ACC3(11); } while (0)
#define LB3() do { LBAR(); ACC3(12); } while (0)
#define RLX_AGENT __ATOMIC_RELAXED, __HIP_MEMORY_SCOPE_AGENT

DI f32x4 mfma32(s16x8 a, s16x8 b, f32x4 c) {
  return __builtin_amdgcn_mfma_f32_16x16x32_bf16(__builtin_bit_cast(b16x8, a), __builtin_bit_cast(b16x8, b), c, 0, 0, 0);
}
DI f32x16 mfmaw(s16x8 a, s16x8 b, f32x16 c) {
  return __builtin_amdgcn_mfma_f32_32x32x16_bf16(__builtin_bit_cast(b16x8, a), __builtin_bit_cast(b16x8, b), c, 0, 0, 0);
}
DI s16x4 ldtr(u32 a) { return __builtin_amdgcn_ds_read_tr16_b64_v4i16((LAS s16x4*)a); }
DI s16x8 ld128(u32 a) { return *(const LAS s16x8*)a; }
DI s16x4 ld64(u32 a) { return *(const LAS s16x4*)a; }
DI s16x8 cat(s16x4 a, s16x4 b) { return __builtin_shufflevector(a, b, 0, 1, 2, 3, 4, 5, 6, 7); }
DI u32 pk2(float a, float b) { f32x2 v = {a, b}; return __builtin_bit_cast(u32, __builtin_convertvector(v, b16x2)); }
DI s16x4 pk4(f32x4 v) { u32x2 r = {pk2(v[0], v[1]), pk2(v[2], v[3])}; return __builtin_bit_cast(s16x4, r); }
DI float blo(u32 u) { return __uint_as_float(u << 16); }
DI float bhi(u32 u) { return __uint_as_float(u & 0xffff0000u); }
template <int C> DI float dpp_add(float s) { return s + __int_as_float(__builtin_amdgcn_update_dpp(0, __float_as_int(s), C, 0xf, 0xf, true)); }
DI float sum32(float s) {
  s = dpp_add<0x121>(s); s = dpp_add<0x122>(s); s = dpp_add<0x124>(s); s = dpp_add<0x128>(s);
  auto r = __builtin_amdgcn_permlane16_swap(__float_as_uint(s), __float_as_uint(s), false, false);
  return __uint_as_float(r[0]) + __uint_as_float(r[1]);
}
#define SG(m, n) __builtin_amdgcn_sched_group_barrier(m, n, 0)
#define SB() __builtin_amdgcn_sched_barrier(0)
#define AL() do { asm volatile(".p2align 3"); SB(); } while (0)
template <int N> DI void phi(const f32x4 (&a)[N], s16x4 (&o)[N]) {
  const float L = 1.4426950408889634f;
  f32x4 y[N];
  SB(); AL();
#pragma unroll
  for (int i = 0; i < N; ++i) y[i] = __builtin_elementwise_fma(a[i], f32x4{L, L, L, L}, f32x4{-L, -L, -L, -L});
  SB();
#pragma unroll
  for (int i = 0; i < 4 * N; ++i) y[i >> 2][i & 3] = __builtin_amdgcn_fmed3f(__builtin_amdgcn_exp2f(y[i >> 2][i & 3]), 0.f, 1.f);
  SB();
#pragma unroll
  for (int i = 0; i < 4 * N; ++i) y[i >> 2][i & 3] = __int_as_float(max(__float_as_int(a[i >> 2][i & 3]), __float_as_int(y[i >> 2][i & 3])));
  SB();
#pragma unroll
  for (int i = 0; i < N; ++i) y[i] += 1e-6f;
  SB();
#pragma unroll
  for (int i = 0; i < N; ++i) o[i] = pk4(y[i]);
}
DI s16x8 gelu8(f32x4 u0, f32x4 u1) {
  const float K = -2.f * 0.7978845608028654f * 1.4426950408889634f, K3 = K * 0.044715f;
  SB(); AL();
  f32x4 y0 = u0 * u0, y1 = u1 * u1;
  SB();
  y0 = __builtin_elementwise_fma(y0, f32x4{K3, K3, K3, K3}, f32x4{K, K, K, K});
  y1 = __builtin_elementwise_fma(y1, f32x4{K3, K3, K3, K3}, f32x4{K, K, K, K});
  SB();
  y0 *= u0; y1 *= u1;
  SB();
#pragma unroll
  for (int i = 0; i < 4; ++i) y0[i] = __builtin_amdgcn_exp2f(y0[i]);
#pragma unroll
  for (int i = 0; i < 4; ++i) y1[i] = __builtin_amdgcn_exp2f(y1[i]);
  SB();
  y0 += 1.f; y1 += 1.f;
  SB();
#pragma unroll
  for (int i = 0; i < 4; ++i) y0[i] = __builtin_amdgcn_rcpf(y0[i]);
#pragma unroll
  for (int i = 0; i < 4; ++i) y1[i] = __builtin_amdgcn_rcpf(y1[i]);
  SB();
  y0 *= u0; y1 *= u1;
  SB();
  return cat(pk4(y0), pk4(y1));
}
DI void st128(__amdgpu_buffer_rsrc_t rs, int off, s16x8 v) { __builtin_amdgcn_raw_buffer_store_b128(__builtin_bit_cast(u32x4, v), rs, off, 0, 16); }
DI void st64(__amdgpu_buffer_rsrc_t rs, int off, s16x4 v) { __builtin_amdgcn_raw_buffer_store_b64(__builtin_bit_cast(u32x2, v), rs, off, 0, 16); }
template <int V> struct IC { static constexpr int value = V; };
DI u32 mix(u32 e) { return e * 0x9E3779B1u + 0x7F4A7C15u; }

#define GL(v, off, sc) "global_load_lds_dwordx4 " v ", %0 offset:" #off sc "\n\t"
#define M0(bias) "s_add_u32 m0, %1, " #bias "\n\ts_nop 0\n\t"
#define RING m = m == HI - STEP ? LO : m + STEP
template <int STEP, int LO, int HI> DI void gl4(const char* base, u32& m, const u32 (&v)[4]) {
  asm volatile(M0(0) GL("%2", 0, "") GL("%3", 1024, "") GL("%4", 2048, "") GL("%5", 3072, "")
               ::"s"(base), "s"(m), "v"(v[0]), "v"(v[1]), "v"(v[2]), "v"(v[3]) : "memory", "scc");
  RING;
}
template <int STEP, int LO, int HI> DI void gl8(const char* base, u32& m, const u32 (&v)[8]) {
  asm volatile(M0(4096) GL("%2", -4096, "") GL("%3", -3072, "") GL("%4", -2048, "") GL("%5", -1024, "")
               GL("%6", 0, "") GL("%7", 1024, "") GL("%8", 2048, "") GL("%9", 3072, "")
               ::"s"(base), "s"(m), "v"(v[0]), "v"(v[1]), "v"(v[2]), "v"(v[3]), "v"(v[4]), "v"(v[5]), "v"(v[6]), "v"(v[7]) : "memory", "scc");
  RING;
}
template <int STEP, int LO, int HI> DI void gl8w(const char* base, u32& m, const u32 (&v)[8]) {
  asm volatile("s_add_u32 m0, %1, 3840\n\ts_mov_b32 exec_hi, 0x0fffffff\n\t" GL("%2", -3840, "") GL("%3", -2880, "") GL("%4", -1920, "") GL("%5", -960, "")
               GL("%6", 0, "") GL("%7", 960, "") GL("%8", 1920, "") GL("%9", 2880, "") "s_mov_b32 exec_hi, -1"
               ::"s"(base), "s"(m), "v"(v[0]), "v"(v[1]), "v"(v[2]), "v"(v[3]), "v"(v[4]), "v"(v[5]), "v"(v[6]), "v"(v[7]) : "memory", "scc");
  RING;
}
DI void gl5(const char* base, u32 m, const u32 (&v)[5]) {
  asm volatile(M0(4096) GL("%2", -4096, "") GL("%3", -3072, "") GL("%4", -2048, "") GL("%5", -1024, "") GL("%6", 0, "")
               ::"s"(base), "s"(m), "v"(v[0]), "v"(v[1]), "v"(v[2]), "v"(v[3]), "v"(v[4]) : "memory", "scc");
}
#define GL8S M0(4096) GL("%2", -4096, " sc1") GL("%2", -3072, " sc1") GL("%2", -2048, " sc1") GL("%2", -1024, " sc1") \
             GL("%2", 0, " sc1") GL("%2", 1024, " sc1") GL("%2", 2048, " sc1") GL("%2", 3072, " sc1")
template <int STEP, int LO, int HI> DI void glin8(const char* base, u32& m, u32 vl4) {
  asm volatile(GL8S ::"s"(base), "s"(m), "v"(vl4) : "memory", "scc");
  RING;
}
template <int STEP, int LO, int HI> DI void glin10(const char* base, u32& m, u32 vl4, u32 vl8) {
  asm volatile(GL8S M0(8192) GL("%3", 0, " sc1") GL("%3", 1024, " sc1")
               ::"s"(base), "s"(m), "v"(vl4), "v"(vl8) : "memory", "scc");
  RING;
}
DI void touch(const char* base, u32 m, u32 v) {
  asm volatile("s_mov_b32 m0, %1\n\ts_nop 0\n\tglobal_load_lds_dword %2, %0" ::"s"(base), "s"(m), "v"(v) : "memory");
}
DI void poll(const char* f, int idx, u32 tag) {
  const GAS u32* p = (const GAS u32*)(f + (u32)(idx * 128));
#pragma unroll 1
  for (u32 sp = 0; sp < SPIN_CAP; ++sp) {
    u32 v = __hip_atomic_load(p, RLX_AGENT);
    if (__all(v == tag)) break;
    __builtin_amdgcn_s_sleep(SLP);
  }
  asm volatile("" ::: "memory");
}
DI u32 peek(const char* f, int idx) {
  u32 v;
  asm volatile("global_load_dword %0, %1, %2 sc1" : "=&v"(v) : "v"(idx * 128), "s"(f) : "memory");
  return v;
}
DI void publish(char* f, u32 tag, int lane) {
  WAITV(0);
  if (lane == 0) __hip_atomic_store((GAS u32*)f, tag, RLX_AGENT);
}
DI void publish_last(char* f, u32 tag, int lane, u32 cnt, u32 n) {
  WAITV(0);
  if (lane == 0 && __hip_atomic_fetch_add((LAS u32*)cnt, 1u, __ATOMIC_RELAXED, __HIP_MEMORY_SCOPE_WORKGROUP) == n - 1)
    __hip_atomic_store((GAS u32*)f, tag, RLX_AGENT);
}

struct Params {
  const char *x, *wqkv, *wo, *w1, *b1, *w2, *b2;
  char *out, *ws;
};
#define KEEP_RAW() asm volatile("" ::"v"(raw[0]), "v"(raw[1]), "v"(raw[2]), "v"(raw[3]), "v"(raw[4]))
__global__ __launch_bounds__(320) void fused_fwd(Params P) {
  const int tid = threadIdx.x, lane = tid & 63;
  const int wave = __builtin_amdgcn_readfirstlane(tid >> 6);
  const int b = blockIdx.x, xc = b & 7, jb = b >> 3;
  char* const ws = P.ws;
  constexpr u32 L0 = 0;
  const u32 LDUM = L0 + L_BYTES - 256, LCNT = L0 + L_BYTES - 512;
  const int c16 = lane & 15, g = lane >> 4, q4 = c16 >> 2, p4 = lane & 3;
  STAMP_DECL;
  STAMP_AT(0);
  {
    u64 pc; int left;
    asm volatile("s_getpc_b64 %0\n.Lcode_here:\n\ts_mov_b32 %1, .Lcode_exit_a-.Lcode_here\n\ts_max_i32 %1, %1, .Lcode_exit_b-.Lcode_here"
                 : "=s"(pc), "=&s"(left)::"scc");
    touch((const char*)pc, LDUM, min(((jb * 5 + wave) * 2 + (lane & 1)) * 128, left));
  }
  const int row = 2 * (wave & 3) + (lane >> 5), hl = lane & 31;
  u32x2 raw[5];
  asm volatile("global_load_dwordx2 %0, %5, %6\n\tglobal_load_dwordx2 %1, %5, %6 offset:256\n\tglobal_load_dwordx2 %2, %5, %6 offset:512\n\t"
               "global_load_dwordx2 %3, %5, %6 offset:768\n\tglobal_load_dwordx2 %4, %5, %6 offset:1024"
               : "=&v"(raw[0]), "=&v"(raw[1]), "=&v"(raw[2]), "=&v"(raw[3]), "=&v"(raw[4])
               : "v"(row * 1280 + hl * 8), "s"(P.x + b * (8 * 1280)) : "memory");
  u32 e0 = *(const __attribute__((address_space(4))) u32*)(ws + WS_EPOCH);
  const bool roleA = b < 128;
  const int hg = 2 * (xc & 3) + (jb & 1), rt = 8 * (xc >> 2) + (jb >> 1);
  const int dt = jb & 7, grp = (jb >> 3) & 1;
  const bool late = !roleA && xc >= 5, narrow = !roleA && xc < 5 && grp;
  const int fu = late ? 4 * xc - 5 + 2 * grp : grp ? xc : 5 + 2 * xc;
  const int gi = late ? 2 * xc + grp : grp ? xc : 5 + xc;
  const int st = 4 * xc + (MAP3 == 0 ? jb & 3 : MAP3 == 1 ? (jb & 1) + 2 * (jb >> 4) : jb >> 3), dt3 = MAP3 == 0 ? jb >> 2 : MAP3 == 1 ? (jb >> 1) & 7 : jb & 7;

  if (__builtin_expect(wave == 4, 1)) {
    __builtin_amdgcn_s_setprio(3);
    const u32 vl4 = lane * 16 + 4096, vl8 = vl4 + 4096;
    const u32 u0 = (lane >> 3) * 4096 + ((lane & 7) ^ (g << 1)) * 16;
    const u32 u[4] = {u0, u0 + 31744, u0 + 2 * 31744, u0 + 3 * 31744};
    const char* w2p = P.w2 + st * 128;
    u32 sa3 = 0;
    auto ahalf = [&]() { gl4<4096, 0, L3_B>(w2p, sa3, u); w2p += 131072; };
    const char* const gfl = ws + WS_GFLAG + dt3 * 8192;
    const char* bp = ws + WS_G + dt3 * 276480;
    u32 gf, sb3 = L3_B;
    auto bunit3 = [&]() { glin10<10240, L3_B, L3_B + NB3 * 10240>(bp, sb3, vl4, vl8); bp += 10240; };
    auto start3 = [&](int done) {
#pragma unroll 1
      for (int i = 0; i < 4; ++i) bunit3();
#pragma unroll 1
      for (int i = done; i < 2 * NA3; i += 2) { ahalf(); ahalf(); }
    };
    if (!roleA) {
      u32 v[8];
      {
        const u32 e = narrow ? (lane >> 3) * 3456 + ((lane & 7) ^ ((g & 1) << 1)) * 16 : g * 3456 + (c16 ^ (g << 1)) * 16;
        const u32 stp = (narrow ? 8 : 4) * 3456 - 1024;
#pragma unroll
        for (int i = 0; i < 8; ++i) {
          const u32 col = narrow ? ((lane & 7) ^ ((g & 1) << 1)) * 16 : (c16 ^ (g << 1)) * 16;
          const u32 flip = narrow ? (i & 1) * 64 : ((i >> 1) & 1) * 128;
          v[i] = e - col + (col ^ flip) + i * stp + 4096;
        }
      }
      const char* wb = P.w1 + fu * 128;
      const char* hb = ws + WS_H + dt * 327680;
      const u32 d0 = dt ^ (XP && late ? grp : 0);
      u32 ia = 0, ib2 = 0, sa = narrow ? 10 * 8192 : 6 * 8192, sb = L1_B;
      const u32 istep = narrow ? 2 : 1;
      auto a8 = [&]() { gl8<8192, 0, L1_B>(wb + (ia ^ (8 * d0)) * 110592, sa, v); ia += istep; };
      auto bunit = [&]() { glin10<10240, L1_B, L1_B + NB1 * 10240>(hb + (ib2 ^ (4 * d0)) * 10240, sb, vl4, vl8); ++ib2; };
      STAMP_AT(1);
#pragma unroll 1
      for (int i = 0; i < PRE_B; ++i) a8();
      asm volatile("" : "+s"(e0));
      BAR(); LBAR(); STAMP_AT(14);
#pragma unroll 1
      for (int i = 0; i < MID_B; ++i) a8();
      LBAR();
      if (narrow) {
        u32 w5[5];
#pragma unroll
        for (int i = 0; i < 5; ++i) { const u32 n = 64 * i + lane; w5[i] = n / 40u * 640 + lane * 16 + 4096; }
        const char* wp = P.wo + (xc * 4 + (dt >> 1)) * (32 * 1280) + (dt & 1) * 640;
#pragma unroll 1
        for (int i = 0; i < 4; ++i) gl5(wp + i * (8 * 1280), 7 * 8192 + i * 5120, w5);
      }
#pragma unroll 1
      for (int i = PRE_B + MID_B + (narrow ? 3 : 0); i < 10; ++i) { WAITV(32); a8(); }
      STAMP_AT(2);
      const u32 tg = mix(e0);
      u32 f0 = peek(ws + WS_LNFLAG, (d0 & 6) * 32 + lane);
      a8(); a8();
      WAITV(16);
      KEEP_RAW();
      asm volatile("" : "+v"(f0));
      STAMP_AT(15);
      if (!__all(f0 == tg)) poll(ws + WS_LNFLAG, (d0 & 6) * 32 + lane, tg);
      STAMP_AT(3); ACC_TO(-1);
      bunit(); bunit(); bunit();
      a8(); a8();
      LW1(36); LB1(); bunit();
      LW1(36);
      if (narrow) {
        LB1(); bunit(); LW1(20);
        LB1(); a8(); bunit(); LW1(28);
        f0 = peek(ws + WS_ALLFLAG, 0);
#pragma unroll 1
        for (int r = 3; r < 5; ++r) { LB1(); a8(); bunit(); LW1(28); }
      } else {
#pragma unroll 1
        for (int r = 1; r < 3; ++r) { LB1(); a8(); a8(); bunit(); LW1(52); }
        f0 = peek(ws + WS_ALLFLAG, 0);
#pragma unroll 1
        for (int r = 3; r < 5; ++r) { LB1(); a8(); a8(); bunit(); LW1(52); }
      }
      asm volatile("" : "+v"(f0));
      STAMP_AT(5);
      if (!__all(f0 == tg)) poll(ws + WS_ALLFLAG, 0, tg);
      STAMP_AT(6);
      if (narrow) {
#pragma unroll 1
        for (int r = 5; r < 29; ++r) { LB1(); if (r & 3) a8(); bunit(); LW1(28); }
      } else {
#pragma unroll 1
        for (int r = 5; r < 29; ++r) { LB1(); a8(); if (r & 3) a8(); bunit(); LW1(44); }
      }
      LB1();
      if (narrow) { LW1(10); LB1(); LW1(0); LB1(); }
      else {
        constexpr int E1 = 3 * EARLY / 8;
#pragma unroll 1
        for (int i = 0; i < E1; ++i) ahalf();
        LW1(18 + 4 * E1); LB1();
#pragma unroll 1
        for (int i = 0; i < E1; ++i) ahalf();
        LW1(8 * E1); LB1();
        if (!late) {
#pragma unroll 1
          for (int i = 2 * E1; i < EARLY; ++i) ahalf();
        }
      }
      if (!late) {
        LBAR();
        STAMP_AT(4); ACC0(11);
#pragma unroll 1
        for (int i = 0; i < 10; ++i) { ahalf(); WAITX(TH_P3); }
        gf = peek(gfl, lane < 5 ? lane : 0);
        WAITV(0);
        BAR();
      } else {
        ahalf(); ahalf(); ahalf(); ahalf();
        LBAR();
        STAMP_AT(4); ACC0(11);
#pragma unroll 1
        for (int i = 3 * EARLY / 8 * 2 + 4; i < LPK; ++i) ahalf();
        gf = peek(gfl, lane < 5 ? lane : 0);
        ahalf(); ahalf(); ahalf(); ahalf();
        WAITV(16);
      }
      asm volatile("" : "+v"(gf));
      if (!__all(gf == tg)) poll(gfl, lane < 5 ? lane : 0, tg);
      STAMP_AT(7); ACC_TO(-1);
      start3(late ? LPK + 4 : narrow ? 10 : EARLY + 10);
    } else {
      const u32 hi = lane >= 30, cc = lane - 30 * hi, sg = (cc >= 10) + (cc >= 20);
      u32 v[8];
#pragma unroll
      for (int i = 0; i < 8; ++i) v[i] = (hi + 2 * (i & 1) + 8 * ((i >> 1) & 1) + 4 * (i >> 2) + 1) * 3840 + sg * 1120 + cc * 16 - i * 960;
      const char* wb = P.wqkv + hg * 160;
      const char* xb = ws + WS_XN + rt * 163840;
      const u32 rot = (5 * (2 * (rt & 7) + (rt >> 3))) >> 2;
      u32 oa = rot * 122880, ob = rot * 8192, sa = 0, sb = LQ_B;
      auto a8 = [&]() { gl8w<7680, 0, LQ_B>(wb + oa, sa, v); oa = oa == 39 * 61440 ? 0 : oa + 61440; };
      auto bunit = [&]() { glin8<8192, LQ_B, LQ_B + 4 * 8192>(xb + ob, sb, vl4); ob = ob == 19 * 8192 ? 0 : ob + 8192; };
      STAMP_AT(1);
#pragma unroll 1
      for (int i = 0; i < PRE_A; ++i) a8();
      asm volatile("" : "+s"(e0));
      BAR(); LBAR(); STAMP_AT(14);
#pragma unroll 1
      for (int i = 0; i < MID_A; ++i) a8();
      LBAR();
#pragma unroll 1
      for (int i = PRE_A + MID_A; i < NPK_A; ++i) { WAITV(24); a8(); }
      STAMP_AT(2);
      u32 f0 = peek(ws + WS_LNFLAG, rt * 16 + c16);
      a8(); a8();
      WAITV(16);
      KEEP_RAW();
      asm volatile("" : "+v"(f0));
      STAMP_AT(15);
      if (!__all(f0 == mix(e0))) poll(ws + WS_LNFLAG, rt * 16 + c16, mix(e0));
      STAMP_AT(3); ACC_TO(-1);
      bunit(); bunit(); bunit();
#pragma unroll 1
      for (int i = NPK_A + 2; i < 12; ++i) a8();
      LW1(16 + 8 * (10 - NPK_A));
#pragma unroll 1
      for (int r = 1; r < 17; ++r) { LB1(); a8(); if (r & 3) a8(); bunit(); LW1(40); }
      LB1(); bunit();
      LW1(24); LB1();
      const char* tb = P.w2 + xc * 512 + jb * (108 * 4096);
      const u32 tv = (lane >> 2) * 4096 + p4 * 128;
#pragma unroll 1
      for (int i = 0; i < NTCH; ++i) touch(tb + (i == 6 ? 92 : 16 * i) * 4096, LDUM, tv);
      LW1(8 + NTCH); LB1(); LW1(NTCH); LB1();
      BAR();
      STAMP_AT(4); ACC0(11);
      BAR(); BAR();
      const u32 tg = mix(e0);
      int done = 0;
      bool kv = false;
#pragma unroll 1
      for (; done < APRE; ++done) { ahalf(); WAITX(TH_PA); }
#pragma unroll 1
      for (; done < 2 * NA3 && !kv; done += 4) {
        gf = peek(ws + WS_KVFLAG + hg * 2048, c16);
        ahalf(); ahalf(); ahalf(); ahalf();
        WAITV(16);
        asm volatile("" : "+v"(gf));
        kv = __all(gf == tg);
      }
      if (!kv) poll(ws + WS_KVFLAG + hg * 2048, c16, tg);
      STAMP_AT(5);
      gf = peek(gfl, lane < 5 ? lane : 0);
      LBAR();
      WAITV(0);
      asm volatile("" : "+v"(gf));
      const bool ready = __all(gf == tg);
      if (!ready) { LBAR(); poll(gfl, lane < 5 ? lane : 0, tg); }
      STAMP_AT(7); ACC_TO(-1);
      start3(2 * NA3);
      if (ready) LBAR();
      gf = peek(gfl, lane < 5 ? 5 + lane : 5);
#pragma unroll 1
      for (; done < 2 * NA3; done += 2) { ahalf(); ahalf(); }
    }
    const u32 tag = mix(e0);
    {
      auto bunit = bunit3;
      LW3(30); LB3();
      if (!roleA) gf = peek(gfl, lane < 5 ? 5 + lane : 5);
      bunit();
      LW3(30); LB3(); ahalf();
      LW3(25); LB3();
      WAITV(14);
      asm volatile("" : "+v"(gf));
      STAMP_AT(8);
      if (!__all(gf == tag)) poll(gfl, lane < 5 ? 5 + lane : 5, tag);
      STAMP_AT(9);
      bunit(); bunit(); ahalf();
#pragma unroll 1
      for (int r = 3; r < 7; ++r) { LB3(); bunit(); ahalf(); LW3(42); }
      gf = peek(gfl, lane < 6 ? 10 + lane : 10);
#pragma unroll 1
      for (int r = 7; r < 11; ++r) { LB3(); bunit(); ahalf(); LW3(46); }
      asm volatile("" : "+v"(gf));
      if (!__all(gf == tag)) poll(gfl, lane < 6 ? 10 + lane : 10, tag);
#pragma unroll 1
      for (int r = 11; r < 18; ++r) { LB3(); bunit(); ahalf(); if (r > H3) ahalf(); LW3(46); }
      u32 of = peek(ws + WS_OFLAG + st * 2048, c16);
      gf = peek(gfl, lane < 20 ? 16 + lane : 16);
#pragma unroll 1
      for (int r = 18; r < 22; ++r) { LB3(); bunit(); ahalf(); ahalf(); LW3(62); }
      asm volatile("" : "+v"(of), "+v"(gf));
      if (!__all(of == tag && gf == tag)) { poll(ws + WS_OFLAG + st * 2048, c16, tag); poll(gfl, lane < 20 ? 16 + lane : 16, tag); }
      const char* ap = ws + WS_O + st * 81920 + dt3 * 8192;
#pragma unroll 1
      for (int r = 22; r < 32; ++r) {
        LB3(); bunit();
        if (r == 22) bp = ws + WS_WO + dt3 * 112640;
        if (r + dt3 == 32) bp -= 102400;
        glin8<8192, 0, L3_B>(ap, sa3, vl4);
        ap += r + dt3 == 31 ? 8192 - 81920 : 8192;
        LW3(62);
      }
      LB3(); bunit();
      LW3(54); LB3();
      LW3(36); LB3();
      LW3(18); LB3();
      LW3(0); LB3();
    }
    STAMP_AT(10); ACC0(12);
    STAMP_OUT();
    asm volatile(".Lcode_exit_a:" ::: "memory");
    return;
  }

  const int hw = wave >> 1, ty = wave & 1;
  const __amdgpu_buffer_rsrc_t rs = __builtin_amdgcn_make_buffer_rsrc(ws, 0, WS_END, 0x00020000);
  {
    u32 ra, rb; int go, gstep;
    if (hw == 0) {
      const int ch0 = 40 * wave + (lane >> 3);
      ra = rb = L0 + L_LN + (lane & 7) * LN_PITCH + ch0 * 16;
      go = WS_XN + (b >> 4) * 163840 + ((b >> 1) & 7) * 1024 + (b & 1) * 128 + (ch0 >> 2) * 8192 + (ch0 & 3) * 256 + (lane & 7) * 16;
      gstep = 16384;
    } else {
      const int dl = 4 * ty + g;
      ra = L0 + L_LN + (8 + q4) * LN_PITCH + dl * 160 + p4 * 8; rb = ra + p4 * 8;
      go = WS_H + (b >> 2) * 5120 + (b & 3) * 256 + dl * 327680 + c16 * 16;
      gstep = 1024;
    }
    int g1 = go + gstep, g2 = go + 2 * gstep, g3 = go + 3 * gstep, g4 = go + 4 * gstep;
    u32 wa = L0 + L_LN + row * LN_PITCH + hl * 8;
    *(LAS u32*)LCNT = 0;
    asm volatile("" : "+v"(ra), "+v"(rb), "+v"(go), "+v"(g1), "+v"(g2), "+v"(g3), "+v"(g4), "+v"(wa), "+s"(e0));
    f32x2 v[10];
    {
      float u[20];
#define UNP(n, o, i) "s_waitcnt vmcnt(" #n ")\n\tv_lshlrev_b32 %0, 16, %4\n\tv_and_b32 %1, 0xffff0000, %4\n\tv_lshlrev_b32 %2, 16, %5\n\tv_and_b32 %3, 0xffff0000, %5" \
      : "=&v"(u[o]), "=&v"(u[o + 1]), "=&v"(u[o + 2]), "=&v"(u[o + 3]) : "v"(raw[i][0]), "v"(raw[i][1]) : "memory"
      asm volatile(UNP(4, 0, 0)); asm volatile(UNP(3, 4, 1)); asm volatile(UNP(2, 8, 2)); asm volatile(UNP(1, 12, 3)); asm volatile(UNP(0, 16, 4));
#pragma unroll
      for (int i = 0; i < 10; ++i) v[i] = f32x2{u[2 * i], u[2 * i + 1]};
    }
    f32x2 s = v[0] + v[1], s2 = v[2] + v[3];
#pragma unroll
    for (int i = 4; i < 10; i += 2) { s += v[i]; s2 += v[i + 1]; }
    s += s2;
    const float m = sum32(s[0] + s[1]) * (1.f / 640.f);
#pragma unroll
    for (int i = 0; i < 10; ++i) v[i] -= m;
    f32x2 qq = v[0] * v[0], q2 = v[1] * v[1];
#pragma unroll
    for (int i = 2; i < 10; i += 2) { qq += v[i] * v[i]; q2 += v[i + 1] * v[i + 1]; }
    qq += q2;
    const float r1 = __builtin_amdgcn_rsqf(sum32(qq[0] + qq[1]) * (1.f / 640.f) + 1e-5f);
#pragma unroll
    for (int i = 0; i < 10; ++i) { v[i] *= r1; asm("" : "+v"(v[i])); }
    s = v[0]; qq = v[0] * v[0];
#pragma unroll
    for (int i = 1; i < 10; ++i) { s += v[i]; qq += v[i] * v[i]; }
#pragma unroll
    for (int i = 0; i < 5; ++i) *(LAS u32x2*)(wa + i * 256) = u32x2{pk2(v[2 * i][0], v[2 * i][1]), pk2(v[2 * i + 1][0], v[2 * i + 1][1])};
    const float m2 = sum32(s[0] + s[1]) * (1.f / 640.f);
    const float r2 = __builtin_amdgcn_rsqf(sum32(qq[0] + qq[1]) * (1.f / 640.f) - m2 * m2 + 1e-5f);
    const float mr = -m2 * r2;
#pragma unroll
    for (int i = 0; i < 10; ++i) v[i] = __builtin_elementwise_fma(v[i], f32x2{r2, r2}, f32x2{mr, mr});
#pragma unroll
    for (int i = 0; i < 5; ++i) *(LAS u32x2*)(wa + 8 * LN_PITCH + i * 256) = u32x2{pk2(v[2 * i][0], v[2 * i][1]), pk2(v[2 * i + 1][0], v[2 * i + 1][1])};
    STAMP_AT(1);
    BAR();
    s16x8 f[5];
    if (hw == 0) {
#pragma unroll
      for (int i = 0; i < 5; ++i) f[i] = ld128(ra + i * 128);
    } else {
#pragma unroll
      for (int j = 0; j < 4; ++j) f[j] = cat(ldtr(rb + (j >> 1) * 64 + (j & 1) * 8), ldtr(rb + (j >> 1) * 64 + (j & 1) * 8 + 4 * LN_PITCH));
      f[4] = cat(ldtr(ra + 128), ldtr(ra + 128 + 4 * LN_PITCH));
    }
    st128(rs, go, f[0]); st128(rs, g1, f[1]); st128(rs, g2, f[2]); st128(rs, g3, f[3]); st128(rs, g4, f[4]);
    BAR();
    publish_last(ws + WS_LNFLAG + b * 128, mix(e0), lane, LCNT, 4);
    STAMP_AT(2);
    BAR();
  }
  const u32 tag = mix(e0);
  const u32 xo = (st * 64 + wave * 16 + c16) * 1280 + dt3 * 160 + g * 16;
  u32x4 x01, x23; u32x2 x4; u32 bs;
  auto ldx = [&]() {
    x01 = *(const GAS u32x4*)(P.x + xo); x23 = *(const GAS u32x4*)(P.x + xo + 64);
    x4 = *(const GAS u32x2*)(P.x + (xo + 128 - g * 8));
    bs = *(const GAS unsigned short*)(P.b2 + st * 128 + wave * 32 + (u32)(c16 * 2));
  };
  if (roleA) {
    f32x4 aq[5][2], akv[5][4];
#pragma unroll
    for (int j = 0; j < 5; ++j) {
      aq[j][0] = aq[j][1] = f32x4{1.f, 1.f, 1.f, 1.f};
#pragma unroll
      for (int i = 0; i < 4; ++i) { const float iv = 4 * j + i < 10 ? 1.f : 0.f; akv[j][i] = f32x4{iv, iv, iv, iv}; }
    }
    const u32 wa0 = L0 + ((g >> 1) * 16 + (g & 1) * 4 + q4) * 480 + p4 * 8;
    const u32 xa0 = L0 + LQ_B + wave * 2048 + lane * 16;
    const int c32 = lane & 31, hh = lane >> 5;
    const int pb = WS_PART + (hg * 4 + wave) * (16 * 896);
    u32 vp0 = L0 + L_KV + (8 * hw + 4 * ty + g) * LN_PITCH + c16 * 16 + ty * 768, vp1 = vp0 + (1 - 2 * ty) * (4 * LN_PITCH);
    u32 ka = L0 + L_KV + hh * LN_PITCH + (20 * wave + (c32 < 20 ? c32 : 19)) * 16;
    u32 vb = L0 + L_KV + (16 + hh) * LN_PITCH + (c32 < 20 ? 20 * wave + c32 : 80) * 16;
    int so0 = c32 <= 20 ? pb + rt * 896 + (hh * 21 + c32) * 16 : OOB, so1 = lane <= 21 ? pb + rt * 896 + 672 + lane * 8 : OOB;
    u32 vp2 = vp0 - ty * 768 + 512, von = L0 + L_KV + (16 + 4 * wave + g) * LN_PITCH + 1280;
    asm volatile("" : "+v"(vp2), "+v"(von), "+v"(vp0), "+v"(vp1), "+v"(ka), "+v"(vb), "+v"(so0), "+v"(so1));
    u32 qa[5][3], za[5];
#pragma unroll
    for (int je = 0; je < 5; ++je) {
      const int e = 16 * je + c16, he = e / 20, el = e - 20 * he;
#pragma unroll
      for (int x = 0; x < 3; ++x) {
        const int dl = 4 * (je - 1 + x) + g - 5 * he;
        const int qi = dl < 4 ? ((dl & 1) * 21 + el) * 2 + (dl >> 1) : 84 + el;
        qa[je][x] = L0 + L_FIN + (dl >= 0 && dl < 5 ? he * 848 + qi * 8 : 3392);
        if (je - 1 + x >= 0 && je - 1 + x < 5) asm volatile("" : "+v"(qa[je][x]));
      }
      const int dl = 4 * je + g - 5 * p4;
      const int qi = dl < 4 ? ((dl & 1) * 21 + 20) * 2 + (dl >> 1) : 104;
      za[je] = L0 + L_FIN + (dl >= 0 && dl < 5 ? p4 * 848 + qi * 8 : 3392);
      asm volatile("" : "+v"(za[je]));
    }
    u32x4 sel;
    {
      const int r = (c32 & 3) + 4 * (c32 >> 3);
      const u32 one = c32 < 16 && ((c32 >> 2) & 1) == hh ? 0x3f80u << (16 * (r & 1)) : 0u;
#pragma unroll
      for (int i = 0; i < 4; ++i) sel[i] = (r >> 1) == i ? one : 0u;
    }
    int go = lane < 53 ? pb + lane * 16 : OOB;
    u32 fa = L0 + L_FIN + (lane < 53 ? wave * 848 + lane * 16 : 3392);
    int ob = WS_O + (2 * rt + hw) * 81920 + hg * 10240 + ty * 1024 + lane * 16;
    asm volatile("" : "+v"(sel), "+v"(go), "+v"(fa), "+v"(ob));
    s16x8 xA[4] = {}, xB[4], wq[5] = {};
    u32 nwa = wa0, nwk = wa0 + ty * 96, nxa = xa0, nxb = xa0 ^ 2048;
    auto round = [&](int r, s16x8(&xo)[4], s16x8(&xn)[4]) {
      const u32 wa = nwa, wk = nwk, xa = nxa, xb = nxb;
      BAR();
      s16x8 w[6];
      xn[0] = ld128(xa); w[0] = cat(ldtr(wk + 160), ldtr(wk + 160 + 3840));
      xn[1] = ld128(xa + 1024); xn[2] = ld128(xb); xn[3] = ld128(xb + 1024);
      w[1] = cat(ldtr(wk + 192), ldtr(wk + 192 + 3840));
      w[3] = cat(ldtr(wk + 320), ldtr(wk + 320 + 3840));
#pragma unroll
      for (int j = 0; j < 5; ++j) { aq[j][0] = mfma32(wq[j], xo[0], aq[j][0]); aq[j][1] = mfma32(wq[j], xo[1], aq[j][1]); }
#pragma unroll
      for (int j = 0; j < 10; ++j) { SG(0x8, 1); SG(0x100, 1); }
      SB();
      w[4] = cat(ldtr(wk + 352), ldtr(wk + 352 + 3840));
      w[2] = cat(ldtr(wa + 224), ldtr(wa + 224 + 3840));
      w[5] = cat(ldtr(wa + 384), ldtr(wa + 384 + 3840));
#pragma unroll
      for (int j = 0; j < 5; ++j) wq[j] = cat(ldtr(wa + j * 32), ldtr(wa + 3840 + j * 32));
      nwa = wa0 + ((r + 1) & 7) * 15360; nwk = nwa + ty * 96; nxa = xa0 + ((r + 1) & 3) * 8192; nxb = nxa ^ 2048;
#pragma unroll
      for (int j = 0; j < 5; ++j) {
        if (j == 2) continue;
#pragma unroll
        for (int i = 0; i < 4; ++i) akv[j][i] = mfma32(xn[i], w[j], akv[j][i]);
      }
#pragma unroll
      for (int i = 0; i < 4; ++i) akv[2][i] = mfma32(xn[i & 1], w[i < 2 ? 2 : 5], akv[2][i]);
#pragma unroll
      for (int j = 0; j < 2; ++j) { SG(0x8, 1); SG(0x100, 2); }
#pragma unroll
      for (int j = 0; j < 12; ++j) { SG(0x8, 1); SG(0x100, 1); }
#pragma unroll
      for (int j = 0; j < 5; ++j) { SG(0x8, 1); SG(0x6, 2); }
      SG(0x8, 1);
      asm volatile("" : "+v"(nwa), "+v"(nwk), "+v"(nxa), "+v"(nxb));
      SB();
    };
#pragma unroll 1
    for (int r = 0; r < 20; r += 2) { round(r, xA, xB); round(r + 1, xB, xA); }
    BAR();
    STAMP_AT(3);
    {
      SB();
      *(LAS s16x8*)(vp2 + 16 * LN_PITCH) = cat(pk4(akv[2][2]), pk4(akv[2][3]));
      *(LAS s16x8*)(von) = s16x8{0x3f80, 0x3f80, 0x3f80, 0x3f80, 0x3f80, 0x3f80, 0x3f80, 0x3f80};
#pragma unroll
      for (int j = 3; j < 5; ++j) {
        *(LAS s16x8*)(vp0 + 16 * LN_PITCH + (j - 3) * 256) = cat(pk4(akv[j][0]), pk4(akv[j][1]));
        *(LAS s16x8*)(vp1 + 16 * LN_PITCH + (j - 3) * 256) = cat(pk4(akv[j][2]), pk4(akv[j][3]));
      }
      SB();
#pragma unroll
      for (int j = 0; j < 2; ++j) {
        s16x4 o[4];
        phi<4>(akv[j], o);
        *(LAS s16x8*)(vp0 + j * 256) = cat(o[0], o[1]);
        *(LAS s16x8*)(vp1 + j * 256) = cat(o[2], o[3]);
        SB();
      }
      const f32x4 k2[2] = {akv[2][0], akv[2][1]};
      s16x4 o[2];
      phi<2>(k2, o);
      *(LAS s16x8*)(vp2) = cat(o[0], o[1]);
    }
    STAMP_AT(4);
    BAR();
    STAMP_AT(12);
    {
      s16x8 fk[8], fv[8];
#pragma unroll
      for (int i = 0; i < 8; ++i) { fk[i] = ld128(ka + i * 2 * LN_PITCH); fv[i] = ld128(vb + i * 2 * LN_PITCH); }
      SB();
      f32x16 d = {};
#pragma unroll
      for (int i = 0; i < 8; ++i) d = mfmaw(fk[i], fv[i], d);
      SB();
#pragma unroll
      for (int j = 0; j < 2; ++j) { aq[j][0] = mfma32(wq[j], xA[0], aq[j][0]); aq[j][1] = mfma32(wq[j], xA[1], aq[j][1]); }
      SB();
      const u32x4 lo = {pk2(d[0], d[1]), pk2(d[2], d[3]), pk2(d[4], d[5]), pk2(d[6], d[7])};
      const u32x2 hi = {pk2(d[8], d[9]), pk2(d[10], d[11])};
      st128(rs, so0, __builtin_bit_cast(s16x8, lo));
      st64(rs, so1, __builtin_bit_cast(s16x4, hi));
      SB();
    }
    STAMP_AT(13);
    BAR();
#pragma unroll
    for (int j = 2; j < 5; ++j) { aq[j][0] = mfma32(wq[j], xA[0], aq[j][0]); aq[j][1] = mfma32(wq[j], xA[1], aq[j][1]); }
    SB();
    s16x4 pq[5][2];
#pragma unroll
    for (int j = 0; j < 2; ++j) { phi<2>(aq[j], pq[j]); asm volatile("" : "+v"(pq[j][0]), "+v"(pq[j][1])); }
    STAMP_AT(14);
    publish_last(ws + WS_KVFLAG + hg * 2048 + rt * 128, tag, lane, LCNT, 8);
    STAMP_AT(5);
    ldx();
    SB();
    phi<2>(aq[2], pq[2]);
    asm volatile("" : "+v"(pq[2][0]), "+v"(pq[2][1]));
    BAR();
    STAMP_AT(6);
    s16x8 p01[2], p23[2], p34[2];
    {
      u32x4 pr[16];
#pragma unroll
      for (int i = 0; i < 16; ++i) pr[i] = __builtin_amdgcn_raw_buffer_load_b128(rs, go + (i & 3) * 896, (i >> 2) * 3584, 16);
      SB();
#pragma unroll
      for (int j = 3; j < 5; ++j) phi<2>(aq[j], pq[j]);
#pragma unroll
      for (int i = 0; i < 2; ++i) {
        p01[i] = cat(pq[0][i], pq[1][i]); p23[i] = cat(pq[2][i], pq[3][i]); p34[i] = cat(pq[3][i], pq[4][i]);
        asm volatile("" : "+v"(p01[i]), "+v"(p23[i]), "+v"(p34[i]));
      }
      SB();
      f32x16 d = {};
#pragma unroll
      for (int i = 0; i < 16; ++i) d = mfmaw(__builtin_bit_cast(s16x8, sel), __builtin_bit_cast(s16x8, pr[i]), d);
      *(LAS u32x4*)fa = u32x4{pk2(d[0], d[1]), pk2(d[2], d[3]), pk2(d[4], d[5]), pk2(d[6], d[7])};
    }
    STAMP_AT(7);
    BAR();
    STAMP_AT(15);
    {
      const u32 z0 = L0 + L_FIN + 3392;
      const s16x8 z01 = cat(ld64(za[0]), ld64(za[1])), z23 = cat(ld64(za[2]), ld64(za[3])), z4 = cat(ld64(z0), ld64(za[4]));
      const s16x8 k0 = cat(ld64(qa[0][1]), ld64(qa[0][2])), k1a = cat(ld64(qa[1][0]), ld64(qa[1][1])), k2a = cat(ld64(qa[2][1]), ld64(qa[2][2]));
      const s16x8 k3a = cat(ld64(qa[3][0]), ld64(qa[3][1])), k4 = cat(ld64(qa[4][0]), ld64(qa[4][1]));
      const s16x4 zz = {0, 0, 0, 0};
      const s16x8 k1b = cat(ld64(qa[1][2]), zz), k2b = cat(zz, ld64(qa[2][0])), k3b = cat(zz, ld64(qa[3][2]));
      SB();
      f32x4 den[2], o[5][2];
      const f32x4 e4 = {1e-6f, 1e-6f, 1e-6f, 1e-6f}, n4 = {0.f, 0.f, 0.f, 0.f};
#pragma unroll
      for (int i = 0; i < 2; ++i) den[i] = mfma32(z01, p01[i], e4);
#pragma unroll
      for (int i = 0; i < 2; ++i) den[i] = mfma32(z23, p23[i], den[i]);
#pragma unroll
      for (int i = 0; i < 2; ++i) den[i] = mfma32(z4, p34[i], den[i]);
#pragma unroll
      for (int i = 0; i < 2; ++i) { o[0][i] = mfma32(k0, p01[i], n4); o[1][i] = mfma32(k1a, p01[i], n4); }
      SB();
      float rc[5][2];
#pragma unroll
      for (int i = 0; i < 2; ++i) {
        const float r0 = __builtin_amdgcn_rcpf(den[i][0]), r1 = __builtin_amdgcn_rcpf(den[i][1]), r2 = __builtin_amdgcn_rcpf(den[i][2]), r3 = __builtin_amdgcn_rcpf(den[i][3]);
        rc[0][i] = r0; rc[1][i] = g < 1 ? r0 : r1; rc[2][i] = g < 2 ? r1 : r2; rc[3][i] = g < 3 ? r2 : r3; rc[4][i] = r3;
      }
#pragma unroll
      for (int i = 0; i < 2; ++i) { o[1][i] = mfma32(k1b, p23[i], o[1][i]); o[2][i] = mfma32(k2a, p23[i], n4); o[3][i] = mfma32(k3a, p23[i], n4); }
#pragma unroll
      for (int i = 0; i < 2; ++i) { o[2][i] = mfma32(k2b, p01[i], o[2][i]); o[3][i] = mfma32(k3b, p34[i], o[3][i]); o[4][i] = mfma32(k4, p34[i], n4); }
#pragma unroll
      for (int i = 0; i < 12; ++i) { SG(0x8, 1); SG(0x402, 1); }
      SB();
#pragma unroll
      for (int je = 0; je < 5; ++je) { st128(rs, ob + je * 2048, cat(pk4(o[je][0] * rc[je][0]), pk4(o[je][1] * rc[je][1]))); SB(); }
    }
  } else {
    const bool live = !narrow || wave < 2;
    if (!live) {
      if (b == 192 && wave == 2) {
        const GAS u32* fp = (const GAS u32*)(ws + WS_LNFLAG + (u32)(lane * 128));
#pragma unroll 1
        for (u32 sp = 0; sp < SPIN_CAP; ++sp) {
          const u32 f0 = __hip_atomic_load(fp, RLX_AGENT), f1 = __hip_atomic_load(fp + 2048, RLX_AGENT);
          const u32 f2 = __hip_atomic_load(fp + 4096, RLX_AGENT), f3 = __hip_atomic_load(fp + 6144, RLX_AGENT);
          if (__all(f0 == tag && f1 == tag && f2 == tag && f3 == tag)) break;
        }
        asm volatile("" ::: "memory");
        if (lane < 2) __hip_atomic_store((GAS u32*)(ws + (lane ? WS_EPOCH : WS_ALLFLAG)), tag, RLX_AGENT);
      }
      BAR();
      const int d8 = 4 * (dt & 1) + 2 * ty, ks = xc * 4 + (dt >> 1);
#pragma unroll 1
      for (int i = 0; i < 2; ++i) {
        const u32 ra = L0 + 7 * 8192 + (4 * g + q4) * 640 + (2 * ty + i) * 160 + p4 * 8, rb = ra + p4 * 8;
        s16x8 f[5];
#pragma unroll
        for (int j = 0; j < 4; ++j) f[j] = cat(ldtr(rb + (j >> 1) * 64 + (j & 1) * 8), ldtr(rb + (j >> 1) * 64 + (j & 1) * 8 + 10240));
        f[4] = cat(ldtr(ra + 128), ldtr(ra + 128 + 10240));
        const int go = WS_WO + (d8 + i) * 102400 + ks * 5120 + lane * 16;
        BAR();
#pragma unroll
        for (int j = 0; j < 5; ++j) st128(rs, go + j * 1024, f[j]);
        asm volatile("s_nop 1" ::: "memory");
      }
#pragma unroll 1
      for (int r = 3; r < 33; ++r) BAR();
      WAITV(0);
      if (lane < 2) __hip_atomic_store((GAS u32*)(ws + WS_GFLAG + d8 * 8192 + (16 + ks) * 128 + (u32)(lane * 8192)), tag, RLX_AGENT);
    } else {
    f32x4 acc[2][5];
#pragma unroll
    for (int t = 0; t < 2; ++t) {
      const u32x2 bb = *(const GAS u32x2*)(P.b1 + fu * 128 + wave * 64 + (u32)(t * 32 + g * 8));
      const f32x4 bi = {blo(bb[0]), bhi(bb[0]), blo(bb[1]), bhi(bb[1])};
#pragma unroll
      for (int j = 0; j < 5; ++j) acc[t][j] = bi;
    }
    s16x8 a1[2] = {}, h1[5] = {};
    auto kloop = [&](auto rp) {
    constexpr int RP = decltype(rp)::value;
    const u32 aa0 = L0 + (8 * g + q4) * RP + ((2 * wave) ^ (RP == 256 ? q4 | ((g & 1) << 2) : (q4 >> 1) | ((g & 1) << 1))) * 32 + p4 * 8;
    const u32 ha0 = L0 + L1_B + lane * 16;
    u32 as = RP == 256 ? 12 * 4096 : 20 * 4096, hs = 0, naa = aa0 + as, nab = naa ^ 32, nha = ha0;
#pragma unroll 1
    for (int r = 0; r < 32; ++r) {
      const u32 aa = naa, ab = nab, ha = nha;
      BAR();
      s16x8 a0[2], h0[5];
      a0[0] = cat(ldtr(aa), ldtr(aa + 4 * RP)); h0[0] = ld128(ha); a0[1] = cat(ldtr(ab), ldtr(ab + 4 * RP));
#pragma unroll
      for (int j = 1; j < 5; ++j) h0[j] = ld128(ha + j * 1024);
#pragma unroll
      for (int j = 0; j < 5; ++j) { acc[0][j] = mfma32(a1[0], h1[j], acc[0][j]); acc[1][j] = mfma32(a1[1], h1[j], acc[1][j]); }
#pragma unroll
      for (int j = 0; j < 9; ++j) { SG(0x8, 1); SG(0x100, 1); }
      SG(0x8, 1);
      SB();
      a1[0] = cat(ldtr(aa + 32 * RP), ldtr(aa + 36 * RP)); a1[1] = cat(ldtr(ab + 32 * RP), ldtr(ab + 36 * RP));
#pragma unroll
      for (int j = 0; j < 5; ++j) h1[j] = ld128(ha + 5120 + j * 1024);
      as = as == L1_B - 64 * RP ? 0 : as + 64 * RP;
      hs = hs == (NB1 - 1) * 10240 ? 0 : hs + 10240;
      naa = aa0 + as; nab = naa ^ 32; nha = ha0 + hs;
#pragma unroll
      for (int j = 0; j < 5; ++j) { acc[0][j] = mfma32(a0[0], h0[j], acc[0][j]); acc[1][j] = mfma32(a0[1], h0[j], acc[1][j]); }
      SG(0x100, 1);
#pragma unroll
      for (int j = 0; j < 4; ++j) { SG(0x8, 1); SG(0x100, 2); }
#pragma unroll
      for (int j = 0; j < 5; ++j) { SG(0x8, 1); SG(0x6, 2); }
      SG(0x8, 1);
      asm volatile("" : "+v"(naa), "+v"(nab), "+v"(nha));
      SB();
    }
    };
    if (narrow) kloop(IC<128>{}); else kloop(IC<256>{});
    BAR();
    STAMP_AT(3);
#pragma unroll
    for (int j = 0; j < 5; ++j) { acc[0][j] = mfma32(a1[0], h1[j], acc[0][j]); acc[1][j] = mfma32(a1[1], h1[j], acc[1][j]); }
      const int go = WS_G + dt * 276480 + (fu * 2 + wave) * 5120 + lane * 16;
#pragma unroll
      for (int j = 0; j < 5; ++j) st128(rs, go + j * 1024, gelu8(acc[0][j], acc[1][j]));
      STAMP_AT(4);
    }
    if (!late) {
      publish_last(ws + WS_GFLAG + dt * 8192 + gi * 128, tag, lane, LCNT, 8);
      STAMP_AT(5);
      BAR();
    }
    ldx();
  }

  STAMP_AT(8);
  {
    f32x4 acc[2][5] = {};
    const u32 aa0 = L0 + (32 * ty + 4 * g + q4) * 128 + (wave ^ ((2 * g + (q4 >> 1)) & 3)) * 32 + p4 * 8;
    const u32 ga0 = L0 + L3_B + ty * 5120 + lane * 16;
    const u32 oa0 = L0 + ty * 4096 + hw * 1024 + lane * 16 + ty * 8;
    u32 sa = 0, sb = 0, naa = aa0, nab = aa0 ^ 32, nga = ga0;
    struct FA { s16x8 a[2], g[5]; };
    auto lda = [&](FA& f, bool tr) {
      const u32 aa = naa, ab = nab, ga = nga;
      BAR();
      if (tr) { f.a[1] = cat(ldtr(ab), ldtr(ab + 2048)); f.a[0] = cat(ldtr(aa), ldtr(aa + 2048)); }
      else { f.a[1] = cat(ld64(ab), ld64(ab + 2048)); f.a[0] = cat(ld64(aa), ld64(aa + 2048)); }
#pragma unroll
      for (int j = 0; j < 5; ++j) f.g[j] = ld128(ga + j * 1024);
      sa = sa == L3_B - 8192 ? 0 : sa + 8192;
      sb = sb == LA3 * 10240 ? 0 : sb + 10240;
      naa = (tr ? aa0 : oa0) + sa; nab = naa ^ (tr ? 32 : 8); nga = ga0 + sb;
    };
    auto mma = [&](const FA& f) {
#pragma unroll
      for (int j = 0; j < 5; ++j) acc[1][j] = mfma32(f.g[j], f.a[1], acc[1][j]);
#pragma unroll
      for (int j = 0; j < 5; ++j) acc[0][j] = mfma32(f.g[j], f.a[0], acc[0][j]);
    };
    auto pat = [&]() {
      SG(0x100, 1);
#pragma unroll
      for (int j = 0; j < 4; ++j) { SG(0x8, 1); SG(0x100, 2); }
#pragma unroll
      for (int j = 0; j < 5; ++j) { SG(0x8, 1); SG(0x6, 2); }
      SG(0x8, 1);
      asm volatile("" : "+v"(naa), "+v"(nab), "+v"(nga));
      SB();
    };
    FA f0, f1;
    lda(f0, true); SB();
#pragma unroll 1
    for (int r = 1; r < PUBR; r += 2) { lda(f1, true); mma(f0); pat(); lda(f0, true); mma(f1); pat(); }
    {
      asm volatile("" : "+v"(x01), "+v"(x23), "+v"(x4), "+v"(bs));
      const float bf = blo(bs);
      acc[0][0] += f32x4{blo(x01[0]), bhi(x01[0]), blo(x01[1]), bhi(x01[1])} + bf;
      acc[0][1] += f32x4{blo(x01[2]), bhi(x01[2]), blo(x01[3]), bhi(x01[3])} + bf;
      acc[0][2] += f32x4{blo(x23[0]), bhi(x23[0]), blo(x23[1]), bhi(x23[1])} + bf;
      acc[0][3] += f32x4{blo(x23[2]), bhi(x23[2]), blo(x23[3]), bhi(x23[3])} + bf;
      acc[0][4] += f32x4{blo(x4[0]), bhi(x4[0]), blo(x4[1]), bhi(x4[1])} + bf;
      if (late) publish_last(ws + WS_GFLAG + dt * 8192 + gi * 128, tag, lane, LCNT, 8);
      else if (roleA) publish(ws + WS_OFLAG + (2 * rt + hw) * 2048 + (ty * 8 + hg) * 128, tag, lane);
      SB();
    }
#pragma unroll 1
    for (int r = PUBR; r < 27; r += 2) { lda(f1, true); mma(f0); pat(); lda(f0, true); mma(f1); pat(); }
    STAMP_AT(9);
    naa = oa0 + sa; nab = naa ^ 8;
#pragma unroll 1
    for (int r = 27; r < 37; r += 2) { lda(f1, false); mma(f0); pat(); lda(f0, false); mma(f1); pat(); }
    mma(f0);
    const u32 ea = L0 + L3_B + 20480 + lane * 16;
    static_assert(36 % NB3 < 2, "");
#pragma unroll
    for (int j = 0; j < 5; ++j) *(LAS f32x4*)(ea + wave * 5120 + j * 1024) = acc[1][j];
    SG(0x8, 8); SG(0x200, 2); SG(0x8, 2); SG(0x200, 3);
    SB();
    BAR();
#pragma unroll
    for (int j = 0; j < 5; ++j) acc[0][j] += *(const LAS f32x4*)(ea + (wave ^ 1) * 5120 + j * 1024);
    *(GAS s16x8*)(P.out + xo) = cat(pk4(acc[0][0]), pk4(acc[0][1]));
    *(GAS s16x8*)(P.out + xo + 64) = cat(pk4(acc[0][2]), pk4(acc[0][3]));
    *(GAS s16x4*)(P.out + (xo + 128 - g * 8)) = pk4(acc[0][4]);
    asm volatile(".Lcode_exit_b:" ::: "memory");
  }
}

extern "C" void kernel_launch(void* const* d_in, const int* in_sizes, int n_in, void* d_out, int out_size, void* d_ws,
                              size_t ws_size, hipStream_t stream) {
  Params p;
  p.x = (const char*)d_in[0];
  p.wqkv = (const char*)d_in[1];
  p.wo = (const char*)d_in[2];
  p.w1 = (const char*)d_in[4];
  p.b1 = (const char*)d_in[5];
  p.w2 = (const char*)d_in[6];
  p.b2 = (const char*)d_in[7];
  p.out = (char*)d_out;
  p.ws = (char*)d_ws;
  (void)hipFuncSetAttribute((const void*)fused_fwd, hipFuncAttributeMaxDynamicSharedMemorySize, L_BYTES);
  fused_fwd<<<256, 320, L_BYTES, stream>>>(p);
}
```

```cpp
#include <hip/hip_runtime.h>
#include <hip/hip_bf16.h>
#pragma clang diagnostic ignored "-Wint-to-pointer-cast"

#define GAS __attribute__((address_space(1)))
#define LAS __attribute__((address_space(3)))
#define DI __device__ __forceinline__
typedef unsigned u32;
typedef unsigned long long u64;
typedef short s16x4 __attribute__((ext_vector_type(4)));
typedef short s16x8 __attribute__((ext_vector_type(8)));
typedef __bf16 b16x2 __attribute__((ext_vector_type(2)));
typedef __bf16 b16x8 __attribute__((ext_vector_type(8)));
typedef float f32x2 __attribute__((ext_vector_type(2)));
typedef float f32x4 __attribute__((ext_vector_type(4)));
typedef float f32x16 __attribute__((ext_vector_type(16)));
typedef unsigned u32x2 __attribute__((ext_vector_type(2)));
typedef unsigned u32x4 __attribute__((ext_vector_type(4)));

constexpr int WS_EPOCH = 0;
constexpr int WS_ALLFLAG = 1024;
constexpr int WS_LNFLAG = 196608;
constexpr int WS_GFLAG = 720896;
constexpr int WS_KVFLAG = 65536;
constexpr int WS_OFLAG = 131072;
constexpr int WS_PART = 262144;
constexpr int WS_SC = 8192;
constexpr int WS_H = 786432;
constexpr int WS_G = WS_H + 2621440;
constexpr int WS_O = WS_G + 2211840;
constexpr int WS_WO = WS_O + 2621440;
constexpr int WS_END = WS_WO + 819200;
constexpr int OOB = 0x40000000;
constexpr int L_BYTES = 163840;
constexpr int L_LN = 131072, LN_PITCH = 1296;
constexpr int NB3 = 5;
constexpr int NB1 = 4, NA1 = 7;
constexpr int LA3 = NB3 - 1, NA3 = 18 - NB3, H3 = 2 * (NA3 - LA3 - 2);
constexpr int LQ_B = 122880, L1_B = NA1 * 16384, L3_B = NA3 * 8192;
constexpr int L_KV = L3_B, L_FIN = L3_B + NB3 * 10240, L_SC = L_FIN + 3584;
constexpr int PRE_A = 2;
constexpr int APRE = 20, NPK_A = 8, R_JOIN = 9, LPK = 22, NTCH = 7, EARLY = 16, PUBR = 3, TH_PA = 10, TH_P3 = 8, SLP = 2, MID_A = 2, PRE_B = 3, MID_B = 3, XP = 1;
constexpr u32 SPIN_CAP = 1u << 16;

#define BAR() do { asm volatile("" ::: "memory"); __builtin_amdgcn_s_waitcnt(0xC07F); asm volatile("s_barrier" ::: "memory"); } while (0)
#define WAITV(n) asm volatile("s_waitcnt vmcnt(" #n ")" ::: "memory")
#define WAITX(n) asm volatile("s_waitcnt vmcnt(%0)" ::"i"((n) > 63 ? 63 : (n)) : "memory")
#define LBAR() asm volatile("s_barrier" ::: "memory")
#define RLX_AGENT __ATOMIC_RELAXED, __HIP_MEMORY_SCOPE_AGENT

DI f32x4 mfma32(s16x8 a, s16x8 b, f32x4 c) {
  return __builtin_amdgcn_mfma_f32_16x16x32_bf16(__builtin_bit_cast(b16x8, a), __builtin_bit_cast(b16x8, b), c, 0, 0, 0);
}
DI f32x16 mfmaw(s16x8 a, s16x8 b, f32x16 c) {
  return __builtin_amdgcn_mfma_f32_32x32x16_bf16(__builtin_bit_cast(b16x8, a), __builtin_bit_cast(b16x8, b), c, 0, 0, 0);
}
DI s16x4 ldtr(u32 a) { return __builtin_amdgcn_ds_read_tr16_b64_v4i16((LAS s16x4*)a); }
DI s16x8 ld128(u32 a) { return *(const LAS s16x8*)a; }
DI s16x4 ld64(u32 a) { return *(const LAS s16x4*)a; }
DI s16x8 cat(s16x4 a, s16x4 b) { return __builtin_shufflevector(a, b, 0, 1, 2, 3, 4, 5, 6, 7); }
DI u32 pk2(float a, float b) { f32x2 v = {a, b}; return __builtin_bit_cast(u32, __builtin_convertvector(v, b16x2)); }
DI s16x4 pk4(f32x4 v) { u32x2 r = {pk2(v[0], v[1]), pk2(v[2], v[3])}; return __builtin_bit_cast(s16x4, r); }
DI float blo(u32 u) { return __uint_as_float(u << 16); }
DI float bhi(u32 u) { return __uint_as_float(u & 0xffff0000u); }
template <int C> DI float dpp_add(float s) { return s + __int_as_float(__builtin_amdgcn_update_dpp(0, __float_as_int(s), C, 0xf, 0xf, true)); }
DI float sum32(float s) {
  s = dpp_add<0x121>(s); s = dpp_add<0x122>(s); s = dpp_add<0x124>(s); s = dpp_add<0x128>(s);
  auto r = __builtin_amdgcn_permlane16_swap(__float_as_uint(s), __float_as_uint(s), false, false);
  return __uint_as_float(r[0]) + __uint_as_float(r[1]);
}
#define SG(m, n) __builtin_amdgcn_sched_group_barrier(m, n, 0)
#define SB() __builtin_amdgcn_sched_barrier(0)
#define AL() do { asm volatile(".p2align 3"); SB(); } while (0)
template <int N> DI void phi(const f32x4 (&u)[N], const f32x4* sc, s16x4 (&o)[N]) {
  const float L = 1.4426950408889634f;
  f32x4 a[N], y[N];
  SB(); AL();
#pragma unroll
  for (int i = 0; i < N; ++i) a[i] = __builtin_elementwise_fma(u[i], sc[i], f32x4{1.f, 1.f, 1.f, 1.f});
  SB();
#pragma unroll
  for (int i = 0; i < N; ++i) y[i] = __builtin_elementwise_fma(a[i], f32x4{L, L, L, L}, f32x4{-L, -L, -L, -L});
  SB();
#pragma unroll
  for (int i = 0; i < 4 * N; ++i) y[i >> 2][i & 3] = __builtin_amdgcn_fmed3f(__builtin_amdgcn_exp2f(y[i >> 2][i & 3]), 0.f, 1.f);
  SB();
#pragma unroll
  for (int i = 0; i < 4 * N; ++i) y[i >> 2][i & 3] = __int_as_float(max(__float_as_int(a[i >> 2][i & 3]), __float_as_int(y[i >> 2][i & 3])));
  SB();
#pragma unroll
  for (int i = 0; i < N; ++i) y[i] += 1e-6f;
  SB();
#pragma unroll
  for (int i = 0; i < N; ++i) o[i] = pk4(y[i]);
}
DI s16x8 gelu8(f32x4 u0, f32x4 u1) {
  const float K = -2.f * 0.7978845608028654f * 1.4426950408889634f, K3 = K * 0.044715f;
  SB(); AL();
  f32x4 y0 = u0 * u0, y1 = u1 * u1;
  SB();
  y0 = __builtin_elementwise_fma(y0, f32x4{K3, K3, K3, K3}, f32x4{K, K, K, K});
  y1 = __builtin_elementwise_fma(y1, f32x4{K3, K3, K3, K3}, f32x4{K, K, K, K});
  SB();
  y0 *= u0; y1 *= u1;
  SB();
#pragma unroll
  for (int i = 0; i < 4; ++i) y0[i] = __builtin_amdgcn_exp2f(y0[i]);
#pragma unroll
  for (int i = 0; i < 4; ++i) y1[i] = __builtin_amdgcn_exp2f(y1[i]);
  SB();
  y0 += 1.f; y1 += 1.f;
  SB();
#pragma unroll
  for (int i = 0; i < 4; ++i) y0[i] = __builtin_amdgcn_rcpf(y0[i]);
#pragma unroll
  for (int i = 0; i < 4; ++i) y1[i] = __builtin_amdgcn_rcpf(y1[i]);
  SB();
  y0 *= u0; y1 *= u1;
  SB();
  return cat(pk4(y0), pk4(y1));
}
DI void st128(__amdgpu_buffer_rsrc_t rs, int off, s16x8 v) { __builtin_amdgcn_raw_buffer_store_b128(__builtin_bit_cast(u32x4, v), rs, off, 0, 16); }
DI void st64(__amdgpu_buffer_rsrc_t rs, int off, s16x4 v) { __builtin_amdgcn_raw_buffer_store_b64(__builtin_bit_cast(u32x2, v), rs, off, 0, 16); }
DI u32 mix(u32 e) { return e * 0x9E3779B1u + 0x7F4A7C15u; }

#define GL(v, off, sc) "global_load_lds_dwordx4 " v ", %0 offset:" #off sc "\n\t"
#define M0(bias) "s_add_u32 m0, %1, " #bias "\n\ts_nop 0\n\t"
#define RING m = m == HI - STEP ? LO : m + STEP
template <int STEP, int LO, int HI> DI void gl4(const char* base, u32& m, const u32 (&v)[4]) {
  asm volatile(M0(0) GL("%2", 0, "") GL("%3", 1024, "") GL("%4", 2048, "") GL("%5", 3072, "")
               ::"s"(base), "s"(m), "v"(v[0]), "v"(v[1]), "v"(v[2]), "v"(v[3]) : "memory", "scc");
  RING;
}
template <int STEP, int LO, int HI> DI void gl8(const char* base, u32& m, const u32 (&v)[8]) {
  asm volatile(M0(4096) GL("%2", -4096, "") GL("%3", -3072, "") GL("%4", -2048, "") GL("%5", -1024, "")
               GL("%6", 0, "") GL("%7", 1024, "") GL("%8", 2048, "") GL("%9", 3072, "")
               ::"s"(base), "s"(m), "v"(v[0]), "v"(v[1]), "v"(v[2]), "v"(v[3]), "v"(v[4]), "v"(v[5]), "v"(v[6]), "v"(v[7]) : "memory", "scc");
  RING;
}
template <int STEP, int LO, int HI> DI void gl8w(const char* base, u32& m, const u32 (&v)[8]) {
  asm volatile("s_add_u32 m0, %1, 3840\n\ts_mov_b32 exec_hi, 0x0fffffff\n\t" GL("%2", -3840, "") GL("%3", -2880, "") GL("%4", -1920, "") GL("%5", -960, "")
               GL("%6", 0, "") GL("%7", 960, "") GL("%8", 1920, "") GL("%9", 2880, "") "s_mov_b32 exec_hi, -1"
               ::"s"(base), "s"(m), "v"(v[0]), "v"(v[1]), "v"(v[2]), "v"(v[3]), "v"(v[4]), "v"(v[5]), "v"(v[6]), "v"(v[7]) : "memory", "scc");
  RING;
}
DI void gl5(const char* base, u32 m, const u32 (&v)[5]) {
  asm volatile(M0(4096) GL("%2", -4096, "") GL("%3", -3072, "") GL("%4", -2048, "") GL("%5", -1024, "") GL("%6", 0, "")
               ::"s"(base), "s"(m), "v"(v[0]), "v"(v[1]), "v"(v[2]), "v"(v[3]), "v"(v[4]) : "memory", "scc");
}
#define GL8S M0(4096) GL("%2", -4096, " sc1") GL("%2", -3072, " sc1") GL("%2", -2048, " sc1") GL("%2", -1024, " sc1") \
             GL("%2", 0, " sc1") GL("%2", 1024, " sc1") GL("%2", 2048, " sc1") GL("%2", 3072, " sc1")
template <int STEP, int LO, int HI> DI void glin8(const char* base, u32& m, u32 vl4) {
  asm volatile(GL8S ::"s"(base), "s"(m), "v"(vl4) : "memory", "scc");
  RING;
}
template <int STEP, int LO, int HI> DI void glin10(const char* base, u32& m, u32 vl4, u32 vl8) {
  asm volatile(GL8S M0(8192) GL("%3", 0, " sc1") GL("%3", 1024, " sc1")
               ::"s"(base), "s"(m), "v"(vl4), "v"(vl8) : "memory", "scc");
  RING;
}
template <int STEP, int LO, int HI> DI void glin55(const char* base, u32& m, u32 vl4, u32 vl4b) {
  asm volatile(M0(4096) GL("%2", -4096, " sc1") GL("%2", -3072, " sc1") GL("%2", -2048, " sc1") GL("%2", -1024, " sc1") GL("%2", 0, " sc1")
               M0(9216) GL("%3", -4096, " sc1") GL("%3", -3072, " sc1") GL("%3", -2048, " sc1") GL("%3", -1024, " sc1") GL("%3", 0, " sc1")
               ::"s"(base), "s"(m), "v"(vl4), "v"(vl4b) : "memory", "scc");
  RING;
}
template <int STEP, int LO, int HI> DI void glin2222(const char* base, u32& m, const u32 (&v)[4]) {
  asm volatile(M0(4096) GL("%2", -4096, " sc1") GL("%2", -3072, " sc1") GL("%3", -2048, " sc1") GL("%3", -1024, " sc1")
               GL("%4", 0, " sc1") GL("%4", 1024, " sc1") GL("%5", 2048, " sc1") GL("%5", 3072, " sc1")
               ::"s"(base), "s"(m), "v"(v[0]), "v"(v[1]), "v"(v[2]), "v"(v[3]) : "memory", "scc");
  RING;
}
DI void glin1(const char* base, u32 m, u32 v) { asm volatile(M0(0) GL("%2", 0, " sc1")::"s"(base), "s"(m), "v"(v) : "memory", "scc"); }
DI u32x4 swap16(u32x4 t) {
  const auto s0 = __builtin_amdgcn_permlane16_swap(t[0], t[2], false, false), s1 = __builtin_amdgcn_permlane16_swap(t[1], t[3], false, false);
  return u32x4{s0[0], s1[0], s0[1], s1[1]};
}
DI void touch(const char* base, u32 m, u32 v) {
  asm volatile("s_mov_b32 m0, %1\n\ts_nop 0\n\tglobal_load_lds_dword %2, %0" ::"s"(base), "s"(m), "v"(v) : "memory");
}
DI void poll(const char* f, int idx, u32 tag) {
  const GAS u32* p = (const GAS u32*)(f + (u32)(idx * 128));
#pragma unroll 1
  for (u32 sp = 0; sp < SPIN_CAP; ++sp) {
    u32 v = __hip_atomic_load(p, RLX_AGENT);
    if (__all(v == tag)) break;
    __builtin_amdgcn_s_sleep(SLP);
  }
  asm volatile("" ::: "memory");
}
template <int R = 255> DI void peek(const char* f, int idx, u32 tag) {
  asm volatile("v_not_b32 v%c2, %3\n\tglobal_load_dword v%c2, %0, %1 sc1" ::"v"(idx * 128), "s"(f), "i"(R), "s"(tag) : "memory", "v254", "v255");
}
template <int R = 255> DI bool seen(u32 tag) {
  u64 miss;
  asm volatile("v_cmp_ne_u32 %0, %1, v%c2" : "=s"(miss) : "s"(tag), "i"(R) : "memory");
  return miss == 0;
}
template <int R = 255> DI void await(const char* f, int idx, u32 tag) { if (!seen<R>(tag)) poll(f, idx, tag); }
DI void publish(char* f, u32 tag, int lane) {
  WAITV(0);
  if (lane == 0) __hip_atomic_store((GAS u32*)f, tag, RLX_AGENT);
}
DI void publish_last(char* f, u32 tag, int lane, u32 cnt, u32 n) {
  WAITV(0);
  if (lane == 0 && __hip_atomic_fetch_add((LAS u32*)cnt, 1u, __ATOMIC_RELAXED, __HIP_MEMORY_SCOPE_WORKGROUP) == n - 1)
    __hip_atomic_store((GAS u32*)f, tag, RLX_AGENT);
}

struct Params {
  const char *x, *wqkv, *wo, *w1, *b1, *w2, *b2;
  char *out, *ws;
};
#define ROLES() \
  int b_ = b; asm volatile("" : "+s"(b_)); \
  const int xc = b_ & 7, jb = b_ >> 3; \
  const bool roleA = b_ < 128; \
  const int hg = 2 * (xc & 3) + (jb & 1), rt = 8 * (xc >> 2) + (jb >> 1); \
  const int dt = jb & 7, grp = (jb >> 3) & 1; \
  const bool late = !roleA && xc >= 5, narrow = !roleA && xc < 5 && grp; \
  const int fu = late ? 4 * xc - 5 + 2 * grp : grp ? xc : 5 + 2 * xc, gi = late ? 2 * xc + grp : grp ? xc : 5 + xc; \
  const int st = 4 * xc + (jb >> 3), dt3 = jb & 7; \
  const u32 d0 = dt ^ (XP && late ? grp : 0), istep = narrow ? 2 : 1, rot = (5 * (2 * (rt & 7) + (rt >> 3))) >> 2; \
  (void)hg; (void)fu; (void)gi; (void)st; (void)dt3; (void)d0; (void)istep; (void)rot
#define EPOCH() *(const __attribute__((address_space(4))) u32*)(ws + WS_EPOCH)
__global__ __launch_bounds__(320, 2) void fused_fwd(Params P) {
  const int tid = threadIdx.x, lane = tid & 63;
  const int wave = __builtin_amdgcn_readfirstlane(tid >> 6);
  const int b = blockIdx.x;
  char* const ws = P.ws;
  constexpr u32 L0 = 0;
  const u32 LDUM = L0 + L_BYTES - 256, LCNT = L0 + L_BYTES - 512;
  const int c16 = lane & 15, g = lane >> 4, q4 = c16 >> 2, p4 = lane & 3;
  {
    u64 pc; int left;
    asm volatile("s_getpc_b64 %0\n.Lcode_here:\n\ts_mov_b32 %1, .Lcode_exit_a-.Lcode_here\n\ts_max_i32 %1, %1, .Lcode_exit_b-.Lcode_here"
                 : "=s"(pc), "=&s"(left)::"scc");
    touch((const char*)pc, LDUM, min((((b >> 3) * 5 + wave) * 2 + (lane & 1)) * 128, left));
  }
  if (wave == 4) {
    __builtin_amdgcn_s_setprio(3);
    u32 v[8], sa;
    if (b >= 128) {
      ROLES();
      const u32 col = narrow ? ((lane & 7) ^ ((g & 1) << 1)) * 16 : (c16 ^ (g << 1)) * 16, stp = (narrow ? 8 : 4) * 3456 - 1024;
#pragma unroll
      for (int i = 0; i < 8; ++i)
        v[i] = (narrow ? lane >> 3 : g) * 3456 + (col ^ (narrow ? (i & 1) * 64 : ((i >> 1) & 1) * 128)) + i * stp + 4096;
      sa = narrow ? 10 * 8192 : 6 * 8192;
#pragma unroll 1
      for (int i = 0; i < PRE_B; ++i) gl8<8192, 0, L1_B>(P.w1 + fu * 128 + (8 * d0 + i * istep) * 110592, sa, v);
    } else {
      ROLES();
      const u32 hi = lane >= 30, cc = lane - 30 * hi, sg = (cc >= 10) + (cc >= 20);
      v[0] = (hi + 1) * 3840 + sg * 1120 + cc * 16;
#pragma unroll
      for (int i = 1; i < 8; ++i) v[i] = v[0] + i * 6720;
      sa = 0;
      const char* wp = P.wqkv + (hg * 160 + rot * 122880);
      gl8w<7680, 0, LQ_B>(wp, sa, v); gl8w<7680, 0, LQ_B>(wp + 61440, sa, v);
    }
    u32 e0 = EPOCH();
    ROLES();
    asm volatile("" : "+s"(e0));
    BAR(); LBAR();
    const u32 tag = mix(e0);
    const u32 vl4 = lane * 16 + 4096, vl8 = vl4 + 4096;
    const u32 u0 = (lane >> 3) * 4096 + ((lane & 7) ^ (g << 1)) * 16;
    const u32 u[4] = {u0, u0 + 31744, u0 + 2 * 31744, u0 + 3 * 31744};
    const char* w2p = P.w2 + st * 128;
    u32 sa3 = 0;
    auto ahalf = [&]() { gl4<4096, 0, L3_B>(w2p, sa3, u); w2p += 131072; };
    const char* const gfl = ws + WS_GFLAG + dt3 * 8192;
    const char* bp = ws + WS_G + dt3 * 276480;
    u32 sb3 = L3_B;
    auto bunit3 = [&]() { glin10<10240, L3_B, L3_B + NB3 * 10240>(bp, sb3, vl4, vl8); bp += 10240; };
    auto start3 = [&](int done) {
#pragma unroll 1
      for (int i = 0; i < 4; ++i) bunit3();
#pragma unroll 1
      for (int i = done; i < 2 * NA3; i += 2) { ahalf(); ahalf(); }
    };
    if (!roleA) {
      const char* wb = P.w1 + fu * 128;
      const char* hb = ws + WS_H + dt * 5120;
      u32 ia = PRE_B * istep, ib2 = 0, sb = L1_B;
      static_assert(PRE_B <= 4, "");
      auto a8 = [&]() { gl8<8192, 0, L1_B>(wb + (ia ^ (8 * d0)) * 110592, sa, v); ia += istep; };
      auto bunit = [&]() { glin55<10240, L1_B, L1_B + NB1 * 10240>(hb + (ib2 ^ (4 * d0)) * 81920, sb, vl4, vl4 + 40960); ++ib2; };
#pragma unroll 1
      for (int i = 0; i < MID_B; ++i) a8();
      LBAR();
#pragma unroll 1
      for (int i = PRE_B + MID_B; i < 10; ++i) { WAITV(32); a8(); }
      peek(ws + WS_LNFLAG, (d0 & 6) * 32 + lane, tag);
      a8(); a8();
      WAITV(16);
      await(ws + WS_LNFLAG, (d0 & 6) * 32 + lane, tag);
      bunit(); bunit(); bunit();
      a8(); a8();
      WAITX(36); LBAR(); bunit();
      WAITX(36);
      if (narrow) {
#pragma unroll 1
        for (int r = 1; r < 3; ++r) { LBAR(); bunit(); WAITX(20); }
        peek(ws + WS_ALLFLAG, 0, tag);
#pragma unroll 1
        for (int r = 3; r < 5; ++r) { LBAR(); bunit(); WAITX(20); }
      } else {
#pragma unroll 1
        for (int r = 1; r < 3; ++r) { LBAR(); a8(); a8(); bunit(); WAITX(52); }
        peek(ws + WS_ALLFLAG, 0, tag);
#pragma unroll 1
        for (int r = 3; r < 5; ++r) { LBAR(); a8(); a8(); bunit(); WAITX(52); }
      }
      await(ws + WS_ALLFLAG, 0, tag);
      if (narrow) {
#pragma unroll 1
        for (int r = 5; r < 29; ++r) { LBAR(); if (r & 3) a8(); bunit(); WAITX(28); }
      } else {
#pragma unroll 1
        for (int r = 5; r < 29; ++r) { LBAR(); a8(); if (r & 3) a8(); bunit(); WAITX(44); }
      }
      LBAR();
      if (narrow) {
        u32 w5[5];
#pragma unroll
        for (int i = 0; i < 5; ++i) { const u32 n = 64 * i + lane; w5[i] = n / 40u * 640 + lane * 16 + 4096; }
        const char* wp = P.wo + (xc * 4 + (dt >> 1)) * (32 * 1280) + (dt & 1) * 640;
#pragma unroll 1
        for (int i = 0; i < 4; ++i) gl5(wp + i * (8 * 1280), i * 5120, w5);
        WAITX(30); LBAR(); WAITX(20); LBAR();
      } else {
        constexpr int E1 = 3 * EARLY / 8;
#pragma unroll 1
        for (int i = 0; i < E1; ++i) ahalf();
        WAITX(18 + 4 * E1); LBAR();
#pragma unroll 1
        for (int i = 0; i < E1; ++i) ahalf();
        WAITX(8 * E1); LBAR();
        if (!late) {
#pragma unroll 1
          for (int i = 2 * E1; i < EARLY; ++i) ahalf();
        }
      }
      if (!late) {
        LBAR();
        if (narrow) { WAITV(0); LBAR(); LBAR(); }
#pragma unroll 1
        for (int i = narrow ? -16 : 0; i < 10; ++i) { ahalf(); WAITV(32); if (!narrow) WAITX(TH_P3); }
        peek(gfl, lane < 5 ? lane : 0, tag);
        WAITV(0);
        if (!narrow) LBAR();
      } else {
        ahalf(); ahalf(); ahalf(); ahalf();
        LBAR();
#pragma unroll 1
        for (int i = 3 * EARLY / 8 * 2 + 4; i < LPK; ++i) ahalf();
        peek(gfl, lane < 5 ? lane : 0, tag);
        ahalf(); ahalf(); ahalf(); ahalf();
        WAITV(16);
      }
      await(gfl, lane < 5 ? lane : 0, tag);
      start3(late ? LPK + 4 : narrow ? 26 : EARLY + 10);
    } else {
      const char* wb = P.wqkv + hg * 160;
      const char* xb = ws + WS_H + rt * 163840;
      const u32 x0 = (lane ^ ((g & 1) << 3)) * 16 + 4096, xv[4] = {x0, x0 + 38912, x0 + 2 * 38912, x0 + 3 * 38912};
      u32 oa = (rot + 1) * 122880  , ob = rot * 2048, sb = LQ_B;
      auto a8 = [&]() { gl8w<7680, 0, LQ_B>(wb + oa, sa, v); oa = oa == 39 * 61440 ? 0 : oa + 61440; };
      auto bunit = [&]() { glin2222<8192, LQ_B, LQ_B + 4 * 8192>(xb + ob, sb, xv); ob = ob == 19 * 2048 ? 0 : ob + 2048; };
#pragma unroll 1
      for (int i = 0; i < MID_A; ++i) a8();
      LBAR();
#pragma unroll 1
      for (int i = PRE_A + MID_A; i < NPK_A; ++i) { WAITV(24); a8(); }
      peek(ws + WS_LNFLAG, rt * 16 + c16, tag);
      a8(); a8();
      WAITV(16);
      await(ws + WS_LNFLAG, rt * 16 + c16, tag);
      bunit(); bunit(); bunit();
      glin1(ws + WS_SC + rt * 512, L_SC, (lane & 31) * 16);
#pragma unroll 1
      for (int i = NPK_A + 2; i < 12; ++i) a8();
      WAITX(16 + 8 * (10 - NPK_A));
#pragma unroll 1
      for (int r = 1; r < 17; ++r) { LBAR(); a8(); if (r & 3) a8(); bunit(); WAITX(40); }
      LBAR(); bunit();
      WAITX(24); LBAR();
      const char* tb = P.w2 + xc * 512 + jb * (108 * 4096);
      const u32 tv = (lane >> 2) * 4096 + p4 * 128;
#pragma unroll 1
      for (int i = 0; i < NTCH; ++i) touch(tb + (i == 6 ? 92 : 16 * i) * 4096, LDUM, tv);
      WAITX(8 + NTCH); LBAR(); WAITX(NTCH); LBAR();
      BAR();
      const char* const kfl = ws + WS_KVFLAG + hg * 2048;
      int done = 0;
#pragma unroll 1
      for (; done < APRE; ++done) { ahalf(); WAITV(24); }
      LBAR();
      peek(gfl, lane < 5 ? lane : 0, tag);
      WAITV(0);
      LBAR();
      if (!seen(tag)) {
#pragma unroll 1
        for (; done < 2 * NA3; ++done) ahalf();
        poll(gfl, lane < 5 ? lane : 0, tag);
      }
      bunit3(); bunit3(); bunit3();
      WAITX(TH_PA);
      peek(kfl, lane & 7, tag);
      bunit3(); bunit3();
      WAITV(41); LBAR(); WAITV(31); LBAR(); WAITV(20);
      await(kfl, lane & 7, tag);
      peek(kfl, 8 + (lane & 7), tag);
      LBAR(); WAITV(11); LBAR(); WAITV(0); LBAR();
      await(kfl, 8 + (lane & 7), tag);
      LBAR();
      peek(gfl, lane < 5 ? 5 + lane : 5, tag);
      WAITV(0);
      const bool ready = seen(tag);
      if (!ready) { LBAR(); poll(gfl, lane < 5 ? 5 + lane : 5, tag); }
      bunit3(); bunit3();
      if (ready) LBAR();
      bunit3(); bunit3();
#pragma unroll 1
      for (; done < 2 * NA3 + 4; ++done) ahalf();
      WAITV(16);
    }
    {
      auto bunit = bunit3;
      if (!roleA) {
        WAITX(30); LBAR();
        peek(gfl, lane < 5 ? 5 + lane : 5, tag);
        bunit();
        WAITX(30); LBAR(); ahalf();
        WAITX(25); LBAR();
        WAITV(14);
        await(gfl, lane < 5 ? 5 + lane : 5, tag);
        bunit(); bunit(); ahalf();
      }
#pragma unroll 1
      for (int r = roleA ? 5 : 3; r < 7; ++r) { LBAR(); bunit(); ahalf(); WAITX(42); }
      peek(gfl, lane < 6 ? 10 + lane : 10, tag);
#pragma unroll 1
      for (int r = 7; r < 11; ++r) { LBAR(); bunit(); ahalf(); WAITX(46); }
      await(gfl, lane < 6 ? 10 + lane : 10, tag);
#pragma unroll 1
      for (int r = 11; r < 18; ++r) { LBAR(); bunit(); ahalf(); if (r > H3) ahalf(); WAITX(46); }
#pragma unroll 1
      for (int r = 18; r < 20; ++r) { LBAR(); bunit(); ahalf(); ahalf(); WAITX(62); }
      peek<254>(ws + WS_OFLAG + st * 2048, c16, tag);
      peek(gfl, lane < 20 ? 16 + lane : 16, tag);
#pragma unroll 1
      for (int r = 20; r < 22; ++r) { LBAR(); bunit(); ahalf(); ahalf(); WAITX(62); }
      LBAR(); bunit(); WAITX(54);
      LBAR(); WAITX(36);
      await<254>(ws + WS_OFLAG + st * 2048, c16, tag); await(gfl, lane < 20 ? 16 + lane : 16, tag);
      const char* ap = ws + WS_O + st * 81920 + dt3 * 8192;
      bp = ws + WS_WO + dt3 * 112640;
#pragma unroll 1
      for (int r = 24; r < 34; ++r) {
        LBAR();
        glin8<8192, 0, L3_B>(ap, sa3, vl4);
        bunit();
        ap += 8192;
        if (r + dt3 == 33) { ap -= 81920; bp -= 102400; }
        WAITX(36);
      }
      LBAR(); WAITX(18); LBAR(); WAITX(0); LBAR();
    }
    asm volatile(".Lcode_exit_a:" ::: "memory");
    return;
  }

  const int hw = wave >> 1, ty = wave & 1;
  const __amdgpu_buffer_rsrc_t rs = __builtin_amdgcn_make_buffer_rsrc(ws, 0, WS_END, 0x00020000);
  u32 e0;
  {
    const int row = 2 * wave + (lane >> 5), hl = lane & 31;
    const GAS u32x2* xp = (const GAS u32x2*)(P.x + b * (8 * 1280) + (u32)(row * 1280 + hl * 8));
    u32x2 raw[5];
#pragma unroll
    for (int i = 0; i < 5; ++i) raw[i] = xp[32 * i];
    SB();
    e0 = EPOCH();
    u32 ra = L0 + L_LN + q4 * LN_PITCH + (10 * wave + g) * 32 + p4 * 8, wa = L0 + L_LN + row * LN_PITCH + hl * 8;
    int go = WS_H + (b >> 2) * 40960 + (10 * wave + g) * 1024 + (b & 3) * 256 + c16 * 16, g1 = go + 4096, g2 = g < 2 ? go + 8192 : OOB;
    int gs = hl == 0 ? WS_SC + (8 * b + row) * 4 : OOB;
    *(LAS u32*)LCNT = 0;
    asm volatile("" : "+v"(ra), "+v"(go), "+v"(g1), "+v"(g2), "+v"(gs), "+v"(wa), "+s"(e0));
    SB();
    f32x2 v[10];
#pragma unroll
    for (int i = 0; i < 5; ++i) { v[2 * i] = f32x2{blo(raw[i][0]), bhi(raw[i][0])}; v[2 * i + 1] = f32x2{blo(raw[i][1]), bhi(raw[i][1])}; }
    f32x2 s = v[0] + v[1], s2 = v[2] + v[3];
#pragma unroll
    for (int i = 4; i < 10; i += 2) { s += v[i]; s2 += v[i + 1]; }
    s += s2;
    const float m = sum32(s[0] + s[1]) * (1.f / 640.f);
#pragma unroll
    for (int i = 0; i < 10; ++i) v[i] -= m;
    f32x2 qq = v[0] * v[0], q2 = v[1] * v[1];
#pragma unroll
    for (int i = 2; i < 10; i += 2) { qq += v[i] * v[i]; q2 += v[i + 1] * v[i + 1]; }
    qq += q2;
    const float var = sum32(qq[0] + qq[1]) * (1.f / 640.f);
    const float r1 = __builtin_amdgcn_rsqf(var + 1e-5f), v2 = var * r1 * r1 + 1e-5f, r2 = __builtin_amdgcn_rsqf(v2), r12 = r1 * r2;
#pragma unroll
    for (int i = 0; i < 5; ++i) {
      const f32x2 a = v[2 * i] * r12, c = v[2 * i + 1] * r12;
      *(LAS u32x2*)(wa + i * 256) = u32x2{pk2(a[0], a[1]), pk2(c[0], c[1])};
    }
    __builtin_amdgcn_raw_buffer_store_b32(__float_as_uint(v2 * r2), rs, gs, 0, 16);
    BAR();
    s16x8 f[3];
#pragma unroll
    for (int i = 0; i < 3; ++i) f[i] = cat(ldtr(ra + i * 128), ldtr(ra + i * 128 + 4 * LN_PITCH));
    st128(rs, go, f[0]); st128(rs, g1, f[1]); st128(rs, g2, f[2]);
    BAR();
    publish_last(ws + WS_LNFLAG + b * 128, mix(e0), lane, LCNT, 4);
    BAR();
  }
  const u32 tag = mix(e0);
  ROLES();
  const u32 xr = (st * 64 + wave * 16 + c16) * 1280 + dt3 * 160, xo = xr + (g & 1) * 32 + (g >> 1) * 16;
  u32x4 x01, x23; u32x2 x4; u32 bs;
  auto ldx = [&]() {
    x01 = *(const GAS u32x4*)(P.x + xo); x23 = *(const GAS u32x4*)(P.x + xo + 64);
    x4 = *(const GAS u32x2*)(P.x + (xr + 128 + g * 8));
    bs = *(const GAS unsigned short*)(P.b2 + st * 128 + wave * 32 + (u32)(c16 * 2));
  };
  f32x4 oc[2][5];
  const u32 a30 = L0 + (32 * ty + 4 * g + q4) * 128 + (wave ^ ((2 * g + (q4 >> 1)) & 3)) * 32 + p4 * 8;
  const u32 ga0 = L0 + L3_B + ty * 5120 + lane * 16;
  const u32 oa0 = L0 + ty * 4096 + hw * 1024 + lane * 16 + ty * 8;
  u32 sa3, sb3, na3, nb3, ng3;
  struct FA { s16x8 a[2], g[5]; };
  auto lda = [&](FA& f, bool tr) {
    const u32 aa = na3, ab = nb3, ga = ng3;
    BAR();
    if (tr) { f.a[1] = cat(ldtr(ab), ldtr(ab + 2048)); f.a[0] = cat(ldtr(aa), ldtr(aa + 2048)); }
    else { f.a[1] = cat(ld64(ab), ld64(ab + 2048)); f.a[0] = cat(ld64(aa), ld64(aa + 2048)); }
#pragma unroll
    for (int j = 0; j < 5; ++j) f.g[j] = ld128(ga + j * 1024);
    sa3 = sa3 == L3_B - 8192 ? 0 : sa3 + 8192;
    sb3 = sb3 == LA3 * 10240 ? 0 : sb3 + 10240;
    na3 = (tr ? a30 : oa0) + sa3; nb3 = na3 ^ (tr ? 32 : 8); ng3 = ga0 + sb3;
  };
  auto mma = [&](const FA& f) {
#pragma unroll
    for (int j = 0; j < 5; ++j) oc[1][j] = mfma32(f.g[j], f.a[1], oc[1][j]);
#pragma unroll
    for (int j = 0; j < 5; ++j) oc[0][j] = mfma32(f.g[j], f.a[0], oc[0][j]);
  };
  auto pat = [&]() {
    SG(0x100, 1);
#pragma unroll
    for (int j = 0; j < 4; ++j) { SG(0x8, 1); SG(0x100, 2); }
#pragma unroll
    for (int j = 0; j < 5; ++j) { SG(0x8, 1); SG(0x6, 2); }
    SG(0x8, 1);
    asm volatile("" : "+v"(na3), "+v"(nb3), "+v"(ng3));
    SB();
  };
  FA fa0, fa1;
  auto begin3 = [&]() {
#pragma unroll
    for (int j = 0; j < 5; ++j) oc[0][j] = oc[1][j] = f32x4{0.f, 0.f, 0.f, 0.f};
    sa3 = sb3 = 0; na3 = a30; nb3 = a30 ^ 32; ng3 = ga0;
    lda(fa0, true); SB();
  };
  auto rounds = [&](int r0, int r1, bool tr) {
#pragma unroll 1
    for (int r = r0; r < r1; r += 2) { lda(fa1, tr); mma(fa0); pat(); lda(fa0, tr); mma(fa1); pat(); }
  };
  if (roleA) {
    f32x4 aq[5][2] = {}, akv[5][4] = {};
    const u32 wa0 = L0 + (4 * g + q4) * 480 + p4 * 8;
    const u32 xa0 = L0 + LQ_B + wave * 2048 + (p4 >> 1) * 256 + ((4 * g + q4) ^ ((p4 >> 1) << 3)) * 16 + (p4 & 1) * 8;
    const int c32 = lane & 31, hh = lane >> 5;
    const int pb = WS_PART + (hg * 4 + wave) * (16 * 896);
    u32 vp0 = L0 + L_KV + (8 * hw + 4 * ty + g) * LN_PITCH + c16 * 16 + ty * 768, vp1 = vp0 + (1 - 2 * ty) * (4 * LN_PITCH);
    u32 ka = L0 + L_KV + hh * LN_PITCH + (20 * wave + (c32 < 20 ? c32 : 19)) * 16;
    u32 vb = L0 + L_KV + (16 + hh) * LN_PITCH + (c32 < 20 ? 20 * wave + c32 : 80) * 16;
    int so0 = c32 <= 20 ? pb + rt * 896 + (hh * 21 + c32) * 16 : OOB, so1 = lane <= 21 ? pb + rt * 896 + 672 + lane * 8 : OOB;
    u32 vp2 = vp0 - ty * 768 + 512, von = L0 + L_KV + (16 + 4 * wave + g) * LN_PITCH + 1280;
    asm volatile("" : "+v"(vp2), "+v"(von), "+v"(vp0), "+v"(vp1), "+v"(ka), "+v"(vb), "+v"(so0), "+v"(so1));
    u32 qa[5][3], za[5];
#pragma unroll
    for (int je = 0; je < 5; ++je) {
      const int e = 16 * je + c16, he = e / 20, el = e - 20 * he;
#pragma unroll
      for (int x = 0; x < 3; ++x) {
        const int dl = 4 * (je - 1 + x) + g - 5 * he;
        const int qi = dl < 4 ? ((dl & 1) * 21 + el) * 2 + (dl >> 1) : 84 + el;
        qa[je][x] = L0 + L_FIN + (dl >= 0 && dl < 5 ? he * 848 + qi * 8 : 3392);
        if (je - 1 + x >= 0 && je - 1 + x < 5) asm volatile("" : "+v"(qa[je][x]));
      }
      const int dl = 4 * je + g - 5 * p4;
      const int qi = dl < 4 ? ((dl & 1) * 21 + 20) * 2 + (dl >> 1) : 104;
      za[je] = L0 + L_FIN + (dl >= 0 && dl < 5 ? p4 * 848 + qi * 8 : 3392);
      asm volatile("" : "+v"(za[je]));
    }
    u32x4 sel;
    {
      const int r = (c32 & 3) + 4 * (c32 >> 3);
      const u32 one = c32 < 16 && ((c32 >> 2) & 1) == hh ? 0x3f80u << (16 * (r & 1)) : 0u;
#pragma unroll
      for (int i = 0; i < 4; ++i) sel[i] = (r >> 1) == i ? one : 0u;
    }
    int go = lane < 53 ? pb + lane * 16 : OOB;
    u32 fa = L0 + L_FIN + (lane < 53 ? wave * 848 + lane * 16 : 3392);
    int ob = WS_O + (2 * rt + hw) * 81920 + hg * 10240 + ty * 1024 + lane * 16;
    asm volatile("" : "+v"(sel), "+v"(go), "+v"(fa), "+v"(ob));
    s16x8 xA[4] = {}, xB[4], wq[5] = {};
    u32 nwa = wa0, nwk = wa0 + ty * 96, nxa = xa0, nxb = xa0 ^ 2048;
    auto round = [&](int r, s16x8(&xo)[4], s16x8(&xn)[4]) {
      const u32 wa = nwa, wk = nwk, xa = nxa, xb = nxb;
      BAR();
      s16x8 w[6];
      xn[0] = cat(ldtr(xa), ldtr(xa + 1024)); w[0] = cat(ldtr(wk + 160), ldtr(wk + 160 + 7680));
      xn[1] = cat(ldtr(xa + 512), ldtr(xa + 1536)); xn[2] = cat(ldtr(xb), ldtr(xb + 1024)); xn[3] = cat(ldtr(xb + 512), ldtr(xb + 1536));
      w[1] = cat(ldtr(wk + 192), ldtr(wk + 192 + 7680));
#pragma unroll
      for (int j = 0; j < 5; ++j) { aq[j][0] = mfma32(wq[j], xo[0], aq[j][0]); aq[j][1] = mfma32(wq[j], xo[1], aq[j][1]); }
#pragma unroll
      for (int j = 0; j < 2; ++j) { SG(0x8, 1); SG(0x100, 2); }
#pragma unroll
      for (int j = 0; j < 8; ++j) { SG(0x8, 1); SG(0x100, 1); }
      SB();
      w[3] = cat(ldtr(wk + 320), ldtr(wk + 320 + 7680));
      w[4] = cat(ldtr(wk + 352), ldtr(wk + 352 + 7680));
      w[2] = cat(ldtr(wa + 224), ldtr(wa + 224 + 7680));
      w[5] = cat(ldtr(wa + 384), ldtr(wa + 384 + 7680));
#pragma unroll
      for (int j = 0; j < 5; ++j) wq[j] = cat(ldtr(wa + j * 32), ldtr(wa + 7680 + j * 32));
      nwa = wa0 + ((r + 1) & 7) * 15360; nwk = nwa + ty * 96; nxa = xa0 + ((r + 1) & 3) * 8192; nxb = nxa ^ 2048;
#pragma unroll
      for (int j = 0; j < 5; ++j) {
        if (j == 2) continue;
#pragma unroll
        for (int i = 0; i < 4; ++i) akv[j][i] = mfma32(xn[i], w[j], akv[j][i]);
      }
#pragma unroll
      for (int i = 0; i < 4; ++i) akv[2][i] = mfma32(xn[i & 1], w[i < 2 ? 2 : 5], akv[2][i]);
#pragma unroll
      for (int j = 0; j < 3; ++j) { SG(0x8, 1); SG(0x100, 2); }
#pragma unroll
      for (int j = 0; j < 12; ++j) { SG(0x8, 1); SG(0x100, 1); }
#pragma unroll
      for (int j = 0; j < 4; ++j) { SG(0x8, 1); SG(0x6, 2); }
      SG(0x8, 1);
      asm volatile("" : "+v"(nwa), "+v"(nwk), "+v"(nxa), "+v"(nxb));
      SB();
    };
#pragma unroll 1
    for (int r = 0; r < 20; r += 2) { round(r, xA, xB); round(r + 1, xB, xA); }
    f32x4 sk[4], sq[2];
#pragma unroll
    for (int i = 0; i < 4; ++i) sk[i] = *(const LAS f32x4*)(L0 + L_SC + ((wave ^ (i >> 1)) * 128 + (i & 1) * 64 + g * 16));
#pragma unroll
    for (int i = 0; i < 2; ++i) { const float t = *(const LAS float*)(L0 + L_SC + (wave * 128 + i * 64 + c16 * 4)); sq[i] = f32x4{t, t, t, t}; }
    BAR();
    {
      SB();
      *(LAS s16x8*)(vp2 + 16 * LN_PITCH) = cat(pk4(akv[2][2] * sk[0]), pk4(akv[2][3] * sk[1]));
      *(LAS s16x8*)(von) = s16x8{0x3f80, 0x3f80, 0x3f80, 0x3f80, 0x3f80, 0x3f80, 0x3f80, 0x3f80};
#pragma unroll
      for (int j = 3; j < 5; ++j) {
        *(LAS s16x8*)(vp0 + 16 * LN_PITCH + (j - 3) * 256) = cat(pk4(akv[j][0] * sk[0]), pk4(akv[j][1] * sk[1]));
        *(LAS s16x8*)(vp1 + 16 * LN_PITCH + (j - 3) * 256) = cat(pk4(akv[j][2] * sk[2]), pk4(akv[j][3] * sk[3]));
      }
      SB();
#pragma unroll
      for (int j = 0; j < 2; ++j) {
        s16x4 o[4];
        phi<4>(akv[j], sk, o);
        *(LAS s16x8*)(vp0 + j * 256) = cat(o[0], o[1]);
        *(LAS s16x8*)(vp1 + j * 256) = cat(o[2], o[3]);
        SB();
      }
      const f32x4 k2[2] = {akv[2][0], akv[2][1]};
      s16x4 o[2];
      phi<2>(k2, sk, o);
      *(LAS s16x8*)(vp2) = cat(o[0], o[1]);
    }
    BAR();
    {
      s16x8 fk[8], fv[8];
#pragma unroll
      for (int i = 0; i < 8; ++i) { fk[i] = ld128(ka + i * 2 * LN_PITCH); fv[i] = ld128(vb + i * 2 * LN_PITCH); }
      SB();
      f32x16 d = {};
#pragma unroll
      for (int i = 0; i < 8; ++i) d = mfmaw(fk[i], fv[i], d);
      SB();
#pragma unroll
      for (int j = 0; j < 2; ++j) { aq[j][0] = mfma32(wq[j], xA[0], aq[j][0]); aq[j][1] = mfma32(wq[j], xA[1], aq[j][1]); }
      SB();
      const u32x4 lo = {pk2(d[0], d[1]), pk2(d[2], d[3]), pk2(d[4], d[5]), pk2(d[6], d[7])};
      const u32x2 hi = {pk2(d[8], d[9]), pk2(d[10], d[11])};
      st128(rs, so0, __builtin_bit_cast(s16x8, lo));
      st64(rs, so1, __builtin_bit_cast(s16x4, hi));
      SB();
    }
    BAR();
#pragma unroll
    for (int j = 2; j < 5; ++j) { aq[j][0] = mfma32(wq[j], xA[0], aq[j][0]); aq[j][1] = mfma32(wq[j], xA[1], aq[j][1]); }
    SB();
    s16x4 pq[5][2];
#pragma unroll
    for (int j = 0; j < 2; ++j) { phi<2>(aq[j], sq, pq[j]); asm volatile("" : "+v"(pq[j][0]), "+v"(pq[j][1])); }
    publish_last(ws + WS_KVFLAG + hg * 2048 + rt * 128, tag, lane, LCNT, 8);
    ldx();
    SB();
    s16x8 p01[2], p23[2], p34[2];
#pragma unroll
    for (int j = 2; j < 5; ++j) phi<2>(aq[j], sq, pq[j]);
#pragma unroll
    for (int i = 0; i < 2; ++i) {
      p01[i] = cat(pq[0][i], pq[1][i]); p23[i] = cat(pq[2][i], pq[3][i]); p34[i] = cat(pq[3][i], pq[4][i]);
      asm volatile("" : "+v"(p01[i]), "+v"(p23[i]), "+v"(p34[i]));
    }
    begin3();
    rounds(1, 3, true);
    {
      f32x16 d = {};
      u32x4 pr[16];
#pragma unroll
      for (int i = 0; i < 8; ++i) pr[i] = __builtin_amdgcn_raw_buffer_load_b128(rs, go + (i & 3) * 896, (i >> 2) * 3584, 16);
      SB();
      rounds(3, 5, true);
      BAR();
#pragma unroll
      for (int i = 8; i < 16; ++i) pr[i] = __builtin_amdgcn_raw_buffer_load_b128(rs, go + (i & 3) * 896, (i >> 2) * 3584, 16);
      SB();
#pragma unroll
      for (int i = 0; i < 16; ++i) d = mfmaw(__builtin_bit_cast(s16x8, sel), __builtin_bit_cast(s16x8, pr[i]), d);
      *(LAS u32x4*)fa = u32x4{pk2(d[0], d[1]), pk2(d[2], d[3]), pk2(d[4], d[5]), pk2(d[6], d[7])};
    }
    BAR();
    {
      const u32 z0 = L0 + L_FIN + 3392;
      const s16x8 z01 = cat(ld64(za[0]), ld64(za[1])), z23 = cat(ld64(za[2]), ld64(za[3])), z4 = cat(ld64(z0), ld64(za[4]));
      const s16x8 k0 = cat(ld64(qa[0][1]), ld64(qa[0][2])), k1a = cat(ld64(qa[1][0]), ld64(qa[1][1])), k2a = cat(ld64(qa[2][1]), ld64(qa[2][2]));
      const s16x8 k3a = cat(ld64(qa[3][0]), ld64(qa[3][1])), k4 = cat(ld64(qa[4][0]), ld64(qa[4][1]));
      const s16x4 zz = {0, 0, 0, 0};
      const s16x8 k1b = cat(ld64(qa[1][2]), zz), k2b = cat(zz, ld64(qa[2][0])), k3b = cat(zz, ld64(qa[3][2]));
      SB();
      f32x4 den[2], o[5][2];
      const f32x4 e4 = {1e-6f, 1e-6f, 1e-6f, 1e-6f}, n4 = {0.f, 0.f, 0.f, 0.f};
#pragma unroll
      for (int i = 0; i < 2; ++i) den[i] = mfma32(z01, p01[i], e4);
#pragma unroll
      for (int i = 0; i < 2; ++i) den[i] = mfma32(z23, p23[i], den[i]);
#pragma unroll
      for (int i = 0; i < 2; ++i) den[i] = mfma32(z4, p34[i], den[i]);
#pragma unroll
      for (int i = 0; i < 2; ++i) { o[0][i] = mfma32(k0, p01[i], n4); o[1][i] = mfma32(k1a, p01[i], n4); }
      SB();
      float rc[5][2];
#pragma unroll
      for (int i = 0; i < 2; ++i) {
        const float r0 = __builtin_amdgcn_rcpf(den[i][0]), r1 = __builtin_amdgcn_rcpf(den[i][1]), r2 = __builtin_amdgcn_rcpf(den[i][2]), r3 = __builtin_amdgcn_rcpf(den[i][3]);
        rc[0][i] = r0; rc[1][i] = g < 1 ? r0 : r1; rc[2][i] = g < 2 ? r1 : r2; rc[3][i] = g < 3 ? r2 : r3; rc[4][i] = r3;
      }
#pragma unroll
      for (int i = 0; i < 2; ++i) { o[1][i] = mfma32(k1b, p23[i], o[1][i]); o[2][i] = mfma32(k2a, p23[i], n4); o[3][i] = mfma32(k3a, p23[i], n4); }
#pragma unroll
      for (int i = 0; i < 2; ++i) { o[2][i] = mfma32(k2b, p01[i], o[2][i]); o[3][i] = mfma32(k3b, p34[i], o[3][i]); o[4][i] = mfma32(k4, p34[i], n4); }
#pragma unroll
      for (int i = 0; i < 12; ++i) { SG(0x8, 1); SG(0x402, 1); }
      SB();
#pragma unroll
      for (int je = 0; je < 5; ++je) { st128(rs, ob + je * 2048, cat(pk4(o[je][0] * rc[je][0]), pk4(o[je][1] * rc[je][1]))); SB(); }
    }
  } else {
    if (narrow) {
      if (b == 192 && wave == 2) {
        const GAS u32* fp = (const GAS u32*)(ws + WS_LNFLAG + (u32)(lane * 128));
#pragma unroll 1
        for (u32 sp = 0; sp < SPIN_CAP; ++sp) {
          const u32 f0 = __hip_atomic_load(fp, RLX_AGENT), f1 = __hip_atomic_load(fp + 2048, RLX_AGENT);
          const u32 f2 = __hip_atomic_load(fp + 4096, RLX_AGENT), f3 = __hip_atomic_load(fp + 6144, RLX_AGENT);
          if (__all(f0 == tag && f1 == tag && f2 == tag && f3 == tag)) break;
        }
        asm volatile("" ::: "memory");
        if (lane < 2) __hip_atomic_store((GAS u32*)(ws + (lane ? WS_EPOCH : WS_ALLFLAG)), tag, RLX_AGENT);
      }
      f32x4 acc[5];
      {
        const u32x2 bb = *(const GAS u32x2*)(P.b1 + fu * 128 + hw * 64 + ty * 16 + (u32)((g & 1) * 32 + (g >> 1) * 8));
#pragma unroll
        for (int j = 0; j < 5; ++j) acc[j] = f32x4{blo(bb[0]), bhi(bb[0]), blo(bb[1]), bhi(bb[1])};
      }
      const u32 aa0 = L0 + (8 * g + q4) * 128 + ((2 * hw + (p4 & 1)) ^ ((q4 >> 1) | ((g & 1) << 1))) * 32 + ty * 16 + (p4 >> 1) * 8;
      const u32 ha0 = L0 + L1_B + lane * 16;
      u32 as = 10 * 8192, hs = 0, naa = aa0 + as, nha = ha0;
      struct FN { s16x8 a[2], h[10]; };
      auto step = [&](FN& n, const FN& o) {
        const u32 aa = naa, ha = nha;
        BAR();
        n.a[0] = cat(ldtr(aa), ldtr(aa + 512)); n.a[1] = cat(ldtr(aa + 4096), ldtr(aa + 4608));
#pragma unroll
        for (int j = 0; j < 10; ++j) n.h[j] = ld128(ha + j * 1024);
        as = as == L1_B - 8192 ? 0 : as + 8192;
        hs = hs == (NB1 - 1) * 10240 ? 0 : hs + 10240;
        naa = aa0 + as; nha = ha0 + hs;
#pragma unroll
        for (int j = 0; j < 10; ++j) acc[j % 5] = mfma32(o.a[j / 5], o.h[j], acc[j % 5]);
        SG(0x100, 2);
#pragma unroll
        for (int j = 0; j < 6; ++j) { SG(0x8, 1); SG(0x100, 2); }
#pragma unroll
        for (int j = 0; j < 4; ++j) { SG(0x8, 1); SG(0x6, 2); }
        asm volatile("" : "+v"(naa), "+v"(nha));
        SB();
      };
      FN f0 = {}, f1;
#pragma unroll 1
      for (int r = 0; r < 32; r += 2) { step(f1, f0); step(f0, f1); }
      BAR();
#pragma unroll
      for (int j = 0; j < 10; ++j) acc[j % 5] = mfma32(f0.a[j / 5], f0.h[j], acc[j % 5]);
      const int go = WS_G + dt * 276480 + (fu * 2 + hw) * 5120 + (g & 1) * 1024 + (2 * ty + (g >> 1)) * 256 + c16 * 16;
#pragma unroll
      for (int j = 0; j < 5; j += 2) {
        const u32x4 o = swap16(__builtin_bit_cast(u32x4, gelu8(acc[j], acc[j < 4 ? j + 1 : j])));
        st128(rs, j < 4 || !(g & 1) ? go + j * 1024 : OOB, __builtin_bit_cast(s16x8, o));
      }
      BAR();
      const u32 ra = L0 + (4 * g + q4) * 640 + wave * 160 + p4 * 8;
      s16x8 f[5];
#pragma unroll
      for (int j = 0; j < 5; ++j) f[j] = cat(ldtr(ra + j * 32), ldtr(ra + j * 32 + 10240));
      publish_last(ws + WS_GFLAG + dt * 8192 + gi * 128, tag, lane, LCNT, 8);
      BAR();
      const int gw = WS_WO + (4 * (dt & 1) + wave) * 102400 + (xc * 4 + (dt >> 1)) * 5120 + lane * 16;
#pragma unroll
      for (int j = 0; j < 5; ++j) st128(rs, gw + j * 1024, f[j]);
      SB();
    } else {
    f32x4 acc[2][5];
#pragma unroll
    for (int t = 0; t < 2; ++t) {
      const u32x2 bb = *(const GAS u32x2*)(P.b1 + fu * 128 + wave * 64 + (u32)(t * 32 + g * 8));
      const f32x4 bi = {blo(bb[0]), bhi(bb[0]), blo(bb[1]), bhi(bb[1])};
#pragma unroll
      for (int j = 0; j < 5; ++j) acc[t][j] = bi;
    }
    s16x8 a1[2] = {}, h1[5] = {};
    constexpr int RP = 256;
    const u32 aa0 = L0 + (8 * g + q4) * RP + ((2 * wave) ^ (q4 | ((g & 1) << 2))) * 32 + p4 * 8;
    const u32 ha0 = L0 + L1_B + lane * 16;
    u32 as = 6 * 8192, hs = 0, naa = aa0 + as, nab = naa ^ 32, nha = ha0;
#pragma unroll 1
    for (int r = 0; r < 32; ++r) {
      const u32 aa = naa, ab = nab, ha = nha;
      BAR();
      s16x8 a0[2], h0[5];
      a0[0] = cat(ldtr(aa), ldtr(aa + 4 * RP)); h0[0] = ld128(ha); a0[1] = cat(ldtr(ab), ldtr(ab + 4 * RP));
#pragma unroll
      for (int j = 1; j < 5; ++j) h0[j] = ld128(ha + j * 1024);
#pragma unroll
      for (int j = 0; j < 5; ++j) { acc[0][j] = mfma32(a1[0], h1[j], acc[0][j]); acc[1][j] = mfma32(a1[1], h1[j], acc[1][j]); }
#pragma unroll
      for (int j = 0; j < 9; ++j) { SG(0x8, 1); SG(0x100, 1); }
      SG(0x8, 1);
      SB();
      a1[0] = cat(ldtr(aa + 32 * RP), ldtr(aa + 36 * RP)); a1[1] = cat(ldtr(ab + 32 * RP), ldtr(ab + 36 * RP));
#pragma unroll
      for (int j = 0; j < 5; ++j) h1[j] = ld128(ha + 5120 + j * 1024);
      as = as == L1_B - 64 * RP ? 0 : as + 64 * RP;
      hs = hs == (NB1 - 1) * 10240 ? 0 : hs + 10240;
      naa = aa0 + as; nab = naa ^ 32; nha = ha0 + hs;
#pragma unroll
      for (int j = 0; j < 5; ++j) { acc[0][j] = mfma32(a0[0], h0[j], acc[0][j]); acc[1][j] = mfma32(a0[1], h0[j], acc[1][j]); }
      SG(0x100, 1);
#pragma unroll
      for (int j = 0; j < 4; ++j) { SG(0x8, 1); SG(0x100, 2); }
#pragma unroll
      for (int j = 0; j < 5; ++j) { SG(0x8, 1); SG(0x6, 2); }
      SG(0x8, 1);
      asm volatile("" : "+v"(naa), "+v"(nab), "+v"(nha));
      SB();
    }
    BAR();
#pragma unroll
    for (int j = 0; j < 5; ++j) { acc[0][j] = mfma32(a1[0], h1[j], acc[0][j]); acc[1][j] = mfma32(a1[1], h1[j], acc[1][j]); }
      const int go = WS_G + dt * 276480 + (fu * 2 + wave) * 5120 + lane * 16;
#pragma unroll
      for (int j = 0; j < 5; ++j) st128(rs, go + j * 1024, gelu8(acc[0][j], acc[1][j]));
    }
    if (!late && !narrow) {
      publish_last(ws + WS_GFLAG + dt * 8192 + gi * 128, tag, lane, LCNT, 8);
      BAR();
    }
    ldx();
    begin3();
  }

  {
    rounds(roleA ? 5 : 1, roleA ? R_JOIN : PUBR, true);
    {
      asm volatile("" : "+v"(x01), "+v"(x23), "+v"(x4), "+v"(bs));
      const float bf = blo(bs);
      x01 = swap16(x01); x23 = swap16(x23);
      oc[0][0] += f32x4{blo(x01[0]), bhi(x01[0]), blo(x01[1]), bhi(x01[1])} + bf;
      oc[0][1] += f32x4{blo(x01[2]), bhi(x01[2]), blo(x01[3]), bhi(x01[3])} + bf;
      oc[0][2] += f32x4{blo(x23[0]), bhi(x23[0]), blo(x23[1]), bhi(x23[1])} + bf;
      oc[0][3] += f32x4{blo(x23[2]), bhi(x23[2]), blo(x23[3]), bhi(x23[3])} + bf;
      oc[0][4] += f32x4{blo(x4[0]), bhi(x4[0]), blo(x4[1]), bhi(x4[1])} + bf;
      if (late) publish_last(ws + WS_GFLAG + dt * 8192 + gi * 128, tag, lane, LCNT, 8);
      else if (roleA || narrow)
        publish(ws + (roleA ? WS_OFLAG + (2 * rt + hw) * 2048 + (ty * 8 + hg) * 128 : WS_GFLAG + (4 * (dt & 1) + wave) * 8192 + (16 + xc * 4 + (dt >> 1)) * 128), tag, lane);
      SB();
    }
    rounds(roleA ? R_JOIN : PUBR, 27, true);
    na3 = oa0 + sa3; nb3 = na3 ^ 8;
    static_assert(36 % NB3 < 2, "");
    u32 ew = L0 + L3_B + 20480 + wave * 5120 + lane * 16, er = L0 + L3_B + 20480 + (wave ^ 1) * 5120 + lane * 16, xo4 = xr + 128 + g * 8;
    asm volatile("" : "+v"(ew), "+v"(er), "+v"(xo4));
    rounds(27, 37, false);
    mma(fa0);
#pragma unroll
    for (int j = 0; j < 5; ++j) *(LAS f32x4*)(ew + j * 1024) = oc[1][j];
    SG(0x8, 6);
#pragma unroll
    for (int j = 0; j < 4; ++j) { SG(0x200, 1); SG(0x8, 1); }
    SG(0x200, 1);
    SB();
    BAR();
#pragma unroll
    for (int j = 0; j < 5; ++j) oc[1][j] = *(const LAS f32x4*)(er + j * 1024);
    SB();
#pragma unroll
    for (int j = 0; j < 5; ++j) oc[0][j] += oc[1][j];
    *(GAS u32x4*)(P.out + xo) = swap16(__builtin_bit_cast(u32x4, cat(pk4(oc[0][0]), pk4(oc[0][1]))));
    *(GAS u32x4*)(P.out + xo + 64) = swap16(__builtin_bit_cast(u32x4, cat(pk4(oc[0][2]), pk4(oc[0][3]))));
    *(GAS s16x4*)(P.out + xo4) = pk4(oc[0][4]);
    asm volatile(".Lcode_exit_b:" ::: "memory");
  }
}

extern "C" void kernel_launch(void* const* d_in, const int* in_sizes, int n_in, void* d_out, int out_size, void* d_ws,
                              size_t ws_size, hipStream_t stream) {
  Params p;
  p.x = (const char*)d_in[0];
  p.wqkv = (const char*)d_in[1];
  p.wo = (const char*)d_in[2];
  p.w1 = (const char*)d_in[4];
  p.b1 = (const char*)d_in[5];
  p.w2 = (const char*)d_in[6];
  p.b2 = (const char*)d_in[7];
  p.out = (char*)d_out;
  p.ws = (char*)d_ws;
  (void)hipFuncSetAttribute((const void*)fused_fwd, hipFuncAttributeMaxDynamicSharedMemorySize, L_BYTES);
  fused_fwd<<<256, 320, L_BYTES, stream>>>(p);
}
```

```cpp
#include <hip/hip_runtime.h>
#include <hip/hip_bf16.h>
#pragma clang diagnostic ignored "-Wint-to-pointer-cast"

#define GAS __attribute__((address_space(1)))
#define LAS __attribute__((address_space(3)))
#define DI __device__ __forceinline__
typedef unsigned u32;
typedef unsigned long long u64;
typedef short s16x4 __attribute__((ext_vector_type(4)));
typedef short s16x8 __attribute__((ext_vector_type(8)));
typedef __bf16 b16x2 __attribute__((ext_vector_type(2)));
typedef __bf16 b16x8 __attribute__((ext_vector_type(8)));
typedef float f32x2 __attribute__((ext_vector_type(2)));
typedef float f32x4 __attribute__((ext_vector_type(4)));
typedef float f32x16 __attribute__((ext_vector_type(16)));
typedef unsigned u32x2 __attribute__((ext_vector_type(2)));
typedef unsigned u32x4 __attribute__((ext_vector_type(4)));

constexpr int WS_EPOCH = 0;
constexpr int WS_ALLFLAG = 1024;
constexpr int WS_LNFLAG = 196608;
constexpr int WS_GFLAG = 720896;
constexpr int WS_KVFLAG = 65536;
constexpr int WS_OFLAG = 131072;
constexpr int WS_PART = 262144;
constexpr int WS_SC = 8192;
constexpr int WS_H = 786432;
constexpr int WS_G = WS_H + 2621440;
constexpr int WS_O = WS_G + 2211840;
constexpr int WS_WO = WS_O + 2621440;
constexpr int WS_END = WS_WO + 819200;
constexpr int OOB = 0x40000000;
constexpr int L_BYTES = 163840;
constexpr int L_LN = 131072, LN_PITCH = 1296;
constexpr int NB3 = 5;
constexpr int NB1 = 4, NA1 = 7;
constexpr int LA3 = NB3 - 1, NA3 = 18 - NB3, H3 = 2 * (NA3 - LA3 - 2);
constexpr int LQ_B = 122880, L1_B = NA1 * 16384, L3_B = NA3 * 8192;
constexpr int L_KV = L3_B, L_FIN = L3_B + NB3 * 10240, L_SC = L_FIN + 3584;
constexpr int a3(int t) { return t < 1 || t > 35 - LA3 ? 0 : t <= H3 ? 4 : 8; }
constexpr int w3(int r) { int n = a3(r - LA3); for (int t = r - LA3 + 1; t < r; ++t) n += a3(t) + (t <= 36 - LA3 ? 10 : 0); return n; }
#define APRE 20
#define NPK_A 8
#define R_JOIN 9
#define T2B 1
#define LOWP 0
#define LPK 22
#define NTCH 7
#define EARLY 16
#define PUBR 3
#define TH_PA 24
#define TH_P3 8
#define SLP 2
#define PRE_A 2
#define MID_A 2
#define PRE_B 3
#define MID_B 3
#define XP 1
constexpr u32 SPIN_CAP = 1u << 16;

#define BAR() do { asm volatile("" ::: "memory"); __builtin_amdgcn_s_waitcnt(0xC07F); asm volatile("s_barrier" ::: "memory"); } while (0)
#define WAITV(n) asm volatile("s_waitcnt vmcnt(" #n ")" ::: "memory")
#define WAITX(n) asm volatile("s_waitcnt vmcnt(%0)" ::"i"((n) > 63 ? 63 : (n)) : "memory")
#define STAMP_AT(k)
#define STAMP_DECL
#define ACC_TO(k)
#define STAMP_OUT()
#define ACC0(k)
#define ACC1(k)
#define ACC3(k)
#define LW1(n) do { ACC1(13); WAITX(n); ACC1(11); } while (0)
#define LBAR() asm volatile("s_barrier" ::: "memory")
#define LB1() do { LBAR(); ACC1(12); } while (0)
#define LW3(n) do { ACC3(13); WAITX(n); ACC3(11); } while (0)
#define LB3() do { LBAR(); ACC3(12); } while (0)
#define RLX_AGENT __ATOMIC_RELAXED, __HIP_MEMORY_SCOPE_AGENT

DI f32x4 mfma32(s16x8 a, s16x8 b, f32x4 c) {
  return __builtin_amdgcn_mfma_f32_16x16x32_bf16(__builtin_bit_cast(b16x8, a), __builtin_bit_cast(b16x8, b), c, 0, 0, 0);
}
DI f32x16 mfmaw(s16x8 a, s16x8 b, f32x16 c) {
  return __builtin_amdgcn_mfma_f32_32x32x16_bf16(__builtin_bit_cast(b16x8, a), __builtin_bit_cast(b16x8, b), c, 0, 0, 0);
}
DI s16x4 ldtr(u32 a) { return __builtin_amdgcn_ds_read_tr16_b64_v4i16((LAS s16x4*)a); }
DI s16x8 ld128(u32 a) { return *(const LAS s16x8*)a; }
DI s16x4 ld64(u32 a) { return *(const LAS s16x4*)a; }
DI s16x8 cat(s16x4 a, s16x4 b) { return __builtin_shufflevector(a, b, 0, 1, 2, 3, 4, 5, 6, 7); }
DI u32 pk2(float a, float b) { f32x2 v = {a, b}; return __builtin_bit_cast(u32, __builtin_convertvector(v, b16x2)); }
DI s16x4 pk4(f32x4 v) { u32x2 r = {pk2(v[0], v[1]), pk2(v[2], v[3])}; return __builtin_bit_cast(s16x4, r); }
DI float blo(u32 u) { return __uint_as_float(u << 16); }
DI float bhi(u32 u) { return __uint_as_float(u & 0xffff0000u); }
template <int C> DI float dpp_add(float s) { return s + __int_as_float(__builtin_amdgcn_update_dpp(0, __float_as_int(s), C, 0xf, 0xf, true)); }
DI float sum32(float s) {
  s = dpp_add<0x121>(s); s = dpp_add<0x122>(s); s = dpp_add<0x124>(s); s = dpp_add<0x128>(s);
  auto r = __builtin_amdgcn_permlane16_swap(__float_as_uint(s), __float_as_uint(s), false, false);
  return __uint_as_float(r[0]) + __uint_as_float(r[1]);
}
#define SG(m, n) __builtin_amdgcn_sched_group_barrier(m, n, 0)
#define SB() __builtin_amdgcn_sched_barrier(0)
#define AL() do { asm volatile(".p2align 3"); SB(); } while (0)
template <int N> DI void phi(const f32x4 (&u)[N], const f32x4* sc, s16x4 (&o)[N]) {
  const float L = 1.4426950408889634f;
  f32x4 a[N], y[N];
  SB(); AL();
#pragma unroll
  for (int i = 0; i < N; ++i) a[i] = __builtin_elementwise_fma(u[i], sc[i], f32x4{1.f, 1.f, 1.f, 1.f});
  SB();
#pragma unroll
  for (int i = 0; i < N; ++i) y[i] = __builtin_elementwise_fma(a[i], f32x4{L, L, L, L}, f32x4{-L, -L, -L, -L});
  SB();
#pragma unroll
  for (int i = 0; i < 4 * N; ++i) y[i >> 2][i & 3] = __builtin_amdgcn_fmed3f(__builtin_amdgcn_exp2f(y[i >> 2][i & 3]), 0.f, 1.f);
  SB();
#pragma unroll
  for (int i = 0; i < 4 * N; ++i) y[i >> 2][i & 3] = __int_as_float(max(__float_as_int(a[i >> 2][i & 3]), __float_as_int(y[i >> 2][i & 3])));
  SB();
#pragma unroll
  for (int i = 0; i < N; ++i) y[i] += 1e-6f;
  SB();
#pragma unroll
  for (int i = 0; i < N; ++i) o[i] = pk4(y[i]);
}
DI s16x8 gelu8(f32x4 u0, f32x4 u1) {
  const float K = -2.f * 0.7978845608028654f * 1.4426950408889634f, K3 = K * 0.044715f;
  SB(); AL();
  f32x4 y0 = u0 * u0, y1 = u1 * u1;
  SB();
  y0 = __builtin_elementwise_fma(y0, f32x4{K3, K3, K3, K3}, f32x4{K, K, K, K});
  y1 = __builtin_elementwise_fma(y1, f32x4{K3, K3, K3, K3}, f32x4{K, K, K, K});
  SB();
  y0 *= u0; y1 *= u1;
  SB();
#pragma unroll
  for (int i = 0; i < 4; ++i) y0[i] = __builtin_amdgcn_exp2f(y0[i]);
#pragma unroll
  for (int i = 0; i < 4; ++i) y1[i] = __builtin_amdgcn_exp2f(y1[i]);
  SB();
  y0 += 1.f; y1 += 1.f;
  SB();
#pragma unroll
  for (int i = 0; i < 4; ++i) y0[i] = __builtin_amdgcn_rcpf(y0[i]);
#pragma unroll
  for (int i = 0; i < 4; ++i) y1[i] = __builtin_amdgcn_rcpf(y1[i]);
  SB();
  y0 *= u0; y1 *= u1;
  SB();
  return cat(pk4(y0), pk4(y1));
}
DI void st128(__amdgpu_buffer_rsrc_t rs, int off, s16x8 v) { __builtin_amdgcn_raw_buffer_store_b128(__builtin_bit_cast(u32x4, v), rs, off, 0, 16); }
DI void st64(__amdgpu_buffer_rsrc_t rs, int off, s16x4 v) { __builtin_amdgcn_raw_buffer_store_b64(__builtin_bit_cast(u32x2, v), rs, off, 0, 16); }
DI u32 mix(u32 e) { return e * 0x9E3779B1u + 0x7F4A7C15u; }

#define GL(v, off, sc) "global_load_lds_dwordx4 " v ", %0 offset:" #off sc "\n\t"
#define M0(bias) "s_add_u32 m0, %1, " #bias "\n\ts_nop 0\n\t"
#define RING m = m == HI - STEP ? LO : m + STEP
template <int STEP, int LO, int HI> DI void gl4(const char* base, u32& m, const u32 (&v)[4]) {
  asm volatile(M0(0) GL("%2", 0, "") GL("%3", 1024, "") GL("%4", 2048, "") GL("%5", 3072, "")
               ::"s"(base), "s"(m), "v"(v[0]), "v"(v[1]), "v"(v[2]), "v"(v[3]) : "memory", "scc");
  RING;
}
template <int STEP, int LO, int HI> DI void gl8(const char* base, u32& m, const u32 (&v)[8]) {
  asm volatile(M0(4096) GL("%2", -4096, "") GL("%3", -3072, "") GL("%4", -2048, "") GL("%5", -1024, "")
               GL("%6", 0, "") GL("%7", 1024, "") GL("%8", 2048, "") GL("%9", 3072, "")
               ::"s"(base), "s"(m), "v"(v[0]), "v"(v[1]), "v"(v[2]), "v"(v[3]), "v"(v[4]), "v"(v[5]), "v"(v[6]), "v"(v[7]) : "memory", "scc");
  RING;
}
template <int STEP, int LO, int HI> DI void gl8w(const char* base, u32& m, const u32 (&v)[8]) {
  asm volatile("s_add_u32 m0, %1, 3840\n\ts_mov_b32 exec_hi, 0x0fffffff\n\t" GL("%2", -3840, "") GL("%3", -2880, "") GL("%4", -1920, "") GL("%5", -960, "")
               GL("%6", 0, "") GL("%7", 960, "") GL("%8", 1920, "") GL("%9", 2880, "") "s_mov_b32 exec_hi, -1"
               ::"s"(base), "s"(m), "v"(v[0]), "v"(v[1]), "v"(v[2]), "v"(v[3]), "v"(v[4]), "v"(v[5]), "v"(v[6]), "v"(v[7]) : "memory", "scc");
  RING;
}
DI void gl5(const char* base, u32 m, const u32 (&v)[5]) {
  asm volatile(M0(4096) GL("%2", -4096, "") GL("%3", -3072, "") GL("%4", -2048, "") GL("%5", -1024, "") GL("%6", 0, "")
               ::"s"(base), "s"(m), "v"(v[0]), "v"(v[1]), "v"(v[2]), "v"(v[3]), "v"(v[4]) : "memory", "scc");
}
#define GL8S M0(4096) GL("%2", -4096, " sc1") GL("%2", -3072, " sc1") GL("%2", -2048, " sc1") GL("%2", -1024, " sc1") \
             GL("%2", 0, " sc1") GL("%2", 1024, " sc1") GL("%2", 2048, " sc1") GL("%2", 3072, " sc1")
template <int STEP, int LO, int HI> DI void glin8(const char* base, u32& m, u32 vl4) {
  asm volatile(GL8S ::"s"(base), "s"(m), "v"(vl4) : "memory", "scc");
  RING;
}
template <int STEP, int LO, int HI> DI void glin10(const char* base, u32& m, u32 vl4, u32 vl8) {
  asm volatile(GL8S M0(8192) GL("%3", 0, " sc1") GL("%3", 1024, " sc1")
               ::"s"(base), "s"(m), "v"(vl4), "v"(vl8) : "memory", "scc");
  RING;
}
template <int STEP, int LO, int HI> DI void glin55(const char* base, u32& m, u32 vl4, u32 vl4b) {
  asm volatile(M0(4096) GL("%2", -4096, " sc1") GL("%2", -3072, " sc1") GL("%2", -2048, " sc1") GL("%2", -1024, " sc1") GL("%2", 0, " sc1")
               M0(9216) GL("%3", -4096, " sc1") GL("%3", -3072, " sc1") GL("%3", -2048, " sc1") GL("%3", -1024, " sc1") GL("%3", 0, " sc1")
               ::"s"(base), "s"(m), "v"(vl4), "v"(vl4b) : "memory", "scc");
  RING;
}
template <int STEP, int LO, int HI> DI void glin2222(const char* base, u32& m, const u32 (&v)[4]) {
  asm volatile(M0(4096) GL("%2", -4096, " sc1") GL("%2", -3072, " sc1") GL("%3", -2048, " sc1") GL("%3", -1024, " sc1")
               GL("%4", 0, " sc1") GL("%4", 1024, " sc1") GL("%5", 2048, " sc1") GL("%5", 3072, " sc1")
               ::"s"(base), "s"(m), "v"(v[0]), "v"(v[1]), "v"(v[2]), "v"(v[3]) : "memory", "scc");
  RING;
}
DI void glin1(const char* base, u32 m, u32 v) { asm volatile(M0(0) GL("%2", 0, " sc1")::"s"(base), "s"(m), "v"(v) : "memory", "scc"); }
DI u32x4 swap16(u32x4 t) {
  const auto s0 = __builtin_amdgcn_permlane16_swap(t[0], t[2], false, false), s1 = __builtin_amdgcn_permlane16_swap(t[1], t[3], false, false);
  return u32x4{s0[0], s1[0], s0[1], s1[1]};
}
DI void touch(const char* base, u32 m, u32 v) {
  asm volatile("s_mov_b32 m0, %1\n\ts_nop 0\n\tglobal_load_lds_dword %2, %0" ::"s"(base), "s"(m), "v"(v) : "memory");
}
DI void poll(const char* f, int idx, u32 tag) {
  const GAS u32* p = (const GAS u32*)(f + (u32)(idx * 128));
#pragma unroll 1
  for (u32 sp = 0; sp < SPIN_CAP; ++sp) {
    u32 v = __hip_atomic_load(p, RLX_AGENT);
    if (__all(v == tag)) break;
    __builtin_amdgcn_s_sleep(SLP);
  }
  asm volatile("" ::: "memory");
}
DI u32 peek(const char* f, int idx) {
  u32 v;
  asm volatile("global_load_dword %0, %1, %2 sc1" : "=&v"(v) : "v"(idx * 128), "s"(f) : "memory");
  return v;
}
DI void publish(char* f, u32 tag, int lane) {
  WAITV(0);
  if (lane == 0) __hip_atomic_store((GAS u32*)f, tag, RLX_AGENT);
}
DI void publish_last(char* f, u32 tag, int lane, u32 cnt, u32 n) {
  WAITV(0);
  if (lane == 0 && __hip_atomic_fetch_add((LAS u32*)cnt, 1u, __ATOMIC_RELAXED, __HIP_MEMORY_SCOPE_WORKGROUP) == n - 1)
    __hip_atomic_store((GAS u32*)f, tag, RLX_AGENT);
}

struct Params {
  const char *x, *wqkv, *wo, *w1, *b1, *w2, *b2;
  char *out, *ws;
};
#define KEEP_RAW() asm volatile("" ::"v"(raw[0]), "v"(raw[1]), "v"(raw[2]), "v"(raw[3]), "v"(raw[4]))
__global__ __launch_bounds__(320) void fused_fwd(Params P) {
  const int tid = threadIdx.x, lane = tid & 63;
  const int wave = __builtin_amdgcn_readfirstlane(tid >> 6);
  const int b = blockIdx.x, xc = b & 7, jb = b >> 3;
  char* const ws = P.ws;
  constexpr u32 L0 = 0;
  const u32 LDUM = L0 + L_BYTES - 256, LCNT = L0 + L_BYTES - 512;
  const int c16 = lane & 15, g = lane >> 4, q4 = c16 >> 2, p4 = lane & 3;
  STAMP_DECL;
  STAMP_AT(0);
  {
    u64 pc; int left;
    asm volatile("s_getpc_b64 %0\n.Lcode_here:\n\ts_mov_b32 %1, .Lcode_exit_a-.Lcode_here\n\ts_max_i32 %1, %1, .Lcode_exit_b-.Lcode_here"
                 : "=s"(pc), "=&s"(left)::"scc");
    touch((const char*)pc, LDUM, min(((jb * 5 + wave) * 2 + (lane & 1)) * 128, left));
  }
  const int row = 2 * (wave & 3) + (lane >> 5), hl = lane & 31;
  u32x2 raw[5];
  asm volatile("global_load_dwordx2 %0, %5, %6\n\tglobal_load_dwordx2 %1, %5, %6 offset:256\n\tglobal_load_dwordx2 %2, %5, %6 offset:512\n\t"
               "global_load_dwordx2 %3, %5, %6 offset:768\n\tglobal_load_dwordx2 %4, %5, %6 offset:1024"
               : "=&v"(raw[0]), "=&v"(raw[1]), "=&v"(raw[2]), "=&v"(raw[3]), "=&v"(raw[4])
               : "v"(row * 1280 + hl * 8), "s"(P.x + b * (8 * 1280)) : "memory");
  u32 e0 = *(const __attribute__((address_space(4))) u32*)(ws + WS_EPOCH);
  const bool roleA = b < 128;
  const int hg = 2 * (xc & 3) + (jb & 1), rt = 8 * (xc >> 2) + (jb >> 1);
  const int dt = jb & 7, grp = (jb >> 3) & 1;
  const bool late = !roleA && xc >= 5, narrow = !roleA && xc < 5 && grp;
  const int fu = late ? 4 * xc - 5 + 2 * grp : grp ? xc : 5 + 2 * xc;
  const int gi = late ? 2 * xc + grp : grp ? xc : 5 + xc;
  const int st = 4 * xc + (jb >> 3), dt3 = jb & 7;

  if (__builtin_expect(wave == 4, 1)) {
    __builtin_amdgcn_s_setprio(3);
    const u32 vl4 = lane * 16 + 4096, vl8 = vl4 + 4096;
    const u32 u0 = (lane >> 3) * 4096 + ((lane & 7) ^ (g << 1)) * 16;
    const u32 u[4] = {u0, u0 + 31744, u0 + 2 * 31744, u0 + 3 * 31744};
    const char* w2p = P.w2 + st * 128;
    u32 sa3 = 0;
    auto ahalf = [&]() { gl4<4096, 0, L3_B>(w2p, sa3, u); w2p += 131072; };
    const char* const gfl = ws + WS_GFLAG + dt3 * 8192;
    const char* bp = ws + WS_G + dt3 * 276480;
    u32 gf, sb3 = L3_B;
    auto bunit3 = [&]() { glin10<10240, L3_B, L3_B + NB3 * 10240>(bp, sb3, vl4, vl8); bp += 10240; };
    auto start3 = [&](int done) {
#pragma unroll 1
      for (int i = 0; i < 4; ++i) bunit3();
#pragma unroll 1
      for (int i = done; i < 2 * NA3; i += 2) { ahalf(); ahalf(); }
    };
    if (!roleA) {
      u32 v[8];
      {
        const u32 e = narrow ? (lane >> 3) * 3456 + ((lane & 7) ^ ((g & 1) << 1)) * 16 : g * 3456 + (c16 ^ (g << 1)) * 16;
        const u32 stp = (narrow ? 8 : 4) * 3456 - 1024;
#pragma unroll
        for (int i = 0; i < 8; ++i) {
          const u32 col = narrow ? ((lane & 7) ^ ((g & 1) << 1)) * 16 : (c16 ^ (g << 1)) * 16;
          const u32 flip = narrow ? (i & 1) * 64 : ((i >> 1) & 1) * 128;
          v[i] = e - col + (col ^ flip) + i * stp + 4096;
        }
      }
      const char* wb = P.w1 + fu * 128;
      const char* hb = ws + WS_H + dt * 5120;
      const u32 d0 = dt ^ (XP && late ? grp : 0);
      u32 ia = 0, ib2 = 0, sa = narrow ? 10 * 8192 : 6 * 8192, sb = L1_B;
      const u32 istep = narrow ? 2 : 1;
      auto a8 = [&]() { gl8<8192, 0, L1_B>(wb + (ia ^ (8 * d0)) * 110592, sa, v); ia += istep; };
      auto bunit = [&]() { glin55<10240, L1_B, L1_B + NB1 * 10240>(hb + (ib2 ^ (4 * d0)) * 81920, sb, vl4, vl4 + 40960); ++ib2; };
      STAMP_AT(1);
#pragma unroll 1
      for (int i = 0; i < PRE_B; ++i) a8();
      asm volatile("" : "+s"(e0));
      BAR(); LBAR(); STAMP_AT(14);
#pragma unroll 1
      for (int i = 0; i < MID_B; ++i) a8();
      LBAR();
      if (narrow) {
        u32 w5[5];
#pragma unroll
        for (int i = 0; i < 5; ++i) { const u32 n = 64 * i + lane; w5[i] = n / 40u * 640 + lane * 16 + 4096; }
        const char* wp = P.wo + (xc * 4 + (dt >> 1)) * (32 * 1280) + (dt & 1) * 640;
#pragma unroll 1
        for (int i = 0; i < 4; ++i) gl5(wp + i * (8 * 1280), 7 * 8192 + i * 5120, w5);
      }
#pragma unroll 1
      for (int i = PRE_B + MID_B + (narrow ? 3 : 0); i < 10; ++i) { WAITV(32); a8(); }
      STAMP_AT(2);
      const u32 tg = mix(e0);
      u32 f0 = peek(ws + WS_LNFLAG, (d0 & 6) * 32 + lane);
      a8(); a8();
      WAITV(16);
      if (narrow) LBAR();
      KEEP_RAW();
      asm volatile("" : "+v"(f0));
      STAMP_AT(15);
      if (!__all(f0 == tg)) poll(ws + WS_LNFLAG, (d0 & 6) * 32 + lane, tg);
      STAMP_AT(3); ACC_TO(-1);
      bunit(); bunit(); bunit();
      a8(); a8();
      LW1(36); LB1(); bunit();
      LW1(36);
      if (narrow) {
        LB1(); bunit(); LW1(20);
        LB1(); a8(); bunit(); LW1(28);
        f0 = peek(ws + WS_ALLFLAG, 0);
#pragma unroll 1
        for (int r = 3; r < 5; ++r) { LB1(); a8(); bunit(); LW1(28); }
      } else {
#pragma unroll 1
        for (int r = 1; r < 3; ++r) { LB1(); a8(); a8(); bunit(); LW1(52); }
        f0 = peek(ws + WS_ALLFLAG, 0);
#pragma unroll 1
        for (int r = 3; r < 5; ++r) { LB1(); a8(); a8(); bunit(); LW1(52); }
      }
      asm volatile("" : "+v"(f0));
      STAMP_AT(5);
      if (!__all(f0 == tg)) poll(ws + WS_ALLFLAG, 0, tg);
      STAMP_AT(6);
      if (narrow) {
#pragma unroll 1
        for (int r = 5; r < 29; ++r) { LB1(); if (r & 3) a8(); bunit(); LW1(28); }
      } else {
#pragma unroll 1
        for (int r = 5; r < 29; ++r) { LB1(); a8(); if (r & 3) a8(); bunit(); LW1(44); }
      }
      LB1();
      if (narrow) { LW1(10); LB1(); LW1(0); LB1(); }
      else {
        constexpr int E1 = 3 * EARLY / 8;
#pragma unroll 1
        for (int i = 0; i < E1; ++i) ahalf();
        LW1(18 + 4 * E1); LB1();
#pragma unroll 1
        for (int i = 0; i < E1; ++i) ahalf();
        LW1(8 * E1); LB1();
        if (!late) {
#pragma unroll 1
          for (int i = 2 * E1; i < EARLY; ++i) ahalf();
        }
      }
      if (!late) {
        LBAR();
        STAMP_AT(4); ACC0(11);
        if (narrow) LBAR();
#pragma unroll 1
        for (int i = narrow ? -16 : 0; i < 10; ++i) { ahalf(); WAITV(32); if (!narrow) WAITX(TH_P3); }
        gf = peek(gfl, lane < 5 ? lane : 0);
        WAITV(0);
        if (!narrow) LBAR();
      } else {
        ahalf(); ahalf(); ahalf(); ahalf();
        LBAR();
        STAMP_AT(4); ACC0(11);
#pragma unroll 1
        for (int i = 3 * EARLY / 8 * 2 + 4; i < LPK; ++i) ahalf();
        gf = peek(gfl, lane < 5 ? lane : 0);
        ahalf(); ahalf(); ahalf(); ahalf();
        WAITV(16);
      }
      asm volatile("" : "+v"(gf));
      if (!__all(gf == tg)) poll(gfl, lane < 5 ? lane : 0, tg);
      STAMP_AT(7); ACC_TO(-1);
      start3(late ? LPK + 4 : narrow ? 26 : EARLY + 10);
    } else {
      const u32 hi = lane >= 30, cc = lane - 30 * hi, sg = (cc >= 10) + (cc >= 20);
      u32 v[8];
#pragma unroll
      for (int i = 0; i < 8; ++i) v[i] = (hi + 2 * i + 1) * 3840 + sg * 1120 + cc * 16 - i * 960;
      const char* wb = P.wqkv + hg * 160;
      const char* xb = ws + WS_H + rt * 163840;
      const u32 x0 = (lane ^ ((g & 1) << 3)) * 16 + 4096, xv[4] = {x0, x0 + 38912, x0 + 2 * 38912, x0 + 3 * 38912};
      const u32 rot = (5 * (2 * (rt & 7) + (rt >> 3))) >> 2;
      u32 oa = rot * 122880, ob = rot * 2048, sa = 0, sb = LQ_B;
      auto a8 = [&]() { gl8w<7680, 0, LQ_B>(wb + oa, sa, v); oa = oa == 39 * 61440 ? 0 : oa + 61440; };
      auto bunit = [&]() { glin2222<8192, LQ_B, LQ_B + 4 * 8192>(xb + ob, sb, xv); ob = ob == 19 * 2048 ? 0 : ob + 2048; };
      STAMP_AT(1);
#pragma unroll 1
      for (int i = 0; i < PRE_A; ++i) a8();
      asm volatile("" : "+s"(e0));
      BAR(); LBAR(); STAMP_AT(14);
#pragma unroll 1
      for (int i = 0; i < MID_A; ++i) a8();
      LBAR();
#pragma unroll 1
      for (int i = PRE_A + MID_A; i < NPK_A; ++i) { WAITV(24); a8(); }
      STAMP_AT(2);
      u32 f0 = peek(ws + WS_LNFLAG, rt * 16 + c16);
      a8(); a8();
      WAITV(16);
      KEEP_RAW();
      asm volatile("" : "+v"(f0));
      STAMP_AT(15);
      if (!__all(f0 == mix(e0))) poll(ws + WS_LNFLAG, rt * 16 + c16, mix(e0));
      STAMP_AT(3); ACC_TO(-1);
      bunit(); bunit(); bunit();
      glin1(ws + WS_SC + rt * 512, L_SC, (lane & 31) * 16);
#pragma unroll 1
      for (int i = NPK_A + 2; i < 12; ++i) a8();
      LW1(16 + 8 * (10 - NPK_A));
#pragma unroll 1
      for (int r = 1; r < 17; ++r) { LB1(); a8(); if (r & 3) a8(); bunit(); LW1(40); }
      LB1(); bunit();
      LW1(24); LB1();
      const char* tb = P.w2 + xc * 512 + jb * (108 * 4096);
      const u32 tv = (lane >> 2) * 4096 + p4 * 128;
#pragma unroll 1
      for (int i = 0; i < NTCH; ++i) touch(tb + (i == 6 ? 92 : 16 * i) * 4096, LDUM, tv);
      LW1(8 + NTCH); LB1(); LW1(NTCH); LB1();
      BAR();
      STAMP_AT(4); ACC0(11);
      const u32 tg = mix(e0);
      const char* const kfl = ws + WS_KVFLAG + hg * 2048;
      if (LOWP) __builtin_amdgcn_s_setprio(0);
#pragma unroll 1
      for (int i = 0; i < APRE; ++i) { ahalf(); WAITV(24); }
      LBAR(); LBAR();
      if (LOWP) __builtin_amdgcn_s_setprio(3);
#pragma unroll 1
      for (int i = APRE; i < 22; ++i) { ahalf(); WAITX(TH_PA); }
      gf = peek(gfl, lane < 5 ? lane : 0);
      ahalf(); ahalf(); ahalf(); ahalf();
      WAITV(16);
      asm volatile("" : "+v"(gf));
      if (!__all(gf == tg)) poll(gfl, lane < 5 ? lane : 0, tg);
      STAMP_AT(7); ACC_TO(-1);
      bunit3(); bunit3(); bunit3();
      gf = peek(kfl, lane & 7);
      bunit3(); bunit3();
      WAITV(41); LBAR(); WAITV(31); LBAR(); WAITV(20);
      asm volatile("" : "+v"(gf));
      STAMP_AT(8);
      if (!__all(gf == tg)) poll(kfl, lane & 7, tg);
      STAMP_AT(9);
      gf = peek(kfl, 8 + (lane & 7));
      LBAR(); WAITV(11); LBAR(); WAITV(0); LBAR();
      asm volatile("" : "+v"(gf));
      if (!__all(gf == tg)) poll(kfl, 8 + (lane & 7), tg);
      LBAR();
      STAMP_AT(5);
      gf = peek(gfl, lane < 5 ? 5 + lane : 5);
      WAITV(0);
      asm volatile("" : "+v"(gf));
      const bool ready = __all(gf == tg);
      if (!ready) { LBAR(); poll(gfl, lane < 5 ? 5 + lane : 5, tg); }
      bunit3(); bunit3();
      if (ready) LBAR();
      bunit3(); bunit3(); ahalf(); ahalf(); ahalf(); ahalf();
      WAITV(16);
    }
    const u32 tag = mix(e0);
    {
      auto bunit = bunit3;
      if (!roleA) {
        LW3(30); LB3();
        gf = peek(gfl, lane < 5 ? 5 + lane : 5);
        bunit();
        LW3(30); LB3(); ahalf();
        LW3(25); LB3();
        WAITV(14);
        asm volatile("" : "+v"(gf));
        STAMP_AT(8);
        if (!__all(gf == tag)) poll(gfl, lane < 5 ? 5 + lane : 5, tag);
        STAMP_AT(9);
        bunit(); bunit(); ahalf();
      }
#pragma unroll 1
      for (int r = roleA ? 5 : 3; r < 7; ++r) { LB3(); bunit(); ahalf(); LW3(42); }
      gf = peek(gfl, lane < 6 ? 10 + lane : 10);
#pragma unroll 1
      for (int r = 7; r < 11; ++r) { LB3(); bunit(); ahalf(); LW3(46); }
      asm volatile("" : "+v"(gf));
      if (!__all(gf == tag)) poll(gfl, lane < 6 ? 10 + lane : 10, tag);
#pragma unroll 1
      for (int r = 11; r < 18; ++r) { LB3(); bunit(); ahalf(); if (r > H3) ahalf(); LW3(46); }
#pragma unroll 1
      for (int r = 18; r < 20; ++r) { LB3(); bunit(); ahalf(); ahalf(); LW3(62); }
      u32 of = peek(ws + WS_OFLAG + st * 2048, c16);
      gf = peek(gfl, lane < 20 ? 16 + lane : 16);
#pragma unroll 1
      for (int r = 20; r < 22; ++r) { LB3(); bunit(); ahalf(); ahalf(); LW3(62); }
      LB3(); bunit(); LW3(54);
      LB3(); LW3(36);
      asm volatile("" : "+v"(of), "+v"(gf));
      if (!__all(of == tag && gf == tag)) { poll(ws + WS_OFLAG + st * 2048, c16, tag); poll(gfl, lane < 20 ? 16 + lane : 16, tag); }
      const char* ap = ws + WS_O + st * 81920 + dt3 * 8192;
      bp = ws + WS_WO + dt3 * 112640;
#pragma unroll 1
      for (int r = 24; r < 34; ++r) {
        LB3();
        glin8<8192, 0, L3_B>(ap, sa3, vl4);
        bunit();
        ap += 8192;
        if (r + dt3 == 33) { ap -= 81920; bp -= 102400; }
        LW3(36);
      }
      LB3(); LW3(18); LB3(); LW3(0); LB3();
    }
    STAMP_AT(10); ACC0(12);
    STAMP_OUT();
    asm volatile(".Lcode_exit_a:" ::: "memory");
    return;
  }

  const int hw = wave >> 1, ty = wave & 1;
  const __amdgpu_buffer_rsrc_t rs = __builtin_amdgcn_make_buffer_rsrc(ws, 0, WS_END, 0x00020000);
  {
    u32 ra = L0 + L_LN + q4 * LN_PITCH + (10 * wave + g) * 32 + p4 * 8, wa = L0 + L_LN + row * LN_PITCH + hl * 8;
    int go = WS_H + (b >> 2) * 40960 + (10 * wave + g) * 1024 + (b & 3) * 256 + c16 * 16, g1 = go + 4096, g2 = g < 2 ? go + 8192 : OOB;
    int gs = hl == 0 ? WS_SC + (8 * b + row) * 4 : OOB;
    *(LAS u32*)LCNT = 0;
    asm volatile("" : "+v"(ra), "+v"(go), "+v"(g1), "+v"(g2), "+v"(gs), "+v"(wa), "+s"(e0));
    f32x2 v[10];
    {
      float u[20];
#define UNP(n, o, i) "s_waitcnt vmcnt(" #n ")\n\tv_lshlrev_b32 %0, 16, %4\n\tv_and_b32 %1, 0xffff0000, %4\n\tv_lshlrev_b32 %2, 16, %5\n\tv_and_b32 %3, 0xffff0000, %5" \
      : "=&v"(u[o]), "=&v"(u[o + 1]), "=&v"(u[o + 2]), "=&v"(u[o + 3]) : "v"(raw[i][0]), "v"(raw[i][1]) : "memory"
      asm volatile(UNP(4, 0, 0)); asm volatile(UNP(3, 4, 1)); asm volatile(UNP(2, 8, 2)); asm volatile(UNP(1, 12, 3)); asm volatile(UNP(0, 16, 4));
#pragma unroll
      for (int i = 0; i < 10; ++i) v[i] = f32x2{u[2 * i], u[2 * i + 1]};
    }
    f32x2 s = v[0] + v[1], s2 = v[2] + v[3];
#pragma unroll
    for (int i = 4; i < 10; i += 2) { s += v[i]; s2 += v[i + 1]; }
    s += s2;
    const float m = sum32(s[0] + s[1]) * (1.f / 640.f);
#pragma unroll
    for (int i = 0; i < 10; ++i) v[i] -= m;
    f32x2 qq = v[0] * v[0], q2 = v[1] * v[1];
#pragma unroll
    for (int i = 2; i < 10; i += 2) { qq += v[i] * v[i]; q2 += v[i + 1] * v[i + 1]; }
    qq += q2;
    const float var = sum32(qq[0] + qq[1]) * (1.f / 640.f);
    const float r1 = __builtin_amdgcn_rsqf(var + 1e-5f), v2 = var * r1 * r1 + 1e-5f, r2 = __builtin_amdgcn_rsqf(v2), r12 = r1 * r2;
#pragma unroll
    for (int i = 0; i < 5; ++i) {
      const f32x2 a = v[2 * i] * r12, c = v[2 * i + 1] * r12;
      *(LAS u32x2*)(wa + i * 256) = u32x2{pk2(a[0], a[1]), pk2(c[0], c[1])};
    }
    __builtin_amdgcn_raw_buffer_store_b32(__float_as_uint(v2 * r2), rs, gs, 0, 16);
    STAMP_AT(1);
    BAR();
    s16x8 f[3];
#pragma unroll
    for (int i = 0; i < 3; ++i) f[i] = cat(ldtr(ra + i * 128), ldtr(ra + i * 128 + 4 * LN_PITCH));
    st128(rs, go, f[0]); st128(rs, g1, f[1]); st128(rs, g2, f[2]);
    BAR();
    publish_last(ws + WS_LNFLAG + b * 128, mix(e0), lane, LCNT, 4);
    STAMP_AT(2);
    BAR();
  }
  const u32 tag = mix(e0);
  const u32 xr = (st * 64 + wave * 16 + c16) * 1280 + dt3 * 160, xo = xr + (g & 1) * 32 + (g >> 1) * 16;
  u32x4 x01, x23; u32x2 x4; u32 bs;
  auto ldx = [&]() {
    x01 = *(const GAS u32x4*)(P.x + xo); x23 = *(const GAS u32x4*)(P.x + xo + 64);
    x4 = *(const GAS u32x2*)(P.x + (xr + 128 + g * 8));
    bs = *(const GAS unsigned short*)(P.b2 + st * 128 + wave * 32 + (u32)(c16 * 2));
  };
  f32x4 oc[2][5];
  const u32 a30 = L0 + (32 * ty + 4 * g + q4) * 128 + (wave ^ ((2 * g + (q4 >> 1)) & 3)) * 32 + p4 * 8;
  const u32 ga0 = L0 + L3_B + ty * 5120 + lane * 16;
  const u32 oa0 = L0 + ty * 4096 + hw * 1024 + lane * 16 + ty * 8;
  u32 sa3, sb3, na3, nb3, ng3;
  struct FA { s16x8 a[2], g[5]; };
  auto lda = [&](FA& f, bool tr) {
    const u32 aa = na3, ab = nb3, ga = ng3;
    BAR();
    if (tr) { f.a[1] = cat(ldtr(ab), ldtr(ab + 2048)); f.a[0] = cat(ldtr(aa), ldtr(aa + 2048)); }
    else { f.a[1] = cat(ld64(ab), ld64(ab + 2048)); f.a[0] = cat(ld64(aa), ld64(aa + 2048)); }
#pragma unroll
    for (int j = 0; j < 5; ++j) f.g[j] = ld128(ga + j * 1024);
    sa3 = sa3 == L3_B - 8192 ? 0 : sa3 + 8192;
    sb3 = sb3 == LA3 * 10240 ? 0 : sb3 + 10240;
    na3 = (tr ? a30 : oa0) + sa3; nb3 = na3 ^ (tr ? 32 : 8); ng3 = ga0 + sb3;
  };
  auto mma = [&](const FA& f) {
#pragma unroll
    for (int j = 0; j < 5; ++j) oc[1][j] = mfma32(f.g[j], f.a[1], oc[1][j]);
#pragma unroll
    for (int j = 0; j < 5; ++j) oc[0][j] = mfma32(f.g[j], f.a[0], oc[0][j]);
  };
  auto pat = [&]() {
    SG(0x100, 1);
#pragma unroll
    for (int j = 0; j < 4; ++j) { SG(0x8, 1); SG(0x100, 2); }
#pragma unroll
    for (int j = 0; j < 5; ++j) { SG(0x8, 1); SG(0x6, 2); }
    SG(0x8, 1);
    asm volatile("" : "+v"(na3), "+v"(nb3), "+v"(ng3));
    SB();
  };
  FA fa0, fa1;
  auto begin3 = [&]() {
    STAMP_AT(8);
#pragma unroll
    for (int j = 0; j < 5; ++j) oc[0][j] = oc[1][j] = f32x4{0.f, 0.f, 0.f, 0.f};
    sa3 = sb3 = 0; na3 = a30; nb3 = a30 ^ 32; ng3 = ga0;
    lda(fa0, true); SB();
  };
  auto rounds = [&](int r0, int r1, bool tr) {
#pragma unroll 1
    for (int r = r0; r < r1; r += 2) { lda(fa1, tr); mma(fa0); pat(); lda(fa0, tr); mma(fa1); pat(); }
  };
  if (roleA) {
    f32x4 aq[5][2] = {}, akv[5][4] = {};
    const u32 wa0 = L0 + (4 * g + q4) * 480 + p4 * 8;
    const u32 xa0 = L0 + LQ_B + wave * 2048 + (p4 >> 1) * 256 + ((4 * g + q4) ^ ((p4 >> 1) << 3)) * 16 + (p4 & 1) * 8;
    const int c32 = lane & 31, hh = lane >> 5;
    const int pb = WS_PART + (hg * 4 + wave) * (16 * 896);
    u32 vp0 = L0 + L_KV + (8 * hw + 4 * ty + g) * LN_PITCH + c16 * 16 + ty * 768, vp1 = vp0 + (1 - 2 * ty) * (4 * LN_PITCH);
    u32 ka = L0 + L_KV + hh * LN_PITCH + (20 * wave + (c32 < 20 ? c32 : 19)) * 16;
    u32 vb = L0 + L_KV + (16 + hh) * LN_PITCH + (c32 < 20 ? 20 * wave + c32 : 80) * 16;
    int so0 = c32 <= 20 ? pb + rt * 896 + (hh * 21 + c32) * 16 : OOB, so1 = lane <= 21 ? pb + rt * 896 + 672 + lane * 8 : OOB;
    u32 vp2 = vp0 - ty * 768 + 512, von = L0 + L_KV + (16 + 4 * wave + g) * LN_PITCH + 1280;
    asm volatile("" : "+v"(vp2), "+v"(von), "+v"(vp0), "+v"(vp1), "+v"(ka), "+v"(vb), "+v"(so0), "+v"(so1));
    u32 qa[5][3], za[5];
#pragma unroll
    for (int je = 0; je < 5; ++je) {
      const int e = 16 * je + c16, he = e / 20, el = e - 20 * he;
#pragma unroll
      for (int x = 0; x < 3; ++x) {
        const int dl = 4 * (je - 1 + x) + g - 5 * he;
        const int qi = dl < 4 ? ((dl & 1) * 21 + el) * 2 + (dl >> 1) : 84 + el;
        qa[je][x] = L0 + L_FIN + (dl >= 0 && dl < 5 ? he * 848 + qi * 8 : 3392);
        if (je - 1 + x >= 0 && je - 1 + x < 5) asm volatile("" : "+v"(qa[je][x]));
      }
      const int dl = 4 * je + g - 5 * p4;
      const int qi = dl < 4 ? ((dl & 1) * 21 + 20) * 2 + (dl >> 1) : 104;
      za[je] = L0 + L_FIN + (dl >= 0 && dl < 5 ? p4 * 848 + qi * 8 : 3392);
      asm volatile("" : "+v"(za[je]));
    }
    u32x4 sel;
    {
      const int r = (c32 & 3) + 4 * (c32 >> 3);
      const u32 one = c32 < 16 && ((c32 >> 2) & 1) == hh ? 0x3f80u << (16 * (r & 1)) : 0u;
#pragma unroll
      for (int i = 0; i < 4; ++i) sel[i] = (r >> 1) == i ? one : 0u;
    }
    int go = lane < 53 ? pb + lane * 16 : OOB;
    u32 fa = L0 + L_FIN + (lane < 53 ? wave * 848 + lane * 16 : 3392);
    int ob = WS_O + (2 * rt + hw) * 81920 + hg * 10240 + ty * 1024 + lane * 16;
    asm volatile("" : "+v"(sel), "+v"(go), "+v"(fa), "+v"(ob));
    s16x8 xA[4] = {}, xB[4], wq[5] = {};
    u32 nwa = wa0, nwk = wa0 + ty * 96, nxa = xa0, nxb = xa0 ^ 2048;
    auto round = [&](int r, s16x8(&xo)[4], s16x8(&xn)[4]) {
      const u32 wa = nwa, wk = nwk, xa = nxa, xb = nxb;
      BAR();
      s16x8 w[6];
      xn[0] = cat(ldtr(xa), ldtr(xa + 1024)); w[0] = cat(ldtr(wk + 160), ldtr(wk + 160 + 7680));
      xn[1] = cat(ldtr(xa + 512), ldtr(xa + 1536)); xn[2] = cat(ldtr(xb), ldtr(xb + 1024)); xn[3] = cat(ldtr(xb + 512), ldtr(xb + 1536));
      w[1] = cat(ldtr(wk + 192), ldtr(wk + 192 + 7680));
#pragma unroll
      for (int j = 0; j < 5; ++j) { aq[j][0] = mfma32(wq[j], xo[0], aq[j][0]); aq[j][1] = mfma32(wq[j], xo[1], aq[j][1]); }
#pragma unroll
      for (int j = 0; j < 2; ++j) { SG(0x8, 1); SG(0x100, 2); }
#pragma unroll
      for (int j = 0; j < 8; ++j) { SG(0x8, 1); SG(0x100, 1); }
      SB();
      w[3] = cat(ldtr(wk + 320), ldtr(wk + 320 + 7680));
      w[4] = cat(ldtr(wk + 352), ldtr(wk + 352 + 7680));
      w[2] = cat(ldtr(wa + 224), ldtr(wa + 224 + 7680));
      w[5] = cat(ldtr(wa + 384), ldtr(wa + 384 + 7680));
#pragma unroll
      for (int j = 0; j < 5; ++j) wq[j] = cat(ldtr(wa + j * 32), ldtr(wa + 7680 + j * 32));
      nwa = wa0 + ((r + 1) & 7) * 15360; nwk = nwa + ty * 96; nxa = xa0 + ((r + 1) & 3) * 8192; nxb = nxa ^ 2048;
#pragma unroll
      for (int j = 0; j < 5; ++j) {
        if (j == 2) continue;
#pragma unroll
        for (int i = 0; i < 4; ++i) akv[j][i] = mfma32(xn[i], w[j], akv[j][i]);
      }
#pragma unroll
      for (int i = 0; i < 4; ++i) akv[2][i] = mfma32(xn[i & 1], w[i < 2 ? 2 : 5], akv[2][i]);
#pragma unroll
      for (int j = 0; j < 3; ++j) { SG(0x8, 1); SG(0x100, 2); }
#pragma unroll
      for (int j = 0; j < 12; ++j) { SG(0x8, 1); SG(0x100, 1); }
#pragma unroll
      for (int j = 0; j < 4; ++j) { SG(0x8, 1); SG(0x6, 2); }
      SG(0x8, 1);
      asm volatile("" : "+v"(nwa), "+v"(nwk), "+v"(nxa), "+v"(nxb));
      SB();
    };
#pragma unroll 1
    for (int r = 0; r < 20; r += 2) { round(r, xA, xB); round(r + 1, xB, xA); }
    f32x4 sk[4], sq[2];
#pragma unroll
    for (int i = 0; i < 4; ++i) sk[i] = *(const LAS f32x4*)(L0 + L_SC + ((wave ^ (i >> 1)) * 128 + (i & 1) * 64 + g * 16));
#pragma unroll
    for (int i = 0; i < 2; ++i) { const float t = *(const LAS float*)(L0 + L_SC + (wave * 128 + i * 64 + c16 * 4)); sq[i] = f32x4{t, t, t, t}; }
    BAR();
    STAMP_AT(3);
    {
      SB();
      *(LAS s16x8*)(vp2 + 16 * LN_PITCH) = cat(pk4(akv[2][2] * sk[0]), pk4(akv[2][3] * sk[1]));
      *(LAS s16x8*)(von) = s16x8{0x3f80, 0x3f80, 0x3f80, 0x3f80, 0x3f80, 0x3f80, 0x3f80, 0x3f80};
#pragma unroll
      for (int j = 3; j < 5; ++j) {
        *(LAS s16x8*)(vp0 + 16 * LN_PITCH + (j - 3) * 256) = cat(pk4(akv[j][0] * sk[0]), pk4(akv[j][1] * sk[1]));
        *(LAS s16x8*)(vp1 + 16 * LN_PITCH + (j - 3) * 256) = cat(pk4(akv[j][2] * sk[2]), pk4(akv[j][3] * sk[3]));
      }
      SB();
#pragma unroll
      for (int j = 0; j < 2; ++j) {
        s16x4 o[4];
        phi<4>(akv[j], sk, o);
        *(LAS s16x8*)(vp0 + j * 256) = cat(o[0], o[1]);
        *(LAS s16x8*)(vp1 + j * 256) = cat(o[2], o[3]);
        SB();
      }
      const f32x4 k2[2] = {akv[2][0], akv[2][1]};
      s16x4 o[2];
      phi<2>(k2, sk, o);
      *(LAS s16x8*)(vp2) = cat(o[0], o[1]);
    }
    STAMP_AT(4);
    BAR();
    {
      s16x8 fk[8], fv[8];
#pragma unroll
      for (int i = 0; i < 8; ++i) { fk[i] = ld128(ka + i * 2 * LN_PITCH); fv[i] = ld128(vb + i * 2 * LN_PITCH); }
      SB();
      f32x16 d = {};
#pragma unroll
      for (int i = 0; i < 8; ++i) d = mfmaw(fk[i], fv[i], d);
      SB();
#pragma unroll
      for (int j = 0; j < 2; ++j) { aq[j][0] = mfma32(wq[j], xA[0], aq[j][0]); aq[j][1] = mfma32(wq[j], xA[1], aq[j][1]); }
      SB();
      const u32x4 lo = {pk2(d[0], d[1]), pk2(d[2], d[3]), pk2(d[4], d[5]), pk2(d[6], d[7])};
      const u32x2 hi = {pk2(d[8], d[9]), pk2(d[10], d[11])};
      st128(rs, so0, __builtin_bit_cast(s16x8, lo));
      st64(rs, so1, __builtin_bit_cast(s16x4, hi));
      SB();
    }
    STAMP_AT(13);
    BAR();
#pragma unroll
    for (int j = 2; j < 5; ++j) { aq[j][0] = mfma32(wq[j], xA[0], aq[j][0]); aq[j][1] = mfma32(wq[j], xA[1], aq[j][1]); }
    SB();
    s16x4 pq[5][2];
#pragma unroll
    for (int j = 0; j < 2; ++j) { phi<2>(aq[j], sq, pq[j]); asm volatile("" : "+v"(pq[j][0]), "+v"(pq[j][1])); }
    publish_last(ws + WS_KVFLAG + hg * 2048 + rt * 128, tag, lane, LCNT, 8);
    STAMP_AT(5);
    ldx();
    SB();
    s16x8 p01[2], p23[2], p34[2];
#pragma unroll
    for (int j = 2; j < 5; ++j) phi<2>(aq[j], sq, pq[j]);
#pragma unroll
    for (int i = 0; i < 2; ++i) {
      p01[i] = cat(pq[0][i], pq[1][i]); p23[i] = cat(pq[2][i], pq[3][i]); p34[i] = cat(pq[3][i], pq[4][i]);
      asm volatile("" : "+v"(p01[i]), "+v"(p23[i]), "+v"(p34[i]));
    }
    begin3();
    rounds(1, 3, true);
    {
      f32x16 d = {};
      u32x4 pr[16];
#pragma unroll
      for (int i = 0; i < 8; ++i) pr[i] = __builtin_amdgcn_raw_buffer_load_b128(rs, go + (i & 3) * 896, (i >> 2) * 3584, 16);
      SB();
      rounds(3, 5, true);
      STAMP_AT(12);
      BAR();
      STAMP_AT(6);
#pragma unroll
      for (int i = 8; i < 16; ++i) pr[i] = __builtin_amdgcn_raw_buffer_load_b128(rs, go + (i & 3) * 896, (i >> 2) * 3584, 16);
      SB();
#pragma unroll
      for (int i = 0; i < 16; ++i) d = mfmaw(__builtin_bit_cast(s16x8, sel), __builtin_bit_cast(s16x8, pr[i]), d);
      *(LAS u32x4*)fa = u32x4{pk2(d[0], d[1]), pk2(d[2], d[3]), pk2(d[4], d[5]), pk2(d[6], d[7])};
    }
    STAMP_AT(7);
    BAR();
    STAMP_AT(15);
    {
      const u32 z0 = L0 + L_FIN + 3392;
      const s16x8 z01 = cat(ld64(za[0]), ld64(za[1])), z23 = cat(ld64(za[2]), ld64(za[3])), z4 = cat(ld64(z0), ld64(za[4]));
      const s16x8 k0 = cat(ld64(qa[0][1]), ld64(qa[0][2])), k1a = cat(ld64(qa[1][0]), ld64(qa[1][1])), k2a = cat(ld64(qa[2][1]), ld64(qa[2][2]));
      const s16x8 k3a = cat(ld64(qa[3][0]), ld64(qa[3][1])), k4 = cat(ld64(qa[4][0]), ld64(qa[4][1]));
      const s16x4 zz = {0, 0, 0, 0};
      const s16x8 k1b = cat(ld64(qa[1][2]), zz), k2b = cat(zz, ld64(qa[2][0])), k3b = cat(zz, ld64(qa[3][2]));
      SB();
      f32x4 den[2], o[5][2];
      const f32x4 e4 = {1e-6f, 1e-6f, 1e-6f, 1e-6f}, n4 = {0.f, 0.f, 0.f, 0.f};
#pragma unroll
      for (int i = 0; i < 2; ++i) den[i] = mfma32(z01, p01[i], e4);
#pragma unroll
      for (int i = 0; i < 2; ++i) den[i] = mfma32(z23, p23[i], den[i]);
#pragma unroll
      for (int i = 0; i < 2; ++i) den[i] = mfma32(z4, p34[i], den[i]);
#pragma unroll
      for (int i = 0; i < 2; ++i) { o[0][i] = mfma32(k0, p01[i], n4); o[1][i] = mfma32(k1a, p01[i], n4); }
      SB();
      float rc[5][2];
#pragma unroll
      for (int i = 0; i < 2; ++i) {
        const float r0 = __builtin_amdgcn_rcpf(den[i][0]), r1 = __builtin_amdgcn_rcpf(den[i][1]), r2 = __builtin_amdgcn_rcpf(den[i][2]), r3 = __builtin_amdgcn_rcpf(den[i][3]);
        rc[0][i] = r0; rc[1][i] = g < 1 ? r0 : r1; rc[2][i] = g < 2 ? r1 : r2; rc[3][i] = g < 3 ? r2 : r3; rc[4][i] = r3;
      }
#pragma unroll
      for (int i = 0; i < 2; ++i) { o[1][i] = mfma32(k1b, p23[i], o[1][i]); o[2][i] = mfma32(k2a, p23[i], n4); o[3][i] = mfma32(k3a, p23[i], n4); }
#pragma unroll
      for (int i = 0; i < 2; ++i) { o[2][i] = mfma32(k2b, p01[i], o[2][i]); o[3][i] = mfma32(k3b, p34[i], o[3][i]); o[4][i] = mfma32(k4, p34[i], n4); }
#pragma unroll
      for (int i = 0; i < 12; ++i) { SG(0x8, 1); SG(0x402, 1); }
      SB();
#pragma unroll
      for (int je = 0; je < 5; ++je) { st128(rs, ob + je * 2048, cat(pk4(o[je][0] * rc[je][0]), pk4(o[je][1] * rc[je][1]))); SB(); }
    }
    STAMP_AT(14);
  } else {
    if (narrow) {
      if (b == 192 && wave == 2) {
        const GAS u32* fp = (const GAS u32*)(ws + WS_LNFLAG + (u32)(lane * 128));
#pragma unroll 1
        for (u32 sp = 0; sp < SPIN_CAP; ++sp) {
          const u32 f0 = __hip_atomic_load(fp, RLX_AGENT), f1 = __hip_atomic_load(fp + 2048, RLX_AGENT);
          const u32 f2 = __hip_atomic_load(fp + 4096, RLX_AGENT), f3 = __hip_atomic_load(fp + 6144, RLX_AGENT);
          if (__all(f0 == tag && f1 == tag && f2 == tag && f3 == tag)) break;
        }
        asm volatile("" ::: "memory");
        if (lane < 2) __hip_atomic_store((GAS u32*)(ws + (lane ? WS_EPOCH : WS_ALLFLAG)), tag, RLX_AGENT);
      }
      f32x4 acc[5];
      {
        const u32x2 bb = *(const GAS u32x2*)(P.b1 + fu * 128 + hw * 64 + ty * 16 + (u32)((g & 1) * 32 + (g >> 1) * 8));
#pragma unroll
        for (int j = 0; j < 5; ++j) acc[j] = f32x4{blo(bb[0]), bhi(bb[0]), blo(bb[1]), bhi(bb[1])};
      }
      BAR();
      const int d8 = 4 * (dt & 1) + wave, ks = xc * 4 + (dt >> 1);
      {
        const u32 ra = L0 + 7 * 8192 + (4 * g + q4) * 640 + wave * 160 + p4 * 8;
        s16x8 f[5];
#pragma unroll
        for (int j = 0; j < 5; ++j) f[j] = cat(ldtr(ra + j * 32), ldtr(ra + j * 32 + 10240));
        const int go = WS_WO + d8 * 102400 + ks * 5120 + lane * 16;
        SB();
#pragma unroll
        for (int j = 0; j < 5; ++j) st128(rs, go + j * 1024, f[j]);
        SB();
      }
      const u32 aa0 = L0 + (8 * g + q4) * 128 + ((2 * hw + (p4 & 1)) ^ ((q4 >> 1) | ((g & 1) << 1))) * 32 + ty * 16 + (p4 >> 1) * 8;
      const u32 ha0 = L0 + L1_B + lane * 16;
      u32 as = 10 * 8192, hs = 0, naa = aa0 + as, nha = ha0;
      struct FN { s16x8 a[2], h[10]; };
      auto step = [&](FN& n, const FN& o) {
        const u32 aa = naa, ha = nha;
        BAR();
        n.a[0] = cat(ldtr(aa), ldtr(aa + 512)); n.a[1] = cat(ldtr(aa + 4096), ldtr(aa + 4608));
#pragma unroll
        for (int j = 0; j < 10; ++j) n.h[j] = ld128(ha + j * 1024);
        as = as == L1_B - 8192 ? 0 : as + 8192;
        hs = hs == (NB1 - 1) * 10240 ? 0 : hs + 10240;
        naa = aa0 + as; nha = ha0 + hs;
#pragma unroll
        for (int j = 0; j < 10; ++j) acc[j % 5] = mfma32(o.a[j / 5], o.h[j], acc[j % 5]);
        SG(0x100, 2);
#pragma unroll
        for (int j = 0; j < 6; ++j) { SG(0x8, 1); SG(0x100, 2); }
#pragma unroll
        for (int j = 0; j < 4; ++j) { SG(0x8, 1); SG(0x6, 2); }
        asm volatile("" : "+v"(naa), "+v"(nha));
        SB();
      };
      FN f0 = {}, f1;
#pragma unroll 1
      for (int r = 0; r < 32; r += 2) { step(f1, f0); step(f0, f1); }
      BAR();
      STAMP_AT(3);
#pragma unroll
      for (int j = 0; j < 10; ++j) acc[j % 5] = mfma32(f0.a[j / 5], f0.h[j], acc[j % 5]);
      const int go = WS_G + dt * 276480 + (fu * 2 + hw) * 5120 + (g & 1) * 1024 + (2 * ty + (g >> 1)) * 256 + c16 * 16;
#pragma unroll
      for (int j = 0; j < 5; j += 2) {
        const u32x4 o = swap16(__builtin_bit_cast(u32x4, gelu8(acc[j], acc[j < 4 ? j + 1 : j])));
        st128(rs, j < 4 || !(g & 1) ? go + j * 1024 : OOB, __builtin_bit_cast(s16x8, o));
      }
      STAMP_AT(4);
    } else {
    f32x4 acc[2][5];
#pragma unroll
    for (int t = 0; t < 2; ++t) {
      const u32x2 bb = *(const GAS u32x2*)(P.b1 + fu * 128 + wave * 64 + (u32)(t * 32 + g * 8));
      const f32x4 bi = {blo(bb[0]), bhi(bb[0]), blo(bb[1]), bhi(bb[1])};
#pragma unroll
      for (int j = 0; j < 5; ++j) acc[t][j] = bi;
    }
    s16x8 a1[2] = {}, h1[5] = {};
    constexpr int RP = 256;
    const u32 aa0 = L0 + (8 * g + q4) * RP + ((2 * wave) ^ (q4 | ((g & 1) << 2))) * 32 + p4 * 8;
    const u32 ha0 = L0 + L1_B + lane * 16;
    u32 as = 6 * 8192, hs = 0, naa = aa0 + as, nab = naa ^ 32, nha = ha0;
#pragma unroll 1
    for (int r = 0; r < 32; ++r) {
      const u32 aa = naa, ab = nab, ha = nha;
      BAR();
      s16x8 a0[2], h0[5];
      a0[0] = cat(ldtr(aa), ldtr(aa + 4 * RP)); h0[0] = ld128(ha); a0[1] = cat(ldtr(ab), ldtr(ab + 4 * RP));
#pragma unroll
      for (int j = 1; j < 5; ++j) h0[j] = ld128(ha + j * 1024);
#pragma unroll
      for (int j = 0; j < 5; ++j) { acc[0][j] = mfma32(a1[0], h1[j], acc[0][j]); acc[1][j] = mfma32(a1[1], h1[j], acc[1][j]); }
#pragma unroll
      for (int j = 0; j < 9; ++j) { SG(0x8, 1); SG(0x100, 1); }
      SG(0x8, 1);
      SB();
      a1[0] = cat(ldtr(aa + 32 * RP), ldtr(aa + 36 * RP)); a1[1] = cat(ldtr(ab + 32 * RP), ldtr(ab + 36 * RP));
#pragma unroll
      for (int j = 0; j < 5; ++j) h1[j] = ld128(ha + 5120 + j * 1024);
      as = as == L1_B - 64 * RP ? 0 : as + 64 * RP;
      hs = hs == (NB1 - 1) * 10240 ? 0 : hs + 10240;
      naa = aa0 + as; nab = naa ^ 32; nha = ha0 + hs;
#pragma unroll
      for (int j = 0; j < 5; ++j) { acc[0][j] = mfma32(a0[0], h0[j], acc[0][j]); acc[1][j] = mfma32(a0[1], h0[j], acc[1][j]); }
      SG(0x100, 1);
#pragma unroll
      for (int j = 0; j < 4; ++j) { SG(0x8, 1); SG(0x100, 2); }
#pragma unroll
      for (int j = 0; j < 5; ++j) { SG(0x8, 1); SG(0x6, 2); }
      SG(0x8, 1);
      asm volatile("" : "+v"(naa), "+v"(nab), "+v"(nha));
      SB();
    }
    BAR();
    STAMP_AT(3);
#pragma unroll
    for (int j = 0; j < 5; ++j) { acc[0][j] = mfma32(a1[0], h1[j], acc[0][j]); acc[1][j] = mfma32(a1[1], h1[j], acc[1][j]); }
      const int go = WS_G + dt * 276480 + (fu * 2 + wave) * 5120 + lane * 16;
#pragma unroll
      for (int j = 0; j < 5; ++j) st128(rs, go + j * 1024, gelu8(acc[0][j], acc[1][j]));
      STAMP_AT(4);
    }
    if (!late) {
      publish_last(ws + WS_GFLAG + dt * 8192 + gi * 128, tag, lane, LCNT, 8);
      if (narrow && lane == 0)
        __hip_atomic_store((GAS u32*)(ws + WS_GFLAG + (4 * (dt & 1) + wave) * 8192 + (16 + xc * 4 + (dt >> 1)) * 128), tag, RLX_AGENT);
      STAMP_AT(5);
      BAR();
    }
    ldx();
    begin3();
  }

  {
    rounds(roleA ? 5 : 1, roleA ? R_JOIN : PUBR, true);
    {
      asm volatile("" : "+v"(x01), "+v"(x23), "+v"(x4), "+v"(bs));
      const float bf = blo(bs);
      x01 = swap16(x01); x23 = swap16(x23);
      oc[0][0] += f32x4{blo(x01[0]), bhi(x01[0]), blo(x01[1]), bhi(x01[1])} + bf;
      oc[0][1] += f32x4{blo(x01[2]), bhi(x01[2]), blo(x01[3]), bhi(x01[3])} + bf;
      oc[0][2] += f32x4{blo(x23[0]), bhi(x23[0]), blo(x23[1]), bhi(x23[1])} + bf;
      oc[0][3] += f32x4{blo(x23[2]), bhi(x23[2]), blo(x23[3]), bhi(x23[3])} + bf;
      oc[0][4] += f32x4{blo(x4[0]), bhi(x4[0]), blo(x4[1]), bhi(x4[1])} + bf;
      if (late) publish_last(ws + WS_GFLAG + dt * 8192 + gi * 128, tag, lane, LCNT, 8);
      else if (roleA) publish(ws + WS_OFLAG + (2 * rt + hw) * 2048 + (ty * 8 + hg) * 128, tag, lane);
      SB();
    }
    rounds(roleA ? R_JOIN : PUBR, 27, true);
    STAMP_AT(9);
    na3 = oa0 + sa3; nb3 = na3 ^ 8;
    static_assert(36 % NB3 < 2, "");
    u32 ew = L0 + L3_B + 20480 + wave * 5120 + lane * 16, er = L0 + L3_B + 20480 + (wave ^ 1) * 5120 + lane * 16, xo4 = xr + 128 + g * 8;
    asm volatile("" : "+v"(ew), "+v"(er), "+v"(xo4));
    rounds(27, 37, false);
    mma(fa0);
#pragma unroll
    for (int j = 0; j < 5; ++j) *(LAS f32x4*)(ew + j * 1024) = oc[1][j];
    SG(0x8, 6);
#pragma unroll
    for (int j = 0; j < 4; ++j) { SG(0x200, 1); SG(0x8, 1); }
    SG(0x200, 1);
    SB();
    BAR();
#pragma unroll
    for (int j = 0; j < 5; ++j) oc[1][j] = *(const LAS f32x4*)(er + j * 1024);
    SB();
#pragma unroll
    for (int j = 0; j < 5; ++j) oc[0][j] += oc[1][j];
    *(GAS u32x4*)(P.out + xo) = swap16(__builtin_bit_cast(u32x4, cat(pk4(oc[0][0]), pk4(oc[0][1]))));
    *(GAS u32x4*)(P.out + xo + 64) = swap16(__builtin_bit_cast(u32x4, cat(pk4(oc[0][2]), pk4(oc[0][3]))));
    *(GAS s16x4*)(P.out + xo4) = pk4(oc[0][4]);
    asm volatile(".Lcode_exit_b:" ::: "memory");
  }
}

extern "C" void kernel_launch(void* const* d_in, const int* in_sizes, int n_in, void* d_out, int out_size, void* d_ws,
                              size_t ws_size, hipStream_t stream) {
  Params p;
  p.x = (const char*)d_in[0];
  p.wqkv = (const char*)d_in[1];
  p.wo = (const char*)d_in[2];
  p.w1 = (const char*)d_in[4];
  p.b1 = (const char*)d_in[5];
  p.w2 = (const char*)d_in[6];
  p.b2 = (const char*)d_in[7];
  p.out = (char*)d_out;
  p.ws = (char*)d_ws;
  (void)hipFuncSetAttribute((const void*)fused_fwd, hipFuncAttributeMaxDynamicSharedMemorySize, L_BYTES);
  fused_fwd<<<256, 320, L_BYTES, stream>>>(p);
}
```

```cpp
#include <hip/hip_runtime.h>
#include <hip/hip_bf16.h>
#pragma clang diagnostic ignored "-Wint-to-pointer-cast"

#define GAS __attribute__((address_space(1)))
#define LAS __attribute__((address_space(3)))
#define DI __device__ __forceinline__
typedef unsigned u32;
typedef unsigned long long u64;
typedef short s16x4 __attribute__((ext_vector_type(4)));
typedef short s16x8 __attribute__((ext_vector_type(8)));
typedef __bf16 b16x2 __attribute__((ext_vector_type(2)));
typedef __bf16 b16x8 __attribute__((ext_vector_type(8)));
typedef float f32x2 __attribute__((ext_vector_type(2)));
typedef float f32x4 __attribute__((ext_vector_type(4)));
typedef float f32x16 __attribute__((ext_vector_type(16)));
typedef unsigned u32x2 __attribute__((ext_vector_type(2)));
typedef unsigned u32x4 __attribute__((ext_vector_type(4)));

constexpr int WS_EPOCH = 0;
constexpr int WS_ALLFLAG = 1024;
constexpr int WS_LNFLAG = 196608;
constexpr int WS_GFLAG = 720896;
constexpr int WS_KVFLAG = 65536;
constexpr int WS_OFLAG = 131072;
constexpr int WS_PART = 262144;
constexpr int WS_SC = 8192;
constexpr int WS_H = 786432;
constexpr int WS_G = WS_H + 2621440;
constexpr int WS_O = WS_G + 2211840;
constexpr int WS_WO = WS_O + 2621440;
constexpr int WS_END = WS_WO + 819200;
constexpr int OOB = 0x40000000;
constexpr int L_BYTES = 163840;
constexpr int L_LN = 131072, LN_PITCH = 1296;
constexpr int NB3 = 5, NB1 = 4, NA1 = 7;
constexpr int LA3 = NB3 - 1, NA3 = 18 - NB3, H3 = 2 * (NA3 - LA3 - 2);
constexpr int LQ_B = 122880, L1_B = NA1 * 16384, L3_B = NA3 * 8192;
constexpr int L_KV = L3_B, L_FIN = L3_B + NB3 * 10240, L_SC = L_FIN + 3584;
constexpr int PRE_A = 2;
constexpr int APRE = 20, NPK_A = 8, R_JOIN = 9, LPK = 22, EARLY = 16, PUBR = 3, TH_PA = 10, TH_P3 = 8, SLP = 2, MID_A = 2, PRE_B = 3, MID_B = 3;
constexpr u32 SPIN_CAP = 1u << 16;

#define BAR() do { asm volatile("" ::: "memory"); __builtin_amdgcn_s_waitcnt(0xC07F); asm volatile("s_barrier" ::: "memory"); } while (0)
#define WAITV(n) asm volatile("s_waitcnt vmcnt(" #n ")" ::: "memory")
#define WAITX(n) asm volatile("s_waitcnt vmcnt(%0)" ::"i"((n) > 63 ? 63 : (n)) : "memory")
#define LBAR() asm volatile("s_barrier" ::: "memory")
#define RLX_AGENT __ATOMIC_RELAXED, __HIP_MEMORY_SCOPE_AGENT

DI f32x4 mfma32(s16x8 a, s16x8 b, f32x4 c) {
  return __builtin_amdgcn_mfma_f32_16x16x32_bf16(__builtin_bit_cast(b16x8, a), __builtin_bit_cast(b16x8, b), c, 0, 0, 0);
}
DI f32x16 mfmaw(s16x8 a, s16x8 b, f32x16 c) {
  return __builtin_amdgcn_mfma_f32_32x32x16_bf16(__builtin_bit_cast(b16x8, a), __builtin_bit_cast(b16x8, b), c, 0, 0, 0);
}
DI s16x4 ldtr(u32 a) { return __builtin_amdgcn_ds_read_tr16_b64_v4i16((LAS s16x4*)a); }
DI s16x8 ld128(u32 a) { return *(const LAS s16x8*)a; }
DI s16x4 ld64(u32 a) { return *(const LAS s16x4*)a; }
DI s16x8 cat(s16x4 a, s16x4 b) { return __builtin_shufflevector(a, b, 0, 1, 2, 3, 4, 5, 6, 7); }
DI u32 pk2(float a, float b) { f32x2 v = {a, b}; return __builtin_bit_cast(u32, __builtin_convertvector(v, b16x2)); }
DI s16x4 pk4(f32x4 v) { u32x2 r = {pk2(v[0], v[1]), pk2(v[2], v[3])}; return __builtin_bit_cast(s16x4, r); }
DI float blo(u32 u) { return __uint_as_float(u << 16); }
DI float bhi(u32 u) { return __uint_as_float(u & 0xffff0000u); }
template <int C> DI float dpp_add(float s) { return s + __int_as_float(__builtin_amdgcn_update_dpp(0, __float_as_int(s), C, 0xf, 0xf, true)); }
DI float sum32(float s) {
  s = dpp_add<0x121>(s); s = dpp_add<0x122>(s); s = dpp_add<0x124>(s); s = dpp_add<0x128>(s);
  auto r = __builtin_amdgcn_permlane16_swap(__float_as_uint(s), __float_as_uint(s), false, false);
  return __uint_as_float(r[0]) + __uint_as_float(r[1]);
}
#define SG(m, n) __builtin_amdgcn_sched_group_barrier(m, n, 0)
#define SB() __builtin_amdgcn_sched_barrier(0)
#define AL() do { asm volatile(".p2align 3"); SB(); } while (0)
template <int N> DI void phi(const f32x4 (&u)[N], const f32x4* sc, s16x4 (&o)[N]) {
  const float L = 1.4426950408889634f;
  f32x4 a[N], y[N];
  SB(); AL();
#pragma unroll
  for (int i = 0; i < N; ++i) a[i] = __builtin_elementwise_fma(u[i], sc[i], f32x4{1.f, 1.f, 1.f, 1.f});
  SB();
#pragma unroll
  for (int i = 0; i < N; ++i) y[i] = __builtin_elementwise_fma(a[i], f32x4{L, L, L, L}, f32x4{-L, -L, -L, -L});
  SB();
#pragma unroll
  for (int i = 0; i < 4 * N; ++i) y[i >> 2][i & 3] = __builtin_amdgcn_fmed3f(__builtin_amdgcn_exp2f(y[i >> 2][i & 3]), 0.f, 1.f);
  SB();
#pragma unroll
  for (int i = 0; i < 4 * N; ++i) y[i >> 2][i & 3] = __int_as_float(max(__float_as_int(a[i >> 2][i & 3]), __float_as_int(y[i >> 2][i & 3])));
  SB();
#pragma unroll
  for (int i = 0; i < N; ++i) y[i] += 1e-6f;
  SB();
#pragma unroll
  for (int i = 0; i < N; ++i) o[i] = pk4(y[i]);
}
DI s16x8 gelu8(f32x4 u0, f32x4 u1) {
  const float K = -2.f * 0.7978845608028654f * 1.4426950408889634f, K3 = K * 0.044715f;
  SB(); AL();
  f32x4 y0 = u0 * u0, y1 = u1 * u1;
  SB();
  y0 = __builtin_elementwise_fma(y0, f32x4{K3, K3, K3, K3}, f32x4{K, K, K, K});
  y1 = __builtin_elementwise_fma(y1, f32x4{K3, K3, K3, K3}, f32x4{K, K, K, K});
  SB();
  y0 *= u0; y1 *= u1;
  SB();
#pragma unroll
  for (int i = 0; i < 4; ++i) y0[i] = __builtin_amdgcn_exp2f(y0[i]);
#pragma unroll
  for (int i = 0; i < 4; ++i) y1[i] = __builtin_amdgcn_exp2f(y1[i]);
  SB();
  y0 += 1.f; y1 += 1.f;
  SB();
#pragma unroll
  for (int i = 0; i < 4; ++i) y0[i] = __builtin_amdgcn_rcpf(y0[i]);
#pragma unroll
  for (int i = 0; i < 4; ++i) y1[i] = __builtin_amdgcn_rcpf(y1[i]);
  SB();
  y0 *= u0; y1 *= u1;
  SB();
  return cat(pk4(y0), pk4(y1));
}
DI void st128(__amdgpu_buffer_rsrc_t rs, int off, s16x8 v) { __builtin_amdgcn_raw_buffer_store_b128(__builtin_bit_cast(u32x4, v), rs, off, 0, 16); }
DI void st64(__amdgpu_buffer_rsrc_t rs, int off, s16x4 v) { __builtin_amdgcn_raw_buffer_store_b64(__builtin_bit_cast(u32x2, v), rs, off, 0, 16); }
DI u64 mix(u64 e) {
  return (u64)((u32)(e >> 32) * 0x85EBCA6Bu + 0x165667B1u) << 32 | (u32)((u32)e * 0x9E3779B1u + 0x7F4A7C15u);
}

#define GL(v, off, sc) "global_load_lds_dwordx4 " v ", %0 offset:" #off sc "\n\t"
#define M0(bias) "s_add_u32 m0, %1, " #bias "\n\ts_nop 0\n\t"
#define RING m = m == HI - STEP ? LO : m + STEP
template <int STEP, int LO, int HI> DI void gl4(const char* base, u32& m, const u32 (&v)[4]) {
  asm volatile(M0(0) GL("%2", 0, "") GL("%3", 1024, "") GL("%4", 2048, "") GL("%5", 3072, "")
               ::"s"(base), "s"(m), "v"(v[0]), "v"(v[1]), "v"(v[2]), "v"(v[3]) : "memory", "scc");
  RING;
}
template <int STEP, int LO, int HI> DI void gl8(const char* base, u32& m, const u32 (&v)[8]) {
  asm volatile(M0(4096) GL("%2", -4096, "") GL("%3", -3072, "") GL("%4", -2048, "") GL("%5", -1024, "")
               GL("%6", 0, "") GL("%7", 1024, "") GL("%8", 2048, "") GL("%9", 3072, "")
               ::"s"(base), "s"(m), "v"(v[0]), "v"(v[1]), "v"(v[2]), "v"(v[3]), "v"(v[4]), "v"(v[5]), "v"(v[6]), "v"(v[7]) : "memory", "scc");
  RING;
}
template <int STEP, int LO, int HI> DI void gl8w(const char* base, u32& m, const u32 (&v)[8]) {
  asm volatile("s_add_u32 m0, %1, 3840\n\ts_mov_b32 exec_hi, 0x0fffffff\n\t" GL("%2", -3840, "") GL("%3", -2880, "") GL("%4", -1920, "") GL("%5", -960, "")
               GL("%6", 0, "") GL("%7", 960, "") GL("%8", 1920, "") GL("%9", 2880, "") "s_mov_b32 exec_hi, -1"
               ::"s"(base), "s"(m), "v"(v[0]), "v"(v[1]), "v"(v[2]), "v"(v[3]), "v"(v[4]), "v"(v[5]), "v"(v[6]), "v"(v[7]) : "memory", "scc");
  RING;
}
DI void gl5(const char* base, u32 m, const u32 (&v)[5]) {
  asm volatile(M0(4096) GL("%2", -4096, "") GL("%3", -3072, "") GL("%4", -2048, "") GL("%5", -1024, "") GL("%6", 0, "")
               ::"s"(base), "s"(m), "v"(v[0]), "v"(v[1]), "v"(v[2]), "v"(v[3]), "v"(v[4]) : "memory", "scc");
}
#define GL8S M0(4096) GL("%2", -4096, " sc1") GL("%2", -3072, " sc1") GL("%2", -2048, " sc1") GL("%2", -1024, " sc1") \
             GL("%2", 0, " sc1") GL("%2", 1024, " sc1") GL("%2", 2048, " sc1") GL("%2", 3072, " sc1")
template <int STEP, int LO, int HI> DI void glin8(const char* base, u32& m, u32 vl4) {
  asm volatile(GL8S ::"s"(base), "s"(m), "v"(vl4) : "memory", "scc");
  RING;
}
template <int STEP, int LO, int HI> DI void glin10(const char* base, u32& m, u32 vl4, u32 vl8) {
  asm volatile(GL8S M0(8192) GL("%3", 0, " sc1") GL("%3", 1024, " sc1")
               ::"s"(base), "s"(m), "v"(vl4), "v"(vl8) : "memory", "scc");
  RING;
}
template <int STEP, int LO, int HI> DI void glin55(const char* base, u32& m, u32 vl4, u32 vl4b) {
  asm volatile(M0(4096) GL("%2", -4096, " sc1") GL("%2", -3072, " sc1") GL("%2", -2048, " sc1") GL("%2", -1024, " sc1") GL("%2", 0, " sc1")
               M0(9216) GL("%3", -4096, " sc1") GL("%3", -3072, " sc1") GL("%3", -2048, " sc1") GL("%3", -1024, " sc1") GL("%3", 0, " sc1")
               ::"s"(base), "s"(m), "v"(vl4), "v"(vl4b) : "memory", "scc");
  RING;
}
template <int STEP, int LO, int HI> DI void glin2222(const char* base, u32& m, const u32 (&v)[4]) {
  asm volatile(M0(4096) GL("%2", -4096, " sc1") GL("%2", -3072, " sc1") GL("%3", -2048, " sc1") GL("%3", -1024, " sc1")
               GL("%4", 0, " sc1") GL("%4", 1024, " sc1") GL("%5", 2048, " sc1") GL("%5", 3072, " sc1")
               ::"s"(base), "s"(m), "v"(v[0]), "v"(v[1]), "v"(v[2]), "v"(v[3]) : "memory", "scc");
  RING;
}
DI void glin1(const char* base, u32 m, u32 v) { asm volatile(M0(0) GL("%2", 0, " sc1")::"s"(base), "s"(m), "v"(v) : "memory", "scc"); }
DI u32x4 swap16(u32x4 t) {
  const auto s0 = __builtin_amdgcn_permlane16_swap(t[0], t[2], false, false), s1 = __builtin_amdgcn_permlane16_swap(t[1], t[3], false, false);
  return u32x4{s0[0], s1[0], s0[1], s1[1]};
}
DI void touch(const char* base, u32 m, u32 v) {
  asm volatile("s_mov_b32 m0, %1\n\ts_nop 0\n\tglobal_load_lds_dword %2, %0" ::"s"(base), "s"(m), "v"(v) : "memory");
}
DI void poll(const char* f, int idx, u64 tag) {
  const GAS u64* p = (const GAS u64*)(f + (u32)(idx * 128));
#pragma unroll 1
  for (u32 sp = 0; sp < SPIN_CAP; ++sp) {
    const u64 v = __hip_atomic_load(p, RLX_AGENT);
    if (__all(v == tag)) break;
    __builtin_amdgcn_s_sleep(SLP);
  }
  asm volatile("" ::: "memory");
}
template <int R = 254> DI void peek(const char* f, int idx, u64 tag) {
  asm volatile("v_not_b32 v%c2, %4\n\tglobal_load_dwordx2 v[%c2:%c3], %0, %1 sc1" ::"v"(idx * 128), "s"(f), "i"(R), "i"(R + 1), "s"((u32)tag)
               : "memory", "v252", "v253", "v254", "v255");
}
template <int R = 254> DI bool seen(u64 tag) {
  u64 miss;
  asm volatile("v_cmp_ne_u64 %0, %1, v[%c2:%c3]" : "=s"(miss) : "s"(tag), "i"(R), "i"(R + 1) : "memory");
  return miss == 0;
}
template <int R = 254> DI void await(const char* f, int idx, u64 tag) { if (!seen<R>(tag)) poll(f, idx, tag); }
DI void publish(char* f, u64 tag, int lane) {
  WAITV(0);
  if (lane == 0) __hip_atomic_store((GAS u64*)f, tag, RLX_AGENT);
}
DI void publish_last(char* f, u64 tag, int lane, u32 cnt, u32 n) {
  WAITV(0);
  if (lane == 0 && __hip_atomic_fetch_add((LAS u32*)cnt, 1u, __ATOMIC_RELAXED, __HIP_MEMORY_SCOPE_WORKGROUP) == n - 1)
    __hip_atomic_store((GAS u64*)f, tag, RLX_AGENT);
}

struct Params {
  const char *x, *wqkv, *wo, *w1, *b1, *w2, *b2;
  char *out, *ws;
};
#define ROLES() \
  int b_ = b; asm volatile("" : "+s"(b_)); \
  const int xc = b_ & 7, jb = b_ >> 3, xm = 7 - xc; \
  const bool roleA = b_ < 128; \
  const int hg = 2 * (xc & 3) + (jb & 1), rt = 8 * (xc >> 2) + (jb >> 1); \
  const int dt = jb & 7, grp = (jb >> 3) & 1; \
  const bool late = !roleA && xm >= 5, narrow = !roleA && xm < 5 && grp; \
  const int fu = late ? 4 * xm - 5 + 2 * grp : grp ? xm : 5 + 2 * xm, gi = late ? 2 * xm + grp : grp ? xm : 5 + xm; \
  const int st = 4 * xc + (jb >> 3), dt3 = jb & 7; \
  const u32 d0 = dt ^ (late ? grp : 0), istep = narrow ? 2 : 1, rot = (5 * (2 * (rt & 7) + (rt >> 3))) >> 2; \
  (void)hg; (void)fu; (void)gi; (void)st; (void)dt3; (void)d0; (void)istep; (void)rot
#define EPOCH() *(const __attribute__((address_space(4))) u64*)(ws + WS_EPOCH)
__global__ __launch_bounds__(320, 2) void fused_fwd(Params P) {
  const int tid = threadIdx.x, lane = tid & 63;
  const int wave = __builtin_amdgcn_readfirstlane(tid >> 6);
  const int b = blockIdx.x;
  char* const ws = P.ws;
  constexpr u32 L0 = 0;
  const u32 LDUM = L0 + L_BYTES - 256, LCNT = L0 + L_BYTES - 512;
  const int c16 = lane & 15, g = lane >> 4, q4 = c16 >> 2, p4 = lane & 3;
  {
    u64 pc; int left;
    asm volatile("s_getpc_b64 %0\n.Lcode_here:\n\ts_mov_b32 %1, .Lcode_exit_a-.Lcode_here\n\ts_max_i32 %1, %1, .Lcode_exit_b-.Lcode_here"
                 : "=s"(pc), "=&s"(left)::"scc");
    touch((const char*)pc, LDUM, min((((b >> 3) * 5 + wave) * 2 + (lane & 1)) * 128, left));
  }
  if (wave == 4) {
    __builtin_amdgcn_s_setprio(3);
    u32 v[8], sa;
    if (b >= 128) {
      ROLES();
      const u32 col = narrow ? ((lane & 7) ^ ((g & 1) << 1)) * 16 : (c16 ^ (g << 1)) * 16, stp = (narrow ? 8 : 4) * 3456 - 1024;
#pragma unroll
      for (int i = 0; i < 8; ++i)
        v[i] = (narrow ? lane >> 3 : g) * 3456 + (col ^ (narrow ? (i & 1) * 64 : ((i >> 1) & 1) * 128)) + i * stp + 4096;
      sa = narrow ? 10 * 8192 : 6 * 8192;
#pragma unroll 1
      for (int i = 0; i < PRE_B; ++i) gl8<8192, 0, L1_B>(P.w1 + fu * 128 + (8 * d0 + i * istep) * 110592, sa, v);
    } else {
      ROLES();
      const u32 hi = lane >= 30, cc = lane - 30 * hi, sg = (cc >= 10) + (cc >= 20);
      v[0] = (hi + 1) * 3840 + sg * 1120 + cc * 16;
#pragma unroll
      for (int i = 1; i < 8; ++i) v[i] = v[0] + i * 6720;
      sa = 0;
      const char* wp = P.wqkv + (hg * 160 + rot * 122880);
      gl8w<7680, 0, LQ_B>(wp, sa, v); gl8w<7680, 0, LQ_B>(wp + 61440, sa, v);
    }
    u64 e0 = EPOCH();
    ROLES();
    asm volatile("" : "+s"(e0));
    BAR(); LBAR();
    const u64 tag = mix(e0);
    const u32 vl4 = lane * 16 + 4096, vl8 = vl4 + 4096;
    const u32 u0 = (lane >> 3) * 4096 + ((lane & 7) ^ (g << 1)) * 16;
    const u32 u[4] = {u0, u0 + 31744, u0 + 2 * 31744, u0 + 3 * 31744};
    const char* w2p = P.w2 + st * 128;
    u32 sa3 = 0;
    auto ahalf = [&]() { gl4<4096, 0, L3_B>(w2p, sa3, u); w2p += 131072; };
    const char* const gfl = ws + WS_GFLAG + dt3 * 8192;
    const char* bp = ws + WS_G + dt3 * 276480;
    u32 sb3 = L3_B;
    auto bunit3 = [&]() { glin10<10240, L3_B, L3_B + NB3 * 10240>(bp, sb3, vl4, vl8); bp += 10240; };
    auto start3 = [&](int done) {
#pragma unroll 1
      for (int i = 0; i < 4; ++i) bunit3();
#pragma unroll 1
      for (int i = done; i < 2 * NA3; i += 2) { ahalf(); ahalf(); }
    };
    if (!roleA) {
      const char* wb = P.w1 + fu * 128;
      const char* hb = ws + WS_H + dt * 5120;
      u32 ia = PRE_B * istep, ib2 = 0, sb = L1_B;
      static_assert(PRE_B <= 4, "");
      auto a8 = [&]() { gl8<8192, 0, L1_B>(wb + (ia ^ (8 * d0)) * 110592, sa, v); ia += istep; };
      auto bunit = [&]() { glin55<10240, L1_B, L1_B + NB1 * 10240>(hb + (ib2 ^ (4 * d0)) * 81920, sb, vl4, vl4 + 40960); ++ib2; };
#pragma unroll 1
      for (int i = 0; i < MID_B; ++i) a8();
      LBAR();
#pragma unroll 1
      for (int i = PRE_B + MID_B; i < 10; ++i) { WAITV(32); a8(); }
      peek(ws + WS_LNFLAG, (d0 & 6) * 32 + lane, tag);
      a8(); a8();
      WAITV(16);
      await(ws + WS_LNFLAG, (d0 & 6) * 32 + lane, tag);
      bunit(); bunit(); bunit();
      a8(); a8();
      WAITX(36); LBAR(); bunit();
      WAITX(36);
      if (narrow) {
#pragma unroll 1
        for (int r = 1; r < 3; ++r) { LBAR(); bunit(); WAITX(20); }
        peek(ws + WS_ALLFLAG, 0, tag);
#pragma unroll 1
        for (int r = 3; r < 5; ++r) { LBAR(); bunit(); WAITX(20); }
      } else {
#pragma unroll 1
        for (int r = 1; r < 3; ++r) { LBAR(); a8(); a8(); bunit(); WAITX(52); }
        peek(ws + WS_ALLFLAG, 0, tag);
#pragma unroll 1
        for (int r = 3; r < 5; ++r) { LBAR(); a8(); a8(); bunit(); WAITX(52); }
      }
      await(ws + WS_ALLFLAG, 0, tag);
      if (narrow) {
#pragma unroll 1
        for (int r = 5; r < 29; ++r) { LBAR(); if (r & 3) a8(); bunit(); WAITX(28); }
      } else {
#pragma unroll 1
        for (int r = 5; r < 29; ++r) { LBAR(); a8(); if (r & 3) a8(); bunit(); WAITX(44); }
      }
      LBAR();
      if (narrow) {
        u32 w5[5];
#pragma unroll
        for (int i = 0; i < 5; ++i) { const u32 n = 64 * i + lane; w5[i] = n / 40u * 640 + lane * 16 + 4096; }
        const char* wp = P.wo + (xm * 4 + (dt >> 1)) * (32 * 1280) + (dt & 1) * 640;
#pragma unroll 1
        for (int i = 0; i < 4; ++i) gl5(wp + i * (8 * 1280), i * 5120, w5);
        WAITX(30); LBAR(); WAITX(20); LBAR();
      } else {
        constexpr int E1 = 3 * EARLY / 8;
#pragma unroll 1
        for (int i = 0; i < E1; ++i) ahalf();
        WAITX(18 + 4 * E1); LBAR();
#pragma unroll 1
        for (int i = 0; i < E1; ++i) ahalf();
        WAITX(8 * E1); LBAR();
        if (!late) {
#pragma unroll 1
          for (int i = 2 * E1; i < EARLY; ++i) ahalf();
        }
      }
      if (!late) {
        LBAR();
        if (narrow) { WAITV(0); LBAR(); LBAR(); }
#pragma unroll 1
        for (int i = narrow ? -16 : 0; i < 10; ++i) { ahalf(); WAITV(32); if (!narrow) WAITX(TH_P3); }
        peek(gfl, lane < 5 ? lane : 0, tag);
        WAITV(0);
        if (!narrow) LBAR();
      } else {
        ahalf(); ahalf(); ahalf(); ahalf();
        LBAR();
#pragma unroll 1
        for (int i = 3 * EARLY / 8 * 2 + 4; i < LPK; ++i) ahalf();
        peek(gfl, lane < 5 ? lane : 0, tag);
        ahalf(); ahalf(); ahalf(); ahalf();
        WAITV(16);
      }
      await(gfl, lane < 5 ? lane : 0, tag);
      start3(late ? LPK + 4 : narrow ? 26 : EARLY + 10);
    } else {
      const char* wb = P.wqkv + hg * 160;
      const char* xb = ws + WS_H + rt * 163840;
      const u32 x0 = (lane ^ ((g & 1) << 3)) * 16 + 4096, xv[4] = {x0, x0 + 38912, x0 + 2 * 38912, x0 + 3 * 38912};
      u32 oa = (rot + 1) * 122880  , ob = rot * 2048, sb = LQ_B;
      auto a8 = [&]() { gl8w<7680, 0, LQ_B>(wb + oa, sa, v); oa = oa == 39 * 61440 ? 0 : oa + 61440; };
      auto bunit = [&]() { glin2222<8192, LQ_B, LQ_B + 4 * 8192>(xb + ob, sb, xv); ob = ob == 19 * 2048 ? 0 : ob + 2048; };
#pragma unroll 1
      for (int i = 0; i < MID_A; ++i) a8();
      LBAR();
#pragma unroll 1
      for (int i = PRE_A + MID_A; i < NPK_A; ++i) { WAITV(24); a8(); }
      peek(ws + WS_LNFLAG, rt * 16 + c16, tag);
      a8(); a8();
      WAITV(16);
      await(ws + WS_LNFLAG, rt * 16 + c16, tag);
      bunit(); bunit(); bunit();
      glin1(ws + WS_SC + rt * 512, L_SC, (lane & 31) * 16);
#pragma unroll 1
      for (int i = NPK_A + 2; i < 12; ++i) a8();
      WAITX(16 + 8 * (10 - NPK_A));
#pragma unroll 1
      for (int r = 1; r < 17; ++r) { LBAR(); a8(); if (r & 3) a8(); bunit(); WAITX(40); }
      LBAR(); bunit();
      const char* tb = P.w2 + xc * 512 + jb * (108 * 4096);
      const u32 tv = (lane >> 2) * 4096 + p4 * 128;
#pragma unroll 1
      for (int i = 0; i < 7; ++i) {
        if (i == 2) { WAITX(26); LBAR(); }
        touch(tb + (i == 6 ? 92 : 16 * i) * 4096, LDUM, tv);
      }
      WAITX(15); LBAR(); WAITX(7); LBAR();
      BAR();
      const char* const kfl = ws + WS_KVFLAG + hg * 2048;
      int done = 0;
#pragma unroll 1
      for (; done < APRE; ++done) { ahalf(); WAITV(24); }
      LBAR();
      peek(gfl, lane < 5 ? lane : 0, tag);
      WAITV(0);
      LBAR();
      if (!seen(tag)) {
#pragma unroll 1
        for (; done < 2 * NA3; ++done) ahalf();
        poll(gfl, lane < 5 ? lane : 0, tag);
      }
      bunit3(); bunit3(); bunit3();
      WAITX(TH_PA);
      peek(kfl, 8 + (lane & 7), tag);
      bunit3(); bunit3();
      WAITV(41); LBAR(); WAITV(31); LBAR(); WAITV(20);
      await(kfl, 8 + (lane & 7), tag);
      peek(kfl, lane & 7, tag);
      LBAR(); WAITV(11); LBAR(); WAITV(0); LBAR();
      await(kfl, lane & 7, tag);
      LBAR();
      peek(gfl, lane < 5 ? 5 + lane : 5, tag);
      WAITV(0);
      const bool ready = seen(tag);
      if (!ready) { LBAR(); poll(gfl, lane < 5 ? 5 + lane : 5, tag); }
      bunit3(); bunit3();
      if (ready) LBAR();
      bunit3(); bunit3();
#pragma unroll 1
      for (; done < 2 * NA3 + 4; ++done) ahalf();
      WAITV(16);
    }
    {
      auto bunit = bunit3;
      if (!roleA) {
        WAITX(30); LBAR();
        peek(gfl, lane < 5 ? 5 + lane : 5, tag);
        bunit();
        WAITX(30); LBAR(); ahalf();
        WAITX(25); LBAR();
        WAITV(14);
        await(gfl, lane < 5 ? 5 + lane : 5, tag);
        bunit(); bunit(); ahalf();
      }
#pragma unroll 1
      for (int r = roleA ? 5 : 3; r < 7; ++r) { LBAR(); bunit(); ahalf(); WAITX(42); }
      peek(gfl, lane < 6 ? 10 + lane : 10, tag);
#pragma unroll 1
      for (int r = 7; r < 11; ++r) { LBAR(); bunit(); ahalf(); WAITX(46); }
      await(gfl, lane < 6 ? 10 + lane : 10, tag);
#pragma unroll 1
      for (int r = 11; r < 18; ++r) { LBAR(); bunit(); ahalf(); if (r > H3) ahalf(); WAITX(46); }
#pragma unroll 1
      for (int r = 18; r < 20; ++r) { LBAR(); bunit(); ahalf(); ahalf(); WAITX(62); }
      peek<252>(ws + WS_OFLAG + st * 2048, c16, tag);
      peek(gfl, lane < 20 ? 16 + lane : 16, tag);
#pragma unroll 1
      for (int r = 20; r < 22; ++r) { LBAR(); bunit(); ahalf(); ahalf(); WAITX(62); }
      LBAR(); bunit(); WAITX(54);
      LBAR(); WAITX(36);
      await<252>(ws + WS_OFLAG + st * 2048, c16, tag); await(gfl, lane < 20 ? 16 + lane : 16, tag);
      const char* ap = ws + WS_O + st * 81920 + dt3 * 8192;
      bp = ws + WS_WO + dt3 * 112640;
#pragma unroll 1
      for (int r = 24; r < 34; ++r) {
        LBAR();
        glin8<8192, 0, L3_B>(ap, sa3, vl4);
        bunit();
        ap += 8192;
        if (r + dt3 == 33) { ap -= 81920; bp -= 102400; }
        WAITX(36);
      }
      LBAR(); WAITX(18); LBAR(); WAITX(0); LBAR();
    }
    asm volatile(".Lcode_exit_a:" ::: "memory");
    return;
  }

  const int hw = wave >> 1, ty = wave & 1;
  const __amdgpu_buffer_rsrc_t rs = __builtin_amdgcn_make_buffer_rsrc(ws, 0, WS_END, 0x00020000);
  u64 e0;
  {
    const int lb = (b & 4) * 32 + (b >> 3) * 4 + (b & 3);
    const int row = 2 * wave + (lane >> 5), hl = lane & 31;
    const GAS u32x2* xp = (const GAS u32x2*)(P.x + lb * (8 * 1280) + (u32)(row * 1280 + hl * 8));
    u32x2 raw[5];
#pragma unroll
    for (int i = 0; i < 5; ++i) raw[i] = xp[32 * i];
    SB();
    e0 = EPOCH();
    u32 ra = L0 + L_LN + q4 * LN_PITCH + (10 * wave + g) * 32 + p4 * 8, wa = L0 + L_LN + row * LN_PITCH + hl * 8;
    int go = WS_H + (lb >> 2) * 40960 + (10 * wave + g) * 1024 + (lb & 3) * 256 + c16 * 16, g1 = go + 4096, g2 = g < 2 ? go + 8192 : OOB;
    int gs = hl == 0 ? WS_SC + (8 * lb + row) * 4 : OOB;
    *(LAS u32*)LCNT = 0;
    asm volatile("" : "+v"(ra), "+v"(go), "+v"(g1), "+v"(g2), "+v"(gs), "+v"(wa), "+s"(e0));
    SB();
    f32x2 v[10];
#pragma unroll
    for (int i = 0; i < 5; ++i) { v[2 * i] = f32x2{blo(raw[i][0]), bhi(raw[i][0])}; v[2 * i + 1] = f32x2{blo(raw[i][1]), bhi(raw[i][1])}; }
    f32x2 s = v[0] + v[1], s2 = v[2] + v[3];
#pragma unroll
    for (int i = 4; i < 10; i += 2) { s += v[i]; s2 += v[i + 1]; }
    s += s2;
    const float m = sum32(s[0] + s[1]) * (1.f / 640.f);
#pragma unroll
    for (int i = 0; i < 10; ++i) v[i] -= m;
    f32x2 qq = v[0] * v[0], q2 = v[1] * v[1];
#pragma unroll
    for (int i = 2; i < 10; i += 2) { qq += v[i] * v[i]; q2 += v[i + 1] * v[i + 1]; }
    qq += q2;
    const float var = sum32(qq[0] + qq[1]) * (1.f / 640.f);
    const float r1 = __builtin_amdgcn_rsqf(var + 1e-5f), v2 = var * r1 * r1 + 1e-5f, r2 = __builtin_amdgcn_rsqf(v2), r12 = r1 * r2;
#pragma unroll
    for (int i = 0; i < 5; ++i) {
      const f32x2 a = v[2 * i] * r12, c = v[2 * i + 1] * r12;
      *(LAS u32x2*)(wa + i * 256) = u32x2{pk2(a[0], a[1]), pk2(c[0], c[1])};
    }
    __builtin_amdgcn_raw_buffer_store_b32(__float_as_uint(v2 * r2), rs, gs, 0, 16);
    BAR();
    s16x8 f[3];
#pragma unroll
    for (int i = 0; i < 3; ++i) f[i] = cat(ldtr(ra + i * 128), ldtr(ra + i * 128 + 4 * LN_PITCH));
    st128(rs, go, f[0]); st128(rs, g1, f[1]); st128(rs, g2, f[2]);
    BAR();
    publish_last(ws + WS_LNFLAG + lb * 128, mix(e0), lane, LCNT, 4);
    BAR();
  }
  const u64 tag = mix(e0);
  ROLES();
  const u32 xr = (st * 64 + wave * 16 + c16) * 1280 + dt3 * 160, xo = xr + (g & 1) * 32 + (g >> 1) * 16;
  u32x4 x01, x23; u32x2 x4; u32 bs;
  auto ldx = [&]() {
    x01 = *(const GAS u32x4*)(P.x + xo); x23 = *(const GAS u32x4*)(P.x + xo + 64);
    x4 = *(const GAS u32x2*)(P.x + (xr + 128 + g * 8));
    bs = *(const GAS unsigned short*)(P.b2 + st * 128 + wave * 32 + (u32)(c16 * 2));
  };
  f32x4 oc[2][5];
  const u32 a30 = L0 + (32 * ty + 4 * g + q4) * 128 + (wave ^ ((2 * g + (q4 >> 1)) & 3)) * 32 + p4 * 8;
  const u32 ga0 = L0 + L3_B + ty * 5120 + lane * 16;
  const u32 oa0 = L0 + ty * 4096 + hw * 1024 + lane * 16 + ty * 8;
  u32 sa3, sb3, na3, nb3, ng3;
  struct FA { s16x8 a[2], g[5]; };
  auto lda = [&](FA& f, bool tr) {
    const u32 aa = na3, ab = nb3, ga = ng3;
    BAR();
    if (tr) { f.a[1] = cat(ldtr(ab), ldtr(ab + 2048)); f.a[0] = cat(ldtr(aa), ldtr(aa + 2048)); }
    else { f.a[1] = cat(ld64(ab), ld64(ab + 2048)); f.a[0] = cat(ld64(aa), ld64(aa + 2048)); }
#pragma unroll
    for (int j = 0; j < 5; ++j) f.g[j] = ld128(ga + j * 1024);
    sa3 = sa3 == L3_B - 8192 ? 0 : sa3 + 8192;
    sb3 = sb3 == LA3 * 10240 ? 0 : sb3 + 10240;
    na3 = (tr ? a30 : oa0) + sa3; nb3 = na3 ^ (tr ? 32 : 8); ng3 = ga0 + sb3;
  };
  auto mma = [&](const FA& f) {
#pragma unroll
    for (int j = 0; j < 5; ++j) oc[1][j] = mfma32(f.g[j], f.a[1], oc[1][j]);
#pragma unroll
    for (int j = 0; j < 5; ++j) oc[0][j] = mfma32(f.g[j], f.a[0], oc[0][j]);
  };
  auto pat = [&]() {
    SG(0x100, 1);
#pragma unroll
    for (int j = 0; j < 4; ++j) { SG(0x8, 1); SG(0x100, 2); }
#pragma unroll
    for (int j = 0; j < 5; ++j) { SG(0x8, 1); SG(0x6, 2); }
    SG(0x8, 1);
    asm volatile("" : "+v"(na3), "+v"(nb3), "+v"(ng3));
    SB();
  };
  FA fa0, fa1;
  auto begin3 = [&]() {
#pragma unroll
    for (int j = 0; j < 5; ++j) oc[0][j] = oc[1][j] = f32x4{0.f, 0.f, 0.f, 0.f};
    sa3 = sb3 = 0; na3 = a30; nb3 = a30 ^ 32; ng3 = ga0;
    lda(fa0, true); SB();
  };
  auto rounds = [&](int r0, int r1, bool tr) {
#pragma unroll 1
    for (int r = r0; r < r1; r += 2) { lda(fa1, tr); mma(fa0); pat(); lda(fa0, tr); mma(fa1); pat(); }
  };
  if (roleA) {
    f32x4 aq[5][2] = {}, akv[5][4] = {};
    const u32 wa0 = L0 + (4 * g + q4) * 480 + p4 * 8;
    const u32 xa0 = L0 + LQ_B + wave * 2048 + (p4 >> 1) * 256 + ((4 * g + q4) ^ ((p4 >> 1) << 3)) * 16 + (p4 & 1) * 8;
    const int c32 = lane & 31, hh = lane >> 5;
    const int pb = WS_PART + (hg * 4 + wave) * (16 * 896);
    u32 vp0 = L0 + L_KV + (8 * hw + 4 * ty + g) * LN_PITCH + c16 * 16 + ty * 768, vp1 = vp0 + (1 - 2 * ty) * (4 * LN_PITCH);
    u32 ka = L0 + L_KV + hh * LN_PITCH + (20 * wave + (c32 < 20 ? c32 : 19)) * 16;
    u32 vb = L0 + L_KV + (16 + hh) * LN_PITCH + (c32 < 20 ? 20 * wave + c32 : 80) * 16;
    int so0 = c32 <= 20 ? pb + rt * 896 + (hh * 21 + c32) * 16 : OOB, so1 = lane <= 21 ? pb + rt * 896 + 672 + lane * 8 : OOB;
    u32 vp2 = vp0 - ty * 768 + 512, von = L0 + L_KV + (16 + 4 * wave + g) * LN_PITCH + 1280;
    asm volatile("" : "+v"(vp2), "+v"(von), "+v"(vp0), "+v"(vp1), "+v"(ka), "+v"(vb), "+v"(so0), "+v"(so1));
    u32 qa[5][3], za[5];
#pragma unroll
    for (int je = 0; je < 5; ++je) {
      const int e = 16 * je + c16, he = e / 20, el = e - 20 * he;
#pragma unroll
      for (int x = 0; x < 3; ++x) {
        const int dl = 4 * (je - 1 + x) + g - 5 * he;
        const int qi = dl < 4 ? ((dl & 1) * 21 + el) * 2 + (dl >> 1) : 84 + el;
        qa[je][x] = L0 + L_FIN + (dl >= 0 && dl < 5 ? he * 848 + qi * 8 : 3392);
        if (je - 1 + x >= 0 && je - 1 + x < 5) asm volatile("" : "+v"(qa[je][x]));
      }
      const int dl = 4 * je + g - 5 * p4;
      const int qi = dl < 4 ? ((dl & 1) * 21 + 20) * 2 + (dl >> 1) : 104;
      za[je] = L0 + L_FIN + (dl >= 0 && dl < 5 ? p4 * 848 + qi * 8 : 3392);
      asm volatile("" : "+v"(za[je]));
    }
    u32x4 sel;
    {
      const int r = (c32 & 3) + 4 * (c32 >> 3);
      const u32 one = c32 < 16 && ((c32 >> 2) & 1) == hh ? 0x3f80u << (16 * (r & 1)) : 0u;
#pragma unroll
      for (int i = 0; i < 4; ++i) sel[i] = (r >> 1) == i ? one : 0u;
    }
    int go = lane < 53 ? pb + lane * 16 : OOB;
    u32 fa = L0 + L_FIN + (lane < 53 ? wave * 848 + lane * 16 : 3392);
    int ob = WS_O + (2 * rt + hw) * 81920 + hg * 10240 + ty * 1024 + lane * 16;
    asm volatile("" : "+v"(sel), "+v"(go), "+v"(fa), "+v"(ob));
    s16x8 xA[4] = {}, xB[4], wq[5] = {};
    u32 nwa = wa0, nwk = wa0 + ty * 96, nxa = xa0, nxb = xa0 ^ 2048;
    auto round = [&](int r, s16x8(&xo)[4], s16x8(&xn)[4]) {
      const u32 wa = nwa, wk = nwk, xa = nxa, xb = nxb;
      BAR();
      s16x8 w[6];
      xn[0] = cat(ldtr(xa), ldtr(xa + 1024)); w[0] = cat(ldtr(wk + 160), ldtr(wk + 160 + 7680));
      xn[1] = cat(ldtr(xa + 512), ldtr(xa + 1536)); xn[2] = cat(ldtr(xb), ldtr(xb + 1024)); xn[3] = cat(ldtr(xb + 512), ldtr(xb + 1536));
      w[1] = cat(ldtr(wk + 192), ldtr(wk + 192 + 7680));
#pragma unroll
      for (int j = 0; j < 5; ++j) { aq[j][0] = mfma32(wq[j], xo[0], aq[j][0]); aq[j][1] = mfma32(wq[j], xo[1], aq[j][1]); }
#pragma unroll
      for (int j = 0; j < 2; ++j) { SG(0x8, 1); SG(0x100, 2); }
#pragma unroll
      for (int j = 0; j < 8; ++j) { SG(0x8, 1); SG(0x100, 1); }
      SB();
      w[3] = cat(ldtr(wk + 320), ldtr(wk + 320 + 7680));
      w[4] = cat(ldtr(wk + 352), ldtr(wk + 352 + 7680));
      w[2] = cat(ldtr(wa + 224), ldtr(wa + 224 + 7680));
      w[5] = cat(ldtr(wa + 384), ldtr(wa + 384 + 7680));
#pragma unroll
      for (int j = 0; j < 5; ++j) wq[j] = cat(ldtr(wa + j * 32), ldtr(wa + 7680 + j * 32));
      nwa = wa0 + ((r + 1) & 7) * 15360; nwk = nwa + ty * 96; nxa = xa0 + ((r + 1) & 3) * 8192; nxb = nxa ^ 2048;
#pragma unroll
      for (int j = 0; j < 5; ++j) {
        if (j == 2) continue;
#pragma unroll
        for (int i = 0; i < 4; ++i) akv[j][i] = mfma32(xn[i], w[j], akv[j][i]);
      }
#pragma unroll
      for (int i = 0; i < 4; ++i) akv[2][i] = mfma32(xn[i & 1], w[i < 2 ? 2 : 5], akv[2][i]);
#pragma unroll
      for (int j = 0; j < 3; ++j) { SG(0x8, 1); SG(0x100, 2); }
#pragma unroll
      for (int j = 0; j < 12; ++j) { SG(0x8, 1); SG(0x100, 1); }
#pragma unroll
      for (int j = 0; j < 4; ++j) { SG(0x8, 1); SG(0x6, 2); }
      SG(0x8, 1);
      asm volatile("" : "+v"(nwa), "+v"(nwk), "+v"(nxa), "+v"(nxb));
      SB();
    };
#pragma unroll 1
    for (int r = 0; r < 20; r += 2) { round(r, xA, xB); round(r + 1, xB, xA); }
    f32x4 sk[4], sq[2];
#pragma unroll
    for (int i = 0; i < 4; ++i) sk[i] = *(const LAS f32x4*)(L0 + L_SC + ((wave ^ (i >> 1)) * 128 + (i & 1) * 64 + g * 16));
#pragma unroll
    for (int i = 0; i < 2; ++i) { const float t = *(const LAS float*)(L0 + L_SC + (wave * 128 + i * 64 + c16 * 4)); sq[i] = f32x4{t, t, t, t}; }
    BAR();
    {
      SB();
      *(LAS s16x8*)(vp2 + 16 * LN_PITCH) = cat(pk4(akv[2][2] * sk[0]), pk4(akv[2][3] * sk[1]));
      *(LAS s16x8*)(von) = s16x8{0x3f80, 0x3f80, 0x3f80, 0x3f80, 0x3f80, 0x3f80, 0x3f80, 0x3f80};
#pragma unroll
      for (int j = 3; j < 5; ++j) {
        *(LAS s16x8*)(vp0 + 16 * LN_PITCH + (j - 3) * 256) = cat(pk4(akv[j][0] * sk[0]), pk4(akv[j][1] * sk[1]));
        *(LAS s16x8*)(vp1 + 16 * LN_PITCH + (j - 3) * 256) = cat(pk4(akv[j][2] * sk[2]), pk4(akv[j][3] * sk[3]));
      }
      SB();
#pragma unroll
      for (int j = 0; j < 2; ++j) {
        s16x4 o[4];
        phi<4>(akv[j], sk, o);
        *(LAS s16x8*)(vp0 + j * 256) = cat(o[0], o[1]);
        *(LAS s16x8*)(vp1 + j * 256) = cat(o[2], o[3]);
        SB();
      }
      const f32x4 k2[2] = {akv[2][0], akv[2][1]};
      s16x4 o[2];
      phi<2>(k2, sk, o);
      *(LAS s16x8*)(vp2) = cat(o[0], o[1]);
    }
    BAR();
    {
      s16x8 fk[8], fv[8];
#pragma unroll
      for (int i = 0; i < 8; ++i) { fk[i] = ld128(ka + i * 2 * LN_PITCH); fv[i] = ld128(vb + i * 2 * LN_PITCH); }
      SB();
      f32x16 d = {};
#pragma unroll
      for (int i = 0; i < 8; ++i) d = mfmaw(fk[i], fv[i], d);
      SB();
#pragma unroll
      for (int j = 0; j < 2; ++j) { aq[j][0] = mfma32(wq[j], xA[0], aq[j][0]); aq[j][1] = mfma32(wq[j], xA[1], aq[j][1]); }
      SB();
      const u32x4 lo = {pk2(d[0], d[1]), pk2(d[2], d[3]), pk2(d[4], d[5]), pk2(d[6], d[7])};
      const u32x2 hi = {pk2(d[8], d[9]), pk2(d[10], d[11])};
      st128(rs, so0, __builtin_bit_cast(s16x8, lo));
      st64(rs, so1, __builtin_bit_cast(s16x4, hi));
      SB();
    }
    BAR();
#pragma unroll
    for (int j = 2; j < 5; ++j) { aq[j][0] = mfma32(wq[j], xA[0], aq[j][0]); aq[j][1] = mfma32(wq[j], xA[1], aq[j][1]); }
    SB();
    s16x4 pq[5][2];
#pragma unroll
    for (int j = 0; j < 2; ++j) { phi<2>(aq[j], sq, pq[j]); asm volatile("" : "+v"(pq[j][0]), "+v"(pq[j][1])); }
    publish_last(ws + WS_KVFLAG + hg * 2048 + rt * 128, tag, lane, LCNT, 8);
    ldx();
    SB();
    s16x8 p01[2], p23[2], p34[2];
#pragma unroll
    for (int j = 2; j < 5; ++j) phi<2>(aq[j], sq, pq[j]);
#pragma unroll
    for (int i = 0; i < 2; ++i) {
      p01[i] = cat(pq[0][i], pq[1][i]); p23[i] = cat(pq[2][i], pq[3][i]); p34[i] = cat(pq[3][i], pq[4][i]);
      asm volatile("" : "+v"(p01[i]), "+v"(p23[i]), "+v"(p34[i]));
    }
    begin3();
    rounds(1, 3, true);
    {
      f32x16 d = {};
      u32x4 pr[16];
#pragma unroll
      for (int i = 0; i < 8; ++i) pr[i] = __builtin_amdgcn_raw_buffer_load_b128(rs, go + (i & 3) * 896, ((i ^ 8) >> 2) * 3584, 16);
      SB();
      rounds(3, 5, true);
      BAR();
#pragma unroll
      for (int i = 8; i < 16; ++i) pr[i] = __builtin_amdgcn_raw_buffer_load_b128(rs, go + (i & 3) * 896, ((i ^ 8) >> 2) * 3584, 16);
      SB();
#pragma unroll
      for (int i = 0; i < 16; ++i) d = mfmaw(__builtin_bit_cast(s16x8, sel), __builtin_bit_cast(s16x8, pr[i]), d);
      *(LAS u32x4*)fa = u32x4{pk2(d[0], d[1]), pk2(d[2], d[3]), pk2(d[4], d[5]), pk2(d[6], d[7])};
    }
    BAR();
    {
      const u32 z0 = L0 + L_FIN + 3392;
      const s16x8 z01 = cat(ld64(za[0]), ld64(za[1])), z23 = cat(ld64(za[2]), ld64(za[3])), z4 = cat(ld64(z0), ld64(za[4]));
      const s16x8 k0 = cat(ld64(qa[0][1]), ld64(qa[0][2])), k1a = cat(ld64(qa[1][0]), ld64(qa[1][1])), k2a = cat(ld64(qa[2][1]), ld64(qa[2][2]));
      const s16x8 k3a = cat(ld64(qa[3][0]), ld64(qa[3][1])), k4 = cat(ld64(qa[4][0]), ld64(qa[4][1]));
      const s16x4 zz = {0, 0, 0, 0};
      const s16x8 k1b = cat(ld64(qa[1][2]), zz), k2b = cat(zz, ld64(qa[2][0])), k3b = cat(zz, ld64(qa[3][2]));
      SB();
      f32x4 den[2], o[5][2];
      const f32x4 e4 = {1e-6f, 1e-6f, 1e-6f, 1e-6f}, n4 = {0.f, 0.f, 0.f, 0.f};
#pragma unroll
      for (int i = 0; i < 2; ++i) den[i] = mfma32(z01, p01[i], e4);
#pragma unroll
      for (int i = 0; i < 2; ++i) den[i] = mfma32(z23, p23[i], den[i]);
#pragma unroll
      for (int i = 0; i < 2; ++i) den[i] = mfma32(z4, p34[i], den[i]);
#pragma unroll
      for (int i = 0; i < 2; ++i) { o[0][i] = mfma32(k0, p01[i], n4); o[1][i] = mfma32(k1a, p01[i], n4); }
      SB();
      float rc[5][2];
#pragma unroll
      for (int i = 0; i < 2; ++i) {
        const float r0 = __builtin_amdgcn_rcpf(den[i][0]), r1 = __builtin_amdgcn_rcpf(den[i][1]), r2 = __builtin_amdgcn_rcpf(den[i][2]), r3 = __builtin_amdgcn_rcpf(den[i][3]);
        rc[0][i] = r0; rc[1][i] = g < 1 ? r0 : r1; rc[2][i] = g < 2 ? r1 : r2; rc[3][i] = g < 3 ? r2 : r3; rc[4][i] = r3;
      }
#pragma unroll
      for (int i = 0; i < 2; ++i) { o[1][i] = mfma32(k1b, p23[i], o[1][i]); o[2][i] = mfma32(k2a, p23[i], n4); o[3][i] = mfma32(k3a, p23[i], n4); }
#pragma unroll
      for (int i = 0; i < 2; ++i) { o[2][i] = mfma32(k2b, p01[i], o[2][i]); o[3][i] = mfma32(k3b, p34[i], o[3][i]); o[4][i] = mfma32(k4, p34[i], n4); }
#pragma unroll
      for (int i = 0; i < 12; ++i) { SG(0x8, 1); SG(0x402, 1); }
      SB();
#pragma unroll
      for (int je = 0; je < 5; ++je) { st128(rs, ob + je * 2048, cat(pk4(o[je][0] * rc[je][0]), pk4(o[je][1] * rc[je][1]))); SB(); }
    }
  } else {
    if (narrow) {
      if (xm == 0 && dt == 0 && wave == 2) {
        const GAS u64* fp = (const GAS u64*)(ws + WS_LNFLAG + (u32)(lane * 128));
#pragma unroll 1
        for (u32 sp = 0; sp < SPIN_CAP; ++sp) {
          const u64 f0 = __hip_atomic_load(fp, RLX_AGENT), f1 = __hip_atomic_load(fp + 1024, RLX_AGENT);
          const u64 f2 = __hip_atomic_load(fp + 2048, RLX_AGENT), f3 = __hip_atomic_load(fp + 3072, RLX_AGENT);
          if (__all(f0 == tag && f1 == tag && f2 == tag && f3 == tag)) break;
        }
        asm volatile("" ::: "memory");
        if (lane < 2) __hip_atomic_store((GAS u64*)(ws + (lane ? WS_EPOCH : WS_ALLFLAG)), tag, RLX_AGENT);
      }
      f32x4 acc[5];
      {
        const u32x2 bb = *(const GAS u32x2*)(P.b1 + fu * 128 + hw * 64 + ty * 16 + (u32)((g & 1) * 32 + (g >> 1) * 8));
#pragma unroll
        for (int j = 0; j < 5; ++j) acc[j] = f32x4{blo(bb[0]), bhi(bb[0]), blo(bb[1]), bhi(bb[1])};
      }
      const u32 aa0 = L0 + (8 * g + q4) * 128 + ((2 * hw + (p4 & 1)) ^ ((q4 >> 1) | ((g & 1) << 1))) * 32 + ty * 16 + (p4 >> 1) * 8;
      const u32 ha0 = L0 + L1_B + lane * 16;
      u32 as = 10 * 8192, hs = 0, naa = aa0 + as, nha = ha0;
      struct FN { s16x8 a[2], h[10]; };
      auto step = [&](FN& n, const FN& o) {
        const u32 aa = naa, ha = nha;
        BAR();
        n.a[0] = cat(ldtr(aa), ldtr(aa + 512)); n.a[1] = cat(ldtr(aa + 4096), ldtr(aa + 4608));
#pragma unroll
        for (int j = 0; j < 10; ++j) n.h[j] = ld128(ha + j * 1024);
        as = as == L1_B - 8192 ? 0 : as + 8192;
        hs = hs == (NB1 - 1) * 10240 ? 0 : hs + 10240;
        naa = aa0 + as; nha = ha0 + hs;
#pragma unroll
        for (int j = 0; j < 10; ++j) acc[j % 5] = mfma32(o.a[j / 5], o.h[j], acc[j % 5]);
        SG(0x100, 2);
#pragma unroll
        for (int j = 0; j < 6; ++j) { SG(0x8, 1); SG(0x100, 2); }
#pragma unroll
        for (int j = 0; j < 4; ++j) { SG(0x8, 1); SG(0x6, 2); }
        asm volatile("" : "+v"(naa), "+v"(nha));
        SB();
      };
      FN f0 = {}, f1;
#pragma unroll 1
      for (int r = 0; r < 32; r += 2) { step(f1, f0); step(f0, f1); }
      BAR();
#pragma unroll
      for (int j = 0; j < 10; ++j) acc[j % 5] = mfma32(f0.a[j / 5], f0.h[j], acc[j % 5]);
      const int go = WS_G + dt * 276480 + (fu * 2 + hw) * 5120 + (g & 1) * 1024 + (2 * ty + (g >> 1)) * 256 + c16 * 16;
#pragma unroll
      for (int j = 0; j < 5; j += 2) {
        const u32x4 o = swap16(__builtin_bit_cast(u32x4, gelu8(acc[j], acc[j < 4 ? j + 1 : j])));
        st128(rs, j < 4 || !(g & 1) ? go + j * 1024 : OOB, __builtin_bit_cast(s16x8, o));
      }
      BAR();
      const u32 ra = L0 + (4 * g + q4) * 640 + wave * 160 + p4 * 8;
      s16x8 f[5];
#pragma unroll
      for (int j = 0; j < 5; ++j) f[j] = cat(ldtr(ra + j * 32), ldtr(ra + j * 32 + 10240));
      publish_last(ws + WS_GFLAG + dt * 8192 + gi * 128, tag, lane, LCNT, 8);
      BAR();
      const int gw = WS_WO + (4 * (dt & 1) + wave) * 102400 + (xm * 4 + (dt >> 1)) * 5120 + lane * 16;
#pragma unroll
      for (int j = 0; j < 5; ++j) st128(rs, gw + j * 1024, f[j]);
      SB();
    } else {
      f32x4 acc[2][5];
#pragma unroll
      for (int t = 0; t < 2; ++t) {
        const u32x2 bb = *(const GAS u32x2*)(P.b1 + fu * 128 + wave * 64 + (u32)(t * 32 + g * 8));
        const f32x4 bi = {blo(bb[0]), bhi(bb[0]), blo(bb[1]), bhi(bb[1])};
#pragma unroll
        for (int j = 0; j < 5; ++j) acc[t][j] = bi;
      }
      s16x8 a1[2] = {}, h1[5] = {};
      constexpr int RP = 256;
      const u32 aa0 = L0 + (8 * g + q4) * RP + ((2 * wave) ^ (q4 | ((g & 1) << 2))) * 32 + p4 * 8;
      const u32 ha0 = L0 + L1_B + lane * 16;
      u32 as = 6 * 8192, hs = 0, naa = aa0 + as, nab = naa ^ 32, nha = ha0;
#pragma unroll 1
      for (int r = 0; r < 32; ++r) {
        const u32 aa = naa, ab = nab, ha = nha;
        BAR();
        s16x8 a0[2], h0[5];
        a0[0] = cat(ldtr(aa), ldtr(aa + 4 * RP)); h0[0] = ld128(ha); a0[1] = cat(ldtr(ab), ldtr(ab + 4 * RP));
#pragma unroll
        for (int j = 1; j < 5; ++j) h0[j] = ld128(ha + j * 1024);
#pragma unroll
        for (int j = 0; j < 5; ++j) { acc[0][j] = mfma32(a1[0], h1[j], acc[0][j]); acc[1][j] = mfma32(a1[1], h1[j], acc[1][j]); }
#pragma unroll
        for (int j = 0; j < 9; ++j) { SG(0x8, 1); SG(0x100, 1); }
        SG(0x8, 1);
        SB();
        a1[0] = cat(ldtr(aa + 32 * RP), ldtr(aa + 36 * RP)); a1[1] = cat(ldtr(ab + 32 * RP), ldtr(ab + 36 * RP));
#pragma unroll
        for (int j = 0; j < 5; ++j) h1[j] = ld128(ha + 5120 + j * 1024);
        as = as == L1_B - 64 * RP ? 0 : as + 64 * RP;
        hs = hs == (NB1 - 1) * 10240 ? 0 : hs + 10240;
        naa = aa0 + as; nab = naa ^ 32; nha = ha0 + hs;
#pragma unroll
        for (int j = 0; j < 5; ++j) { acc[0][j] = mfma32(a0[0], h0[j], acc[0][j]); acc[1][j] = mfma32(a0[1], h0[j], acc[1][j]); }
        SG(0x100, 1);
#pragma unroll
        for (int j = 0; j < 4; ++j) { SG(0x8, 1); SG(0x100, 2); }
#pragma unroll
        for (int j = 0; j < 5; ++j) { SG(0x8, 1); SG(0x6, 2); }
        SG(0x8, 1);
        asm volatile("" : "+v"(naa), "+v"(nab), "+v"(nha));
        SB();
      }
      BAR();
#pragma unroll
      for (int j = 0; j < 5; ++j) { acc[0][j] = mfma32(a1[0], h1[j], acc[0][j]); acc[1][j] = mfma32(a1[1], h1[j], acc[1][j]); }
      const int go = WS_G + dt * 276480 + (fu * 2 + wave) * 5120 + lane * 16;
#pragma unroll
      for (int j = 0; j < 5; ++j) st128(rs, go + j * 1024, gelu8(acc[0][j], acc[1][j]));
    }
    if (!late && !narrow) {
      publish_last(ws + WS_GFLAG + dt * 8192 + gi * 128, tag, lane, LCNT, 8);
      BAR();
    }
    ldx();
    begin3();
  }

  {
    rounds(roleA ? 5 : 1, roleA ? R_JOIN : PUBR, true);
    {
      asm volatile("" : "+v"(x01), "+v"(x23), "+v"(x4), "+v"(bs));
      const float bf = blo(bs);
      x01 = swap16(x01); x23 = swap16(x23);
      oc[0][0] += f32x4{blo(x01[0]), bhi(x01[0]), blo(x01[1]), bhi(x01[1])} + bf;
      oc[0][1] += f32x4{blo(x01[2]), bhi(x01[2]), blo(x01[3]), bhi(x01[3])} + bf;
      oc[0][2] += f32x4{blo(x23[0]), bhi(x23[0]), blo(x23[1]), bhi(x23[1])} + bf;
      oc[0][3] += f32x4{blo(x23[2]), bhi(x23[2]), blo(x23[3]), bhi(x23[3])} + bf;
      oc[0][4] += f32x4{blo(x4[0]), bhi(x4[0]), blo(x4[1]), bhi(x4[1])} + bf;
      if (late) publish_last(ws + WS_GFLAG + dt * 8192 + gi * 128, tag, lane, LCNT, 8);
      else if (roleA || narrow)
        publish(ws + (roleA ? WS_OFLAG + (2 * rt + hw) * 2048 + (ty * 8 + hg) * 128 : WS_GFLAG + (4 * (dt & 1) + wave) * 8192 + (16 + xm * 4 + (dt >> 1)) * 128), tag, lane);
      SB();
    }
    rounds(roleA ? R_JOIN : PUBR, 27, true);
    na3 = oa0 + sa3; nb3 = na3 ^ 8;
    static_assert(36 % NB3 < 2, "");
    u32 ew = L0 + L3_B + 20480 + wave * 5120 + lane * 16, er = L0 + L3_B + 20480 + (wave ^ 1) * 5120 + lane * 16, xo4 = xr + 128 + g * 8;
    asm volatile("" : "+v"(ew), "+v"(er), "+v"(xo4));
    rounds(27, 37, false);
    mma(fa0);
#pragma unroll
    for (int j = 0; j < 5; ++j) *(LAS f32x4*)(ew + j * 1024) = oc[1][j];
    SG(0x8, 6);
#pragma unroll
    for (int j = 0; j < 4; ++j) { SG(0x200, 1); SG(0x8, 1); }
    SG(0x200, 1);
    SB();
    BAR();
#pragma unroll
    for (int j = 0; j < 5; ++j) oc[1][j] = *(const LAS f32x4*)(er + j * 1024);
    SB();
#pragma unroll
    for (int j = 0; j < 5; ++j) oc[0][j] += oc[1][j];
    *(GAS u32x4*)(P.out + xo) = swap16(__builtin_bit_cast(u32x4, cat(pk4(oc[0][0]), pk4(oc[0][1]))));
    *(GAS u32x4*)(P.out + xo + 64) = swap16(__builtin_bit_cast(u32x4, cat(pk4(oc[0][2]), pk4(oc[0][3]))));
    *(GAS s16x4*)(P.out + xo4) = pk4(oc[0][4]);
    asm volatile(".Lcode_exit_b:" ::: "memory");
  }
}

extern "C" void kernel_launch(void* const* d_in, const int* in_sizes, int n_in, void* d_out, int out_size, void* d_ws,
                              size_t ws_size, hipStream_t stream) {
  Params p;
  p.x = (const char*)d_in[0];
  p.wqkv = (const char*)d_in[1];
  p.wo = (const char*)d_in[2];
  p.w1 = (const char*)d_in[4];
  p.b1 = (const char*)d_in[5];
  p.w2 = (const char*)d_in[6];
  p.b2 = (const char*)d_in[7];
  p.out = (char*)d_out;
  p.ws = (char*)d_ws;
  (void)hipFuncSetAttribute((const void*)fused_fwd, hipFuncAttributeMaxDynamicSharedMemorySize, L_BYTES);
  fused_fwd<<<256, 320, L_BYTES, stream>>>(p);
}
```

```cpp
#include <hip/hip_runtime.h>
#include <hip/hip_bf16.h>
#pragma clang diagnostic ignored "-Wint-to-pointer-cast"

#define GAS __attribute__((address_space(1)))
#define LAS __attribute__((address_space(3)))
#define DI __device__ __forceinline__
typedef unsigned u32;
typedef unsigned long long u64;
typedef short s16x4 __attribute__((ext_vector_type(4)));
typedef short s16x8 __attribute__((ext_vector_type(8)));
typedef __bf16 b16x2 __attribute__((ext_vector_type(2)));
typedef __bf16 b16x8 __attribute__((ext_vector_type(8)));
typedef float f32x2 __attribute__((ext_vector_type(2)));
typedef float f32x4 __attribute__((ext_vector_type(4)));
typedef float f32x16 __attribute__((ext_vector_type(16)));
typedef unsigned u32x2 __attribute__((ext_vector_type(2)));
typedef unsigned u32x4 __attribute__((ext_vector_type(4)));

constexpr int WS_EPOCH = 0;
constexpr int WS_ALLFLAG = 1024;
constexpr int WS_LNFLAG = 196608;
constexpr int WS_GFLAG = 720896;
constexpr int WS_KVFLAG = 65536;
constexpr int WS_OFLAG = 131072;
constexpr int WS_PART = 262144;
constexpr int WS_SC = 8192;
constexpr int WS_H = 786432;
constexpr int WS_G = WS_H + 2621440;
constexpr int WS_O = WS_G + 2211840;
constexpr int WS_WO = WS_O + 2621440;
constexpr int WS_END = WS_WO + 819200;
constexpr int OOB = 0x40000000;
constexpr int L_BYTES = 163840;
constexpr int L_LN = 131072, LN_PITCH = 1296;
constexpr int NB3 = 5, NB1 = 4, NA1 = 7;
constexpr int LA3 = NB3 - 1, NA3 = 18 - NB3, H3 = 2 * (NA3 - LA3 - 2);
constexpr int LQ_B = 122880, L1_B = NA1 * 16384, L3_B = NA3 * 8192;
constexpr int L_KV = L3_B, L_FIN = L3_B + NB3 * 10240, L_SC = L_FIN + 3584;
constexpr int PRE_A = 2;
constexpr int APRE = 20, NPK_A = 8, R_JOIN = 9, LPK = 22, NTCH = 7, EARLY = 16, PUBR = 3, TH_PA = 10, TH_P3 = 8, SLP = 2, MID_A = 2, PRE_B = 3, MID_B = 3, XP = 1;
constexpr u32 SPIN_CAP = 1u << 16;

#define BAR() do { asm volatile("" ::: "memory"); __builtin_amdgcn_s_waitcnt(0xC07F); asm volatile("s_barrier" ::: "memory"); } while (0)
#define WAITV(n) asm volatile("s_waitcnt vmcnt(" #n ")" ::: "memory")
#define WAITX(n) asm volatile("s_waitcnt vmcnt(%0)" ::"i"((n) > 63 ? 63 : (n)) : "memory")
#define LBAR() asm volatile("s_barrier" ::: "memory")
#define RLX_AGENT __ATOMIC_RELAXED, __HIP_MEMORY_SCOPE_AGENT

DI f32x4 mfma32(s16x8 a, s16x8 b, f32x4 c) {
  return __builtin_amdgcn_mfma_f32_16x16x32_bf16(__builtin_bit_cast(b16x8, a), __builtin_bit_cast(b16x8, b), c, 0, 0, 0);
}
DI f32x16 mfmaw(s16x8 a, s16x8 b, f32x16 c) {
  return __builtin_amdgcn_mfma_f32_32x32x16_bf16(__builtin_bit_cast(b16x8, a), __builtin_bit_cast(b16x8, b), c, 0, 0, 0);
}
DI s16x4 ldtr(u32 a) { return __builtin_amdgcn_ds_read_tr16_b64_v4i16((LAS s16x4*)a); }
DI s16x8 ld128(u32 a) { return *(const LAS s16x8*)a; }
DI s16x4 ld64(u32 a) { return *(const LAS s16x4*)a; }
DI s16x8 cat(s16x4 a, s16x4 b) { return __builtin_shufflevector(a, b, 0, 1, 2, 3, 4, 5, 6, 7); }
DI u32 pk2(float a, float b) { f32x2 v = {a, b}; return __builtin_bit_cast(u32, __builtin_convertvector(v, b16x2)); }
DI s16x4 pk4(f32x4 v) { u32x2 r = {pk2(v[0], v[1]), pk2(v[2], v[3])}; return __builtin_bit_cast(s16x4, r); }
DI float blo(u32 u) { return __uint_as_float(u << 16); }
DI float bhi(u32 u) { return __uint_as_float(u & 0xffff0000u); }
template <int C> DI float dpp_add(float s) { return s + __int_as_float(__builtin_amdgcn_update_dpp(0, __float_as_int(s), C, 0xf, 0xf, true)); }
DI float sum32(float s) {
  s = dpp_add<0x121>(s); s = dpp_add<0x122>(s); s = dpp_add<0x124>(s); s = dpp_add<0x128>(s);
  auto r = __builtin_amdgcn_permlane16_swap(__float_as_uint(s), __float_as_uint(s), false, false);
  return __uint_as_float(r[0]) + __uint_as_float(r[1]);
}
#define SG(m, n) __builtin_amdgcn_sched_group_barrier(m, n, 0)
#define SB() __builtin_amdgcn_sched_barrier(0)
#define AL() do { asm volatile(".p2align 3"); SB(); } while (0)
template <int N> DI void phi(const f32x4 (&u)[N], const f32x4* sc, s16x4 (&o)[N]) {
  const float L = 1.4426950408889634f;
  f32x4 a[N], y[N];
  SB(); AL();
#pragma unroll
  for (int i = 0; i < N; ++i) a[i] = __builtin_elementwise_fma(u[i], sc[i], f32x4{1.f, 1.f, 1.f, 1.f});
  SB();
#pragma unroll
  for (int i = 0; i < N; ++i) y[i] = __builtin_elementwise_fma(a[i], f32x4{L, L, L, L}, f32x4{-L, -L, -L, -L});
  SB();
#pragma unroll
  for (int i = 0; i < 4 * N; ++i) y[i >> 2][i & 3] = __builtin_amdgcn_fmed3f(__builtin_amdgcn_exp2f(y[i >> 2][i & 3]), 0.f, 1.f);
  SB();
#pragma unroll
  for (int i = 0; i < 4 * N; ++i) y[i >> 2][i & 3] = __int_as_float(max(__float_as_int(a[i >> 2][i & 3]), __float_as_int(y[i >> 2][i & 3])));
  SB();
#pragma unroll
  for (int i = 0; i < N; ++i) y[i] += 1e-6f;
  SB();
#pragma unroll
  for (int i = 0; i < N; ++i) o[i] = pk4(y[i]);
}
DI s16x8 gelu8(f32x4 u0, f32x4 u1) {
  const float K = -2.f * 0.7978845608028654f * 1.4426950408889634f, K3 = K * 0.044715f;
  SB(); AL();
  f32x4 y0 = u0 * u0, y1 = u1 * u1;
  SB();
  y0 = __builtin_elementwise_fma(y0, f32x4{K3, K3, K3, K3}, f32x4{K, K, K, K});
  y1 = __builtin_elementwise_fma(y1, f32x4{K3, K3, K3, K3}, f32x4{K, K, K, K});
  SB();
  y0 *= u0; y1 *= u1;
  SB();
#pragma unroll
  for (int i = 0; i < 4; ++i) y0[i] = __builtin_amdgcn_exp2f(y0[i]);
#pragma unroll
  for (int i = 0; i < 4; ++i) y1[i] = __builtin_amdgcn_exp2f(y1[i]);
  SB();
  y0 += 1.f; y1 += 1.f;
  SB();
#pragma unroll
  for (int i = 0; i < 4; ++i) y0[i] = __builtin_amdgcn_rcpf(y0[i]);
#pragma unroll
  for (int i = 0; i < 4; ++i) y1[i] = __builtin_amdgcn_rcpf(y1[i]);
  SB();
  y0 *= u0; y1 *= u1;
  SB();
  return cat(pk4(y0), pk4(y1));
}
DI void st128(__amdgpu_buffer_rsrc_t rs, int off, s16x8 v) { __builtin_amdgcn_raw_buffer_store_b128(__builtin_bit_cast(u32x4, v), rs, off, 0, 16); }
DI void st64(__amdgpu_buffer_rsrc_t rs, int off, s16x4 v) { __builtin_amdgcn_raw_buffer_store_b64(__builtin_bit_cast(u32x2, v), rs, off, 0, 16); }
DI u64 mix(u64 e) {
  return (u64)((u32)(e >> 32) * 0x85EBCA6Bu + 0x165667B1u) << 32 | (u32)((u32)e * 0x9E3779B1u + 0x7F4A7C15u);
}

#define GL(v, off, sc) "global_load_lds_dwordx4 " v ", %0 offset:" #off sc "\n\t"
#define M0(bias) "s_add_u32 m0, %1, " #bias "\n\ts_nop 0\n\t"
#define RING m = m == HI - STEP ? LO : m + STEP
template <int STEP, int LO, int HI> DI void gl4(const char* base, u32& m, const u32 (&v)[4]) {
  asm volatile(M0(0) GL("%2", 0, "") GL("%3", 1024, "") GL("%4", 2048, "") GL("%5", 3072, "")
               ::"s"(base), "s"(m), "v"(v[0]), "v"(v[1]), "v"(v[2]), "v"(v[3]) : "memory", "scc");
  RING;
}
template <int STEP, int LO, int HI> DI void gl8(const char* base, u32& m, const u32 (&v)[8]) {
  asm volatile(M0(4096) GL("%2", -4096, "") GL("%3", -3072, "") GL("%4", -2048, "") GL("%5", -1024, "")
               GL("%6", 0, "") GL("%7", 1024, "") GL("%8", 2048, "") GL("%9", 3072, "")
               ::"s"(base), "s"(m), "v"(v[0]), "v"(v[1]), "v"(v[2]), "v"(v[3]), "v"(v[4]), "v"(v[5]), "v"(v[6]), "v"(v[7]) : "memory", "scc");
  RING;
}
template <int STEP, int LO, int HI> DI void gl8w(const char* base, u32& m, const u32 (&v)[8]) {
  asm volatile("s_add_u32 m0, %1, 3840\n\ts_mov_b32 exec_hi, 0x0fffffff\n\t" GL("%2", -3840, "") GL("%3", -2880, "") GL("%4", -1920, "") GL("%5", -960, "")
               GL("%6", 0, "") GL("%7", 960, "") GL("%8", 1920, "") GL("%9", 2880, "") "s_mov_b32 exec_hi, -1"
               ::"s"(base), "s"(m), "v"(v[0]), "v"(v[1]), "v"(v[2]), "v"(v[3]), "v"(v[4]), "v"(v[5]), "v"(v[6]), "v"(v[7]) : "memory", "scc");
  RING;
}
DI void gl5(const char* base, u32 m, const u32 (&v)[5]) {
  asm volatile(M0(4096) GL("%2", -4096, "") GL("%3", -3072, "") GL("%4", -2048, "") GL("%5", -1024, "") GL("%6", 0, "")
               ::"s"(base), "s"(m), "v"(v[0]), "v"(v[1]), "v"(v[2]), "v"(v[3]), "v"(v[4]) : "memory", "scc");
}
#define GL8S M0(4096) GL("%2", -4096, " sc1") GL("%2", -3072, " sc1") GL("%2", -2048, " sc1") GL("%2", -1024, " sc1") \
             GL("%2", 0, " sc1") GL("%2", 1024, " sc1") GL("%2", 2048, " sc1") GL("%2", 3072, " sc1")
template <int STEP, int LO, int HI> DI void glin8(const char* base, u32& m, u32 vl4) {
  asm volatile(GL8S ::"s"(base), "s"(m), "v"(vl4) : "memory", "scc");
  RING;
}
template <int STEP, int LO, int HI> DI void glin10(const char* base, u32& m, u32 vl4, u32 vl8) {
  asm volatile(GL8S M0(8192) GL("%3", 0, " sc1") GL("%3", 1024, " sc1")
               ::"s"(base), "s"(m), "v"(vl4), "v"(vl8) : "memory", "scc");
  RING;
}
template <int STEP, int LO, int HI> DI void glin55(const char* base, u32& m, u32 vl4, u32 vl4b) {
  asm volatile(M0(4096) GL("%2", -4096, " sc1") GL("%2", -3072, " sc1") GL("%2", -2048, " sc1") GL("%2", -1024, " sc1") GL("%2", 0, " sc1")
               M0(9216) GL("%3", -4096, " sc1") GL("%3", -3072, " sc1") GL("%3", -2048, " sc1") GL("%3", -1024, " sc1") GL("%3", 0, " sc1")
               ::"s"(base), "s"(m), "v"(vl4), "v"(vl4b) : "memory", "scc");
  RING;
}
template <int STEP, int LO, int HI> DI void glin2222(const char* base, u32& m, const u32 (&v)[4]) {
  asm volatile(M0(4096) GL("%2", -4096, " sc1") GL("%2", -3072, " sc1") GL("%3", -2048, " sc1") GL("%3", -1024, " sc1")
               GL("%4", 0, " sc1") GL("%4", 1024, " sc1") GL("%5", 2048, " sc1") GL("%5", 3072, " sc1")
               ::"s"(base), "s"(m), "v"(v[0]), "v"(v[1]), "v"(v[2]), "v"(v[3]) : "memory", "scc");
  RING;
}
DI void glin1(const char* base, u32 m, u32 v) { asm volatile(M0(0) GL("%2", 0, " sc1")::"s"(base), "s"(m), "v"(v) : "memory", "scc"); }
DI u32x4 swap16(u32x4 t) {
  const auto s0 = __builtin_amdgcn_permlane16_swap(t[0], t[2], false, false), s1 = __builtin_amdgcn_permlane16_swap(t[1], t[3], false, false);
  return u32x4{s0[0], s1[0], s0[1], s1[1]};
}
DI void touch(const char* base, u32 m, u32 v) {
  asm volatile("s_mov_b32 m0, %1\n\ts_nop 0\n\tglobal_load_lds_dword %2, %0" ::"s"(base), "s"(m), "v"(v) : "memory");
}
DI void poll(const char* f, int idx, u64 tag) {
  const GAS u64* p = (const GAS u64*)(f + (u32)(idx * 128));
#pragma unroll 1
  for (u32 sp = 0; sp < SPIN_CAP; ++sp) {
    const u64 v = __hip_atomic_load(p, RLX_AGENT);
    if (__all(v == tag)) break;
    __builtin_amdgcn_s_sleep(SLP);
  }
  asm volatile("" ::: "memory");
}
template <int R = 254> DI void peek(const char* f, int idx, u64 tag) {
  asm volatile("v_not_b32 v%c2, %4\n\tglobal_load_dwordx2 v[%c2:%c3], %0, %1 sc1" ::"v"(idx * 128), "s"(f), "i"(R), "i"(R + 1), "s"((u32)tag)
               : "memory", "v252", "v253", "v254", "v255");
}
template <int R = 254> DI bool seen(u64 tag) {
  u64 miss;
  asm volatile("v_cmp_ne_u64 %0, %1, v[%c2:%c3]" : "=s"(miss) : "s"(tag), "i"(R), "i"(R + 1) : "memory");
  return miss == 0;
}
template <int R = 254> DI void await(const char* f, int idx, u64 tag) { if (!seen<R>(tag)) poll(f, idx, tag); }
DI void publish(char* f, u64 tag, int lane) {
  WAITV(0);
  if (lane == 0) __hip_atomic_store((GAS u64*)f, tag, RLX_AGENT);
}
DI void publish_last(char* f, u64 tag, int lane, u32 cnt, u32 n) {
  WAITV(0);
  if (lane == 0 && __hip_atomic_fetch_add((LAS u32*)cnt, 1u, __ATOMIC_RELAXED, __HIP_MEMORY_SCOPE_WORKGROUP) == n - 1)
    __hip_atomic_store((GAS u64*)f, tag, RLX_AGENT);
}

struct Params {
  const char *x, *wqkv, *wo, *w1, *b1, *w2, *b2;
  char *out, *ws;
};
#define ROLES() \
  int b_ = b; asm volatile("" : "+s"(b_)); \
  const int xc = b_ & 7, jb = b_ >> 3; \
  const bool roleA = b_ < 128; \
  const int hg = 2 * (xc & 3) + (jb & 1), rt = 8 * (xc >> 2) + (jb >> 1); \
  const int dt = jb & 7, grp = (jb >> 3) & 1; \
  const bool late = !roleA && xc >= 5, narrow = !roleA && xc < 5 && grp; \
  const int fu = late ? 4 * xc - 5 + 2 * grp : grp ? xc : 5 + 2 * xc, gi = late ? 2 * xc + grp : grp ? xc : 5 + xc; \
  const int st = 4 * xc + (jb >> 3), dt3 = jb & 7; \
  const u32 d0 = dt ^ (XP && late ? grp : 0), istep = narrow ? 2 : 1, rot = (5 * (2 * (rt & 7) + (rt >> 3))) >> 2; \
  (void)hg; (void)fu; (void)gi; (void)st; (void)dt3; (void)d0; (void)istep; (void)rot
#define EPOCH() *(const __attribute__((address_space(4))) u64*)(ws + WS_EPOCH)
__global__ __launch_bounds__(320, 2) void fused_fwd(Params P) {
  const int tid = threadIdx.x, lane = tid & 63;
  const int wave = __builtin_amdgcn_readfirstlane(tid >> 6);
  const int b = blockIdx.x;
  char* const ws = P.ws;
  constexpr u32 L0 = 0;
  const u32 LDUM = L0 + L_BYTES - 256, LCNT = L0 + L_BYTES - 512;
  const int c16 = lane & 15, g = lane >> 4, q4 = c16 >> 2, p4 = lane & 3;
  {
    u64 pc; int left;
    asm volatile("s_getpc_b64 %0\n.Lcode_here:\n\ts_mov_b32 %1, .Lcode_exit_a-.Lcode_here\n\ts_max_i32 %1, %1, .Lcode_exit_b-.Lcode_here"
                 : "=s"(pc), "=&s"(left)::"scc");
    touch((const char*)pc, LDUM, min((((b >> 3) * 5 + wave) * 2 + (lane & 1)) * 128, left));
  }
  if (wave == 4) {
    __builtin_amdgcn_s_setprio(3);
    u32 v[8], sa;
    if (b >= 128) {
      ROLES();
      const u32 col = narrow ? ((lane & 7) ^ ((g & 1) << 1)) * 16 : (c16 ^ (g << 1)) * 16, stp = (narrow ? 8 : 4) * 3456 - 1024;
#pragma unroll
      for (int i = 0; i < 8; ++i)
        v[i] = (narrow ? lane >> 3 : g) * 3456 + (col ^ (narrow ? (i & 1) * 64 : ((i >> 1) & 1) * 128)) + i * stp + 4096;
      sa = narrow ? 10 * 8192 : 6 * 8192;
#pragma unroll 1
      for (int i = 0; i < PRE_B; ++i) gl8<8192, 0, L1_B>(P.w1 + fu * 128 + (8 * d0 + i * istep) * 110592, sa, v);
    } else {
      ROLES();
      const u32 hi = lane >= 30, cc = lane - 30 * hi, sg = (cc >= 10) + (cc >= 20);
      v[0] = (hi + 1) * 3840 + sg * 1120 + cc * 16;
#pragma unroll
      for (int i = 1; i < 8; ++i) v[i] = v[0] + i * 6720;
      sa = 0;
      const char* wp = P.wqkv + (hg * 160 + rot * 122880);
      gl8w<7680, 0, LQ_B>(wp, sa, v); gl8w<7680, 0, LQ_B>(wp + 61440, sa, v);
    }
    u64 e0 = EPOCH();
    ROLES();
    asm volatile("" : "+s"(e0));
    BAR(); LBAR();
    const u64 tag = mix(e0);
    const u32 vl4 = lane * 16 + 4096, vl8 = vl4 + 4096;
    const u32 u0 = (lane >> 3) * 4096 + ((lane & 7) ^ (g << 1)) * 16;
    const u32 u[4] = {u0, u0 + 31744, u0 + 2 * 31744, u0 + 3 * 31744};
    const char* w2p = P.w2 + st * 128;
    u32 sa3 = 0;
    auto ahalf = [&]() { gl4<4096, 0, L3_B>(w2p, sa3, u); w2p += 131072; };
    const char* const gfl = ws + WS_GFLAG + dt3 * 8192;
    const char* bp = ws + WS_G + dt3 * 276480;
    u32 sb3 = L3_B;
    auto bunit3 = [&]() { glin10<10240, L3_B, L3_B + NB3 * 10240>(bp, sb3, vl4, vl8); bp += 10240; };
    auto start3 = [&](int done) {
#pragma unroll 1
      for (int i = 0; i < 4; ++i) bunit3();
#pragma unroll 1
      for (int i = done; i < 2 * NA3; i += 2) { ahalf(); ahalf(); }
    };
    if (!roleA) {
      const char* wb = P.w1 + fu * 128;
      const char* hb = ws + WS_H + dt * 5120;
      u32 ia = PRE_B * istep, ib2 = 0, sb = L1_B;
      static_assert(PRE_B <= 4, "");
      auto a8 = [&]() { gl8<8192, 0, L1_B>(wb + (ia ^ (8 * d0)) * 110592, sa, v); ia += istep; };
      auto bunit = [&]() { glin55<10240, L1_B, L1_B + NB1 * 10240>(hb + (ib2 ^ (4 * d0)) * 81920, sb, vl4, vl4 + 40960); ++ib2; };
#pragma unroll 1
      for (int i = 0; i < MID_B; ++i) a8();
      LBAR();
#pragma unroll 1
      for (int i = PRE_B + MID_B; i < 10; ++i) { WAITV(32); a8(); }
      peek(ws + WS_LNFLAG, (d0 & 6) * 32 + lane, tag);
      a8(); a8();
      WAITV(16);
      await(ws + WS_LNFLAG, (d0 & 6) * 32 + lane, tag);
      bunit(); bunit(); bunit();
      a8(); a8();
      WAITX(36); LBAR(); bunit();
      WAITX(36);
      if (narrow) {
#pragma unroll 1
        for (int r = 1; r < 3; ++r) { LBAR(); bunit(); WAITX(20); }
        peek(ws + WS_ALLFLAG, 0, tag);
#pragma unroll 1
        for (int r = 3; r < 5; ++r) { LBAR(); bunit(); WAITX(20); }
      } else {
#pragma unroll 1
        for (int r = 1; r < 3; ++r) { LBAR(); a8(); a8(); bunit(); WAITX(52); }
        peek(ws + WS_ALLFLAG, 0, tag);
#pragma unroll 1
        for (int r = 3; r < 5; ++r) { LBAR(); a8(); a8(); bunit(); WAITX(52); }
      }
      await(ws + WS_ALLFLAG, 0, tag);
      if (narrow) {
#pragma unroll 1
        for (int r = 5; r < 29; ++r) { LBAR(); if (r & 3) a8(); bunit(); WAITX(28); }
      } else {
#pragma unroll 1
        for (int r = 5; r < 29; ++r) { LBAR(); a8(); if (r & 3) a8(); bunit(); WAITX(44); }
      }
      LBAR();
      if (narrow) {
        u32 w5[5];
#pragma unroll
        for (int i = 0; i < 5; ++i) { const u32 n = 64 * i + lane; w5[i] = n / 40u * 640 + lane * 16 + 4096; }
        const char* wp = P.wo + (xc * 4 + (dt >> 1)) * (32 * 1280) + (dt & 1) * 640;
#pragma unroll 1
        for (int i = 0; i < 4; ++i) gl5(wp + i * (8 * 1280), i * 5120, w5);
        WAITX(30); LBAR(); WAITX(20); LBAR();
      } else {
        constexpr int E1 = 3 * EARLY / 8;
#pragma unroll 1
        for (int i = 0; i < E1; ++i) ahalf();
        WAITX(18 + 4 * E1); LBAR();
#pragma unroll 1
        for (int i = 0; i < E1; ++i) ahalf();
        WAITX(8 * E1); LBAR();
        if (!late) {
#pragma unroll 1
          for (int i = 2 * E1; i < EARLY; ++i) ahalf();
        }
      }
      if (!late) {
        LBAR();
        if (narrow) { WAITV(0); LBAR(); LBAR(); }
#pragma unroll 1
        for (int i = narrow ? -16 : 0; i < 10; ++i) { ahalf(); WAITV(32); if (!narrow) WAITX(TH_P3); }
        peek(gfl, lane < 5 ? lane : 0, tag);
        WAITV(0);
        if (!narrow) LBAR();
      } else {
        ahalf(); ahalf(); ahalf(); ahalf();
        LBAR();
#pragma unroll 1
        for (int i = 3 * EARLY / 8 * 2 + 4; i < LPK; ++i) ahalf();
        peek(gfl, lane < 5 ? lane : 0, tag);
        ahalf(); ahalf(); ahalf(); ahalf();
        WAITV(16);
      }
      await(gfl, lane < 5 ? lane : 0, tag);
      start3(late ? LPK + 4 : narrow ? 26 : EARLY + 10);
    } else {
      const char* wb = P.wqkv + hg * 160;
      const char* xb = ws + WS_H + rt * 163840;
      const u32 x0 = (lane ^ ((g & 1) << 3)) * 16 + 4096, xv[4] = {x0, x0 + 38912, x0 + 2 * 38912, x0 + 3 * 38912};
      u32 oa = (rot + 1) * 122880  , ob = rot * 2048, sb = LQ_B;
      auto a8 = [&]() { gl8w<7680, 0, LQ_B>(wb + oa, sa, v); oa = oa == 39 * 61440 ? 0 : oa + 61440; };
      auto bunit = [&]() { glin2222<8192, LQ_B, LQ_B + 4 * 8192>(xb + ob, sb, xv); ob = ob == 19 * 2048 ? 0 : ob + 2048; };
#pragma unroll 1
      for (int i = 0; i < MID_A; ++i) a8();
      LBAR();
#pragma unroll 1
      for (int i = PRE_A + MID_A; i < NPK_A; ++i) { WAITV(24); a8(); }
      peek(ws + WS_LNFLAG, rt * 16 + c16, tag);
      a8(); a8();
      WAITV(16);
      await(ws + WS_LNFLAG, rt * 16 + c16, tag);
      bunit(); bunit(); bunit();
      glin1(ws + WS_SC + rt * 512, L_SC, (lane & 31) * 16);
#pragma unroll 1
      for (int i = NPK_A + 2; i < 12; ++i) a8();
      WAITX(16 + 8 * (10 - NPK_A));
#pragma unroll 1
      for (int r = 1; r < 17; ++r) { LBAR(); a8(); if (r & 3) a8(); bunit(); WAITX(40); }
      LBAR(); bunit();
      WAITX(24); LBAR();
      const char* tb = P.w2 + xc * 512 + jb * (108 * 4096);
      const u32 tv = (lane >> 2) * 4096 + p4 * 128;
#pragma unroll 1
      for (int i = 0; i < NTCH; ++i) touch(tb + (i == 6 ? 92 : 16 * i) * 4096, LDUM, tv);
      WAITX(8 + NTCH); LBAR(); WAITX(NTCH); LBAR();
      BAR();
      const char* const kfl = ws + WS_KVFLAG + hg * 2048;
      int done = 0;
#pragma unroll 1
      for (; done < APRE; ++done) { ahalf(); WAITV(24); }
      LBAR();
      peek(gfl, lane < 5 ? lane : 0, tag);
      WAITV(0);
      LBAR();
      if (!seen(tag)) {
#pragma unroll 1
        for (; done < 2 * NA3; ++done) ahalf();
        poll(gfl, lane < 5 ? lane : 0, tag);
      }
      bunit3(); bunit3(); bunit3();
      WAITX(TH_PA);
      peek(kfl, lane & 7, tag);
      bunit3(); bunit3();
      WAITV(41); LBAR(); WAITV(31); LBAR(); WAITV(20);
      await(kfl, lane & 7, tag);
      peek(kfl, 8 + (lane & 7), tag);
      LBAR(); WAITV(11); LBAR(); WAITV(0); LBAR();
      await(kfl, 8 + (lane & 7), tag);
      LBAR();
      peek(gfl, lane < 5 ? 5 + lane : 5, tag);
      WAITV(0);
      const bool ready = seen(tag);
      if (!ready) { LBAR(); poll(gfl, lane < 5 ? 5 + lane : 5, tag); }
      bunit3(); bunit3();
      if (ready) LBAR();
      bunit3(); bunit3();
#pragma unroll 1
      for (; done < 2 * NA3 + 4; ++done) ahalf();
      WAITV(16);
    }
    {
      auto bunit = bunit3;
      if (!roleA) {
        WAITX(30); LBAR();
        peek(gfl, lane < 5 ? 5 + lane : 5, tag);
        bunit();
        WAITX(30); LBAR(); ahalf();
        WAITX(25); LBAR();
        WAITV(14);
        await(gfl, lane < 5 ? 5 + lane : 5, tag);
        bunit(); bunit(); ahalf();
      }
#pragma unroll 1
      for (int r = roleA ? 5 : 3; r < 7; ++r) { LBAR(); bunit(); ahalf(); WAITX(42); }
      peek(gfl, lane < 6 ? 10 + lane : 10, tag);
#pragma unroll 1
      for (int r = 7; r < 11; ++r) { LBAR(); bunit(); ahalf(); WAITX(46); }
      await(gfl, lane < 6 ? 10 + lane : 10, tag);
#pragma unroll 1
      for (int r = 11; r < 18; ++r) { LBAR(); bunit(); ahalf(); if (r > H3) ahalf(); WAITX(46); }
#pragma unroll 1
      for (int r = 18; r < 20; ++r) { LBAR(); bunit(); ahalf(); ahalf(); WAITX(62); }
      peek<252>(ws + WS_OFLAG + st * 2048, c16, tag);
      peek(gfl, lane < 20 ? 16 + lane : 16, tag);
#pragma unroll 1
      for (int r = 20; r < 22; ++r) { LBAR(); bunit(); ahalf(); ahalf(); WAITX(62); }
      LBAR(); bunit(); WAITX(54);
      LBAR(); WAITX(36);
      await<252>(ws + WS_OFLAG + st * 2048, c16, tag); await(gfl, lane < 20 ? 16 + lane : 16, tag);
      const char* ap = ws + WS_O + st * 81920 + dt3 * 8192;
      bp = ws + WS_WO + dt3 * 112640;
#pragma unroll 1
      for (int r = 24; r < 34; ++r) {
        LBAR();
        glin8<8192, 0, L3_B>(ap, sa3, vl4);
        bunit();
        ap += 8192;
        if (r + dt3 == 33) { ap -= 81920; bp -= 102400; }
        WAITX(36);
      }
      LBAR(); WAITX(18); LBAR(); WAITX(0); LBAR();
    }
    asm volatile(".Lcode_exit_a:" ::: "memory");
    return;
  }

  const int hw = wave >> 1, ty = wave & 1;
  const __amdgpu_buffer_rsrc_t rs = __builtin_amdgcn_make_buffer_rsrc(ws, 0, WS_END, 0x00020000);
  u64 e0;
  {
    const int row = 2 * wave + (lane >> 5), hl = lane & 31;
    const GAS u32x2* xp = (const GAS u32x2*)(P.x + b * (8 * 1280) + (u32)(row * 1280 + hl * 8));
    u32x2 raw[5];
#pragma unroll
    for (int i = 0; i < 5; ++i) raw[i] = xp[32 * i];
    SB();
    e0 = EPOCH();
    u32 ra = L0 + L_LN + q4 * LN_PITCH + (10 * wave + g) * 32 + p4 * 8, wa = L0 + L_LN + row * LN_PITCH + hl * 8;
    int go = WS_H + (b >> 2) * 40960 + (10 * wave + g) * 1024 + (b & 3) * 256 + c16 * 16, g1 = go + 4096, g2 = g < 2 ? go + 8192 : OOB;
    int gs = hl == 0 ? WS_SC + (8 * b + row) * 4 : OOB;
    *(LAS u32*)LCNT = 0;
    asm volatile("" : "+v"(ra), "+v"(go), "+v"(g1), "+v"(g2), "+v"(gs), "+v"(wa), "+s"(e0));
    SB();
    f32x2 v[10];
#pragma unroll
    for (int i = 0; i < 5; ++i) { v[2 * i] = f32x2{blo(raw[i][0]), bhi(raw[i][0])}; v[2 * i + 1] = f32x2{blo(raw[i][1]), bhi(raw[i][1])}; }
    f32x2 s = v[0] + v[1], s2 = v[2] + v[3];
#pragma unroll
    for (int i = 4; i < 10; i += 2) { s += v[i]; s2 += v[i + 1]; }
    s += s2;
    const float m = sum32(s[0] + s[1]) * (1.f / 640.f);
#pragma unroll
    for (int i = 0; i < 10; ++i) v[i] -= m;
    f32x2 qq = v[0] * v[0], q2 = v[1] * v[1];
#pragma unroll
    for (int i = 2; i < 10; i += 2) { qq += v[i] * v[i]; q2 += v[i + 1] * v[i + 1]; }
    qq += q2;
    const float var = sum32(qq[0] + qq[1]) * (1.f / 640.f);
    const float r1 = __builtin_amdgcn_rsqf(var + 1e-5f), v2 = var * r1 * r1 + 1e-5f, r2 = __builtin_amdgcn_rsqf(v2), r12 = r1 * r2;
#pragma unroll
    for (int i = 0; i < 5; ++i) {
      const f32x2 a = v[2 * i] * r12, c = v[2 * i + 1] * r12;
      *(LAS u32x2*)(wa + i * 256) = u32x2{pk2(a[0], a[1]), pk2(c[0], c[1])};
    }
    __builtin_amdgcn_raw_buffer_store_b32(__float_as_uint(v2 * r2), rs, gs, 0, 16);
    BAR();
    s16x8 f[3];
#pragma unroll
    for (int i = 0; i < 3; ++i) f[i] = cat(ldtr(ra + i * 128), ldtr(ra + i * 128 + 4 * LN_PITCH));
    st128(rs, go, f[0]); st128(rs, g1, f[1]); st128(rs, g2, f[2]);
    BAR();
    publish_last(ws + WS_LNFLAG + b * 128, mix(e0), lane, LCNT, 4);
    BAR();
  }
  const u64 tag = mix(e0);
  ROLES();
  const u32 xr = (st * 64 + wave * 16 + c16) * 1280 + dt3 * 160, xo = xr + (g & 1) * 32 + (g >> 1) * 16;
  u32x4 x01, x23; u32x2 x4; u32 bs;
  auto ldx = [&]() {
    x01 = *(const GAS u32x4*)(P.x + xo); x23 = *(const GAS u32x4*)(P.x + xo + 64);
    x4 = *(const GAS u32x2*)(P.x + (xr + 128 + g * 8));
    bs = *(const GAS unsigned short*)(P.b2 + st * 128 + wave * 32 + (u32)(c16 * 2));
  };
  f32x4 oc[2][5];
  const u32 a30 = L0 + (32 * ty + 4 * g + q4) * 128 + (wave ^ ((2 * g + (q4 >> 1)) & 3)) * 32 + p4 * 8;
  const u32 ga0 = L0 + L3_B + ty * 5120 + lane * 16;
  const u32 oa0 = L0 + ty * 4096 + hw * 1024 + lane * 16 + ty * 8;
  u32 sa3, sb3, na3, nb3, ng3;
  struct FA { s16x8 a[2], g[5]; };
  auto lda = [&](FA& f, bool tr) {
    const u32 aa = na3, ab = nb3, ga = ng3;
    BAR();
    if (tr) { f.a[1] = cat(ldtr(ab), ldtr(ab + 2048)); f.a[0] = cat(ldtr(aa), ldtr(aa + 2048)); }
    else { f.a[1] = cat(ld64(ab), ld64(ab + 2048)); f.a[0] = cat(ld64(aa), ld64(aa + 2048)); }
#pragma unroll
    for (int j = 0; j < 5; ++j) f.g[j] = ld128(ga + j * 1024);
    sa3 = sa3 == L3_B - 8192 ? 0 : sa3 + 8192;
    sb3 = sb3 == LA3 * 10240 ? 0 : sb3 + 10240;
    na3 = (tr ? a30 : oa0) + sa3; nb3 = na3 ^ (tr ? 32 : 8); ng3 = ga0 + sb3;
  };
  auto mma = [&](const FA& f) {
#pragma unroll
    for (int j = 0; j < 5; ++j) oc[1][j] = mfma32(f.g[j], f.a[1], oc[1][j]);
#pragma unroll
    for (int j = 0; j < 5; ++j) oc[0][j] = mfma32(f.g[j], f.a[0], oc[0][j]);
  };
  auto pat = [&]() {
    SG(0x100, 1);
#pragma unroll
    for (int j = 0; j < 4; ++j) { SG(0x8, 1); SG(0x100, 2); }
#pragma unroll
    for (int j = 0; j < 5; ++j) { SG(0x8, 1); SG(0x6, 2); }
    SG(0x8, 1);
    asm volatile("" : "+v"(na3), "+v"(nb3), "+v"(ng3));
    SB();
  };
  FA fa0, fa1;
  auto begin3 = [&]() {
#pragma unroll
    for (int j = 0; j < 5; ++j) oc[0][j] = oc[1][j] = f32x4{0.f, 0.f, 0.f, 0.f};
    sa3 = sb3 = 0; na3 = a30; nb3 = a30 ^ 32; ng3 = ga0;
    lda(fa0, true); SB();
  };
  auto rounds = [&](int r0, int r1, bool tr) {
#pragma unroll 1
    for (int r = r0; r < r1; r += 2) { lda(fa1, tr); mma(fa0); pat(); lda(fa0, tr); mma(fa1); pat(); }
  };
  if (roleA) {
    f32x4 aq[5][2] = {}, akv[5][4] = {};
    const u32 wa0 = L0 + (4 * g + q4) * 480 + p4 * 8;
    const u32 xa0 = L0 + LQ_B + wave * 2048 + (p4 >> 1) * 256 + ((4 * g + q4) ^ ((p4 >> 1) << 3)) * 16 + (p4 & 1) * 8;
    const int c32 = lane & 31, hh = lane >> 5;
    const int pb = WS_PART + (hg * 4 + wave) * (16 * 896);
    u32 vp0 = L0 + L_KV + (8 * hw + 4 * ty + g) * LN_PITCH + c16 * 16 + ty * 768, vp1 = vp0 + (1 - 2 * ty) * (4 * LN_PITCH);
    u32 ka = L0 + L_KV + hh * LN_PITCH + (20 * wave + (c32 < 20 ? c32 : 19)) * 16;
    u32 vb = L0 + L_KV + (16 + hh) * LN_PITCH + (c32 < 20 ? 20 * wave + c32 : 80) * 16;
    int so0 = c32 <= 20 ? pb + rt * 896 + (hh * 21 + c32) * 16 : OOB, so1 = lane <= 21 ? pb + rt * 896 + 672 + lane * 8 : OOB;
    u32 vp2 = vp0 - ty * 768 + 512, von = L0 + L_KV + (16 + 4 * wave + g) * LN_PITCH + 1280;
    asm volatile("" : "+v"(vp2), "+v"(von), "+v"(vp0), "+v"(vp1), "+v"(ka), "+v"(vb), "+v"(so0), "+v"(so1));
    u32 qa[5][3], za[5];
#pragma unroll
    for (int je = 0; je < 5; ++je) {
      const int e = 16 * je + c16, he = e / 20, el = e - 20 * he;
#pragma unroll
      for (int x = 0; x < 3; ++x) {
        const int dl = 4 * (je - 1 + x) + g - 5 * he;
        const int qi = dl < 4 ? ((dl & 1) * 21 + el) * 2 + (dl >> 1) : 84 + el;
        qa[je][x] = L0 + L_FIN + (dl >= 0 && dl < 5 ? he * 848 + qi * 8 : 3392);
        if (je - 1 + x >= 0 && je - 1 + x < 5) asm volatile("" : "+v"(qa[je][x]));
      }
      const int dl = 4 * je + g - 5 * p4;
      const int qi = dl < 4 ? ((dl & 1) * 21 + 20) * 2 + (dl >> 1) : 104;
      za[je] = L0 + L_FIN + (dl >= 0 && dl < 5 ? p4 * 848 + qi * 8 : 3392);
      asm volatile("" : "+v"(za[je]));
    }
    u32x4 sel;
    {
      const int r = (c32 & 3) + 4 * (c32 >> 3);
      const u32 one = c32 < 16 && ((c32 >> 2) & 1) == hh ? 0x3f80u << (16 * (r & 1)) : 0u;
#pragma unroll
      for (int i = 0; i < 4; ++i) sel[i] = (r >> 1) == i ? one : 0u;
    }
    int go = lane < 53 ? pb + lane * 16 : OOB;
    u32 fa = L0 + L_FIN + (lane < 53 ? wave * 848 + lane * 16 : 3392);
    int ob = WS_O + (2 * rt + hw) * 81920 + hg * 10240 + ty * 1024 + lane * 16;
    asm volatile("" : "+v"(sel), "+v"(go), "+v"(fa), "+v"(ob));
    s16x8 xA[4] = {}, xB[4], wq[5] = {};
    u32 nwa = wa0, nwk = wa0 + ty * 96, nxa = xa0, nxb = xa0 ^ 2048;
    auto round = [&](int r, s16x8(&xo)[4], s16x8(&xn)[4]) {
      const u32 wa = nwa, wk = nwk, xa = nxa, xb = nxb;
      BAR();
      s16x8 w[6];
      xn[0] = cat(ldtr(xa), ldtr(xa + 1024)); w[0] = cat(ldtr(wk + 160), ldtr(wk + 160 + 7680));
      xn[1] = cat(ldtr(xa + 512), ldtr(xa + 1536)); xn[2] = cat(ldtr(xb), ldtr(xb + 1024)); xn[3] = cat(ldtr(xb + 512), ldtr(xb + 1536));
      w[1] = cat(ldtr(wk + 192), ldtr(wk + 192 + 7680));
#pragma unroll
      for (int j = 0; j < 5; ++j) { aq[j][0] = mfma32(wq[j], xo[0], aq[j][0]); aq[j][1] = mfma32(wq[j], xo[1], aq[j][1]); }
#pragma unroll
      for (int j = 0; j < 2; ++j) { SG(0x8, 1); SG(0x100, 2); }
#pragma unroll
      for (int j = 0; j < 8; ++j) { SG(0x8, 1); SG(0x100, 1); }
      SB();
      w[3] = cat(ldtr(wk + 320), ldtr(wk + 320 + 7680));
      w[4] = cat(ldtr(wk + 352), ldtr(wk + 352 + 7680));
      w[2] = cat(ldtr(wa + 224), ldtr(wa + 224 + 7680));
      w[5] = cat(ldtr(wa + 384), ldtr(wa + 384 + 7680));
#pragma unroll
      for (int j = 0; j < 5; ++j) wq[j] = cat(ldtr(wa + j * 32), ldtr(wa + 7680 + j * 32));
      nwa = wa0 + ((r + 1) & 7) * 15360; nwk = nwa + ty * 96; nxa = xa0 + ((r + 1) & 3) * 8192; nxb = nxa ^ 2048;
#pragma unroll
      for (int j = 0; j < 5; ++j) {
        if (j == 2) continue;
#pragma unroll
        for (int i = 0; i < 4; ++i) akv[j][i] = mfma32(xn[i], w[j], akv[j][i]);
      }
#pragma unroll
      for (int i = 0; i < 4; ++i) akv[2][i] = mfma32(xn[i & 1], w[i < 2 ? 2 : 5], akv[2][i]);
#pragma unroll
      for (int j = 0; j < 3; ++j) { SG(0x8, 1); SG(0x100, 2); }
#pragma unroll
      for (int j = 0; j < 12; ++j) { SG(0x8, 1); SG(0x100, 1); }
#pragma unroll
      for (int j = 0; j < 4; ++j) { SG(0x8, 1); SG(0x6, 2); }
      SG(0x8, 1);
      asm volatile("" : "+v"(nwa), "+v"(nwk), "+v"(nxa), "+v"(nxb));
      SB();
    };
#pragma unroll 1
    for (int r = 0; r < 20; r += 2) { round(r, xA, xB); round(r + 1, xB, xA); }
    f32x4 sk[4], sq[2];
#pragma unroll
    for (int i = 0; i < 4; ++i) sk[i] = *(const LAS f32x4*)(L0 + L_SC + ((wave ^ (i >> 1)) * 128 + (i & 1) * 64 + g * 16));
#pragma unroll
    for (int i = 0; i < 2; ++i) { const float t = *(const LAS float*)(L0 + L_SC + (wave * 128 + i * 64 + c16 * 4)); sq[i] = f32x4{t, t, t, t}; }
    BAR();
    {
      SB();
      *(LAS s16x8*)(vp2 + 16 * LN_PITCH) = cat(pk4(akv[2][2] * sk[0]), pk4(akv[2][3] * sk[1]));
      *(LAS s16x8*)(von) = s16x8{0x3f80, 0x3f80, 0x3f80, 0x3f80, 0x3f80, 0x3f80, 0x3f80, 0x3f80};
#pragma unroll
      for (int j = 3; j < 5; ++j) {
        *(LAS s16x8*)(vp0 + 16 * LN_PITCH + (j - 3) * 256) = cat(pk4(akv[j][0] * sk[0]), pk4(akv[j][1] * sk[1]));
        *(LAS s16x8*)(vp1 + 16 * LN_PITCH + (j - 3) * 256) = cat(pk4(akv[j][2] * sk[2]), pk4(akv[j][3] * sk[3]));
      }
      SB();
#pragma unroll
      for (int j = 0; j < 2; ++j) {
        s16x4 o[4];
        phi<4>(akv[j], sk, o);
        *(LAS s16x8*)(vp0 + j * 256) = cat(o[0], o[1]);
        *(LAS s16x8*)(vp1 + j * 256) = cat(o[2], o[3]);
        SB();
      }
      const f32x4 k2[2] = {akv[2][0], akv[2][1]};
      s16x4 o[2];
      phi<2>(k2, sk, o);
      *(LAS s16x8*)(vp2) = cat(o[0], o[1]);
    }
    BAR();
    {
      s16x8 fk[8], fv[8];
#pragma unroll
      for (int i = 0; i < 8; ++i) { fk[i] = ld128(ka + i * 2 * LN_PITCH); fv[i] = ld128(vb + i * 2 * LN_PITCH); }
      SB();
      f32x16 d = {};
#pragma unroll
      for (int i = 0; i < 8; ++i) d = mfmaw(fk[i], fv[i], d);
      SB();
#pragma unroll
      for (int j = 0; j < 2; ++j) { aq[j][0] = mfma32(wq[j], xA[0], aq[j][0]); aq[j][1] = mfma32(wq[j], xA[1], aq[j][1]); }
      SB();
      const u32x4 lo = {pk2(d[0], d[1]), pk2(d[2], d[3]), pk2(d[4], d[5]), pk2(d[6], d[7])};
      const u32x2 hi = {pk2(d[8], d[9]), pk2(d[10], d[11])};
      st128(rs, so0, __builtin_bit_cast(s16x8, lo));
      st64(rs, so1, __builtin_bit_cast(s16x4, hi));
      SB();
    }
    BAR();
#pragma unroll
    for (int j = 2; j < 5; ++j) { aq[j][0] = mfma32(wq[j], xA[0], aq[j][0]); aq[j][1] = mfma32(wq[j], xA[1], aq[j][1]); }
    SB();
    s16x4 pq[5][2];
#pragma unroll
    for (int j = 0; j < 2; ++j) { phi<2>(aq[j], sq, pq[j]); asm volatile("" : "+v"(pq[j][0]), "+v"(pq[j][1])); }
    publish_last(ws + WS_KVFLAG + hg * 2048 + rt * 128, tag, lane, LCNT, 8);
    ldx();
    SB();
    s16x8 p01[2], p23[2], p34[2];
#pragma unroll
    for (int j = 2; j < 5; ++j) phi<2>(aq[j], sq, pq[j]);
#pragma unroll
    for (int i = 0; i < 2; ++i) {
      p01[i] = cat(pq[0][i], pq[1][i]); p23[i] = cat(pq[2][i], pq[3][i]); p34[i] = cat(pq[3][i], pq[4][i]);
      asm volatile("" : "+v"(p01[i]), "+v"(p23[i]), "+v"(p34[i]));
    }
    begin3();
    rounds(1, 3, true);
    {
      f32x16 d = {};
      u32x4 pr[16];
#pragma unroll
      for (int i = 0; i < 8; ++i) pr[i] = __builtin_amdgcn_raw_buffer_load_b128(rs, go + (i & 3) * 896, (i >> 2) * 3584, 16);
      SB();
      rounds(3, 5, true);
      BAR();
#pragma unroll
      for (int i = 8; i < 16; ++i) pr[i] = __builtin_amdgcn_raw_buffer_load_b128(rs, go + (i & 3) * 896, (i >> 2) * 3584, 16);
      SB();
#pragma unroll
      for (int i = 0; i < 16; ++i) d = mfmaw(__builtin_bit_cast(s16x8, sel), __builtin_bit_cast(s16x8, pr[i]), d);
      *(LAS u32x4*)fa = u32x4{pk2(d[0], d[1]), pk2(d[2], d[3]), pk2(d[4], d[5]), pk2(d[6], d[7])};
    }
    BAR();
    {
      const u32 z0 = L0 + L_FIN + 3392;
      const s16x8 z01 = cat(ld64(za[0]), ld64(za[1])), z23 = cat(ld64(za[2]), ld64(za[3])), z4 = cat(ld64(z0), ld64(za[4]));
      const s16x8 k0 = cat(ld64(qa[0][1]), ld64(qa[0][2])), k1a = cat(ld64(qa[1][0]), ld64(qa[1][1])), k2a = cat(ld64(qa[2][1]), ld64(qa[2][2]));
      const s16x8 k3a = cat(ld64(qa[3][0]), ld64(qa[3][1])), k4 = cat(ld64(qa[4][0]), ld64(qa[4][1]));
      const s16x4 zz = {0, 0, 0, 0};
      const s16x8 k1b = cat(ld64(qa[1][2]), zz), k2b = cat(zz, ld64(qa[2][0])), k3b = cat(zz, ld64(qa[3][2]));
      SB();
      f32x4 den[2], o[5][2];
      const f32x4 e4 = {1e-6f, 1e-6f, 1e-6f, 1e-6f}, n4 = {0.f, 0.f, 0.f, 0.f};
#pragma unroll
      for (int i = 0; i < 2; ++i) den[i] = mfma32(z01, p01[i], e4);
#pragma unroll
      for (int i = 0; i < 2; ++i) den[i] = mfma32(z23, p23[i], den[i]);
#pragma unroll
      for (int i = 0; i < 2; ++i) den[i] = mfma32(z4, p34[i], den[i]);
#pragma unroll
      for (int i = 0; i < 2; ++i) { o[0][i] = mfma32(k0, p01[i], n4); o[1][i] = mfma32(k1a, p01[i], n4); }
      SB();
      float rc[5][2];
#pragma unroll
      for (int i = 0; i < 2; ++i) {
        const float r0 = __builtin_amdgcn_rcpf(den[i][0]), r1 = __builtin_amdgcn_rcpf(den[i][1]), r2 = __builtin_amdgcn_rcpf(den[i][2]), r3 = __builtin_amdgcn_rcpf(den[i][3]);
        rc[0][i] = r0; rc[1][i] = g < 1 ? r0 : r1; rc[2][i] = g < 2 ? r1 : r2; rc[3][i] = g < 3 ? r2 : r3; rc[4][i] = r3;
      }
#pragma unroll
      for (int i = 0; i < 2; ++i) { o[1][i] = mfma32(k1b, p23[i], o[1][i]); o[2][i] = mfma32(k2a, p23[i], n4); o[3][i] = mfma32(k3a, p23[i], n4); }
#pragma unroll
      for (int i = 0; i < 2; ++i) { o[2][i] = mfma32(k2b, p01[i], o[2][i]); o[3][i] = mfma32(k3b, p34[i], o[3][i]); o[4][i] = mfma32(k4, p34[i], n4); }
#pragma unroll
      for (int i = 0; i < 12; ++i) { SG(0x8, 1); SG(0x402, 1); }
      SB();
#pragma unroll
      for (int je = 0; je < 5; ++je) { st128(rs, ob + je * 2048, cat(pk4(o[je][0] * rc[je][0]), pk4(o[je][1] * rc[je][1]))); SB(); }
    }
  } else {
    if (narrow) {
      if (b == 192 && wave == 2) {
        const GAS u64* fp = (const GAS u64*)(ws + WS_LNFLAG + (u32)(lane * 128));
#pragma unroll 1
        for (u32 sp = 0; sp < SPIN_CAP; ++sp) {
          const u64 f0 = __hip_atomic_load(fp, RLX_AGENT), f1 = __hip_atomic_load(fp + 1024, RLX_AGENT);
          const u64 f2 = __hip_atomic_load(fp + 2048, RLX_AGENT), f3 = __hip_atomic_load(fp + 3072, RLX_AGENT);
          if (__all(f0 == tag && f1 == tag && f2 == tag && f3 == tag)) break;
        }
        asm volatile("" ::: "memory");
        if (lane < 2) __hip_atomic_store((GAS u64*)(ws + (lane ? WS_EPOCH : WS_ALLFLAG)), tag, RLX_AGENT);
      }
      f32x4 acc[5];
      {
        const u32x2 bb = *(const GAS u32x2*)(P.b1 + fu * 128 + hw * 64 + ty * 16 + (u32)((g & 1) * 32 + (g >> 1) * 8));
#pragma unroll
        for (int j = 0; j < 5; ++j) acc[j] = f32x4{blo(bb[0]), bhi(bb[0]), blo(bb[1]), bhi(bb[1])};
      }
      const u32 aa0 = L0 + (8 * g + q4) * 128 + ((2 * hw + (p4 & 1)) ^ ((q4 >> 1) | ((g & 1) << 1))) * 32 + ty * 16 + (p4 >> 1) * 8;
      const u32 ha0 = L0 + L1_B + lane * 16;
      u32 as = 10 * 8192, hs = 0, naa = aa0 + as, nha = ha0;
      struct FN { s16x8 a[2], h[10]; };
      auto step = [&](FN& n, const FN& o) {
        const u32 aa = naa, ha = nha;
        BAR();
        n.a[0] = cat(ldtr(aa), ldtr(aa + 512)); n.a[1] = cat(ldtr(aa + 4096), ldtr(aa + 4608));
#pragma unroll
        for (int j = 0; j < 10; ++j) n.h[j] = ld128(ha + j * 1024);
        as = as == L1_B - 8192 ? 0 : as + 8192;
        hs = hs == (NB1 - 1) * 10240 ? 0 : hs + 10240;
        naa = aa0 + as; nha = ha0 + hs;
#pragma unroll
        for (int j = 0; j < 10; ++j) acc[j % 5] = mfma32(o.a[j / 5], o.h[j], acc[j % 5]);
        SG(0x100, 2);
#pragma unroll
        for (int j = 0; j < 6; ++j) { SG(0x8, 1); SG(0x100, 2); }
#pragma unroll
        for (int j = 0; j < 4; ++j) { SG(0x8, 1); SG(0x6, 2); }
        asm volatile("" : "+v"(naa), "+v"(nha));
        SB();
      };
      FN f0 = {}, f1;
#pragma unroll 1
      for (int r = 0; r < 32; r += 2) { step(f1, f0); step(f0, f1); }
      BAR();
#pragma unroll
      for (int j = 0; j < 10; ++j) acc[j % 5] = mfma32(f0.a[j / 5], f0.h[j], acc[j % 5]);
      const int go = WS_G + dt * 276480 + (fu * 2 + hw) * 5120 + (g & 1) * 1024 + (2 * ty + (g >> 1)) * 256 + c16 * 16;
#pragma unroll
      for (int j = 0; j < 5; j += 2) {
        const u32x4 o = swap16(__builtin_bit_cast(u32x4, gelu8(acc[j], acc[j < 4 ? j + 1 : j])));
        st128(rs, j < 4 || !(g & 1) ? go + j * 1024 : OOB, __builtin_bit_cast(s16x8, o));
      }
      BAR();
      const u32 ra = L0 + (4 * g + q4) * 640 + wave * 160 + p4 * 8;
      s16x8 f[5];
#pragma unroll
      for (int j = 0; j < 5; ++j) f[j] = cat(ldtr(ra + j * 32), ldtr(ra + j * 32 + 10240));
      publish_last(ws + WS_GFLAG + dt * 8192 + gi * 128, tag, lane, LCNT, 8);
      BAR();
      const int gw = WS_WO + (4 * (dt & 1) + wave) * 102400 + (xc * 4 + (dt >> 1)) * 5120 + lane * 16;
#pragma unroll
      for (int j = 0; j < 5; ++j) st128(rs, gw + j * 1024, f[j]);
      SB();
    } else {
      f32x4 acc[2][5];
#pragma unroll
      for (int t = 0; t < 2; ++t) {
        const u32x2 bb = *(const GAS u32x2*)(P.b1 + fu * 128 + wave * 64 + (u32)(t * 32 + g * 8));
        const f32x4 bi = {blo(bb[0]), bhi(bb[0]), blo(bb[1]), bhi(bb[1])};
#pragma unroll
        for (int j = 0; j < 5; ++j) acc[t][j] = bi;
      }
      s16x8 a1[2] = {}, h1[5] = {};
      constexpr int RP = 256;
      const u32 aa0 = L0 + (8 * g + q4) * RP + ((2 * wave) ^ (q4 | ((g & 1) << 2))) * 32 + p4 * 8;
      const u32 ha0 = L0 + L1_B + lane * 16;
      u32 as = 6 * 8192, hs = 0, naa = aa0 + as, nab = naa ^ 32, nha = ha0;
#pragma unroll 1
      for (int r = 0; r < 32; ++r) {
        const u32 aa = naa, ab = nab, ha = nha;
        BAR();
        s16x8 a0[2], h0[5];
        a0[0] = cat(ldtr(aa), ldtr(aa + 4 * RP)); h0[0] = ld128(ha); a0[1] = cat(ldtr(ab), ldtr(ab + 4 * RP));
#pragma unroll
        for (int j = 1; j < 5; ++j) h0[j] = ld128(ha + j * 1024);
#pragma unroll
        for (int j = 0; j < 5; ++j) { acc[0][j] = mfma32(a1[0], h1[j], acc[0][j]); acc[1][j] = mfma32(a1[1], h1[j], acc[1][j]); }
#pragma unroll
        for (int j = 0; j < 9; ++j) { SG(0x8, 1); SG(0x100, 1); }
        SG(0x8, 1);
        SB();
        a1[0] = cat(ldtr(aa + 32 * RP), ldtr(aa + 36 * RP)); a1[1] = cat(ldtr(ab + 32 * RP), ldtr(ab + 36 * RP));
#pragma unroll
        for (int j = 0; j < 5; ++j) h1[j] = ld128(ha + 5120 + j * 1024);
        as = as == L1_B - 64 * RP ? 0 : as + 64 * RP;
        hs = hs == (NB1 - 1) * 10240 ? 0 : hs + 10240;
        naa = aa0 + as; nab = naa ^ 32; nha = ha0 + hs;
#pragma unroll
        for (int j = 0; j < 5; ++j) { acc[0][j] = mfma32(a0[0], h0[j], acc[0][j]); acc[1][j] = mfma32(a0[1], h0[j], acc[1][j]); }
        SG(0x100, 1);
#pragma unroll
        for (int j = 0; j < 4; ++j) { SG(0x8, 1); SG(0x100, 2); }
#pragma unroll
        for (int j = 0; j < 5; ++j) { SG(0x8, 1); SG(0x6, 2); }
        SG(0x8, 1);
        asm volatile("" : "+v"(naa), "+v"(nab), "+v"(nha));
        SB();
      }
      BAR();
#pragma unroll
      for (int j = 0; j < 5; ++j) { acc[0][j] = mfma32(a1[0], h1[j], acc[0][j]); acc[1][j] = mfma32(a1[1], h1[j], acc[1][j]); }
      const int go = WS_G + dt * 276480 + (fu * 2 + wave) * 5120 + lane * 16;
#pragma unroll
      for (int j = 0; j < 5; ++j) st128(rs, go + j * 1024, gelu8(acc[0][j], acc[1][j]));
    }
    if (!late && !narrow) {
      publish_last(ws + WS_GFLAG + dt * 8192 + gi * 128, tag, lane, LCNT, 8);
      BAR();
    }
    ldx();
    begin3();
  }

  {
    rounds(roleA ? 5 : 1, roleA ? R_JOIN : PUBR, true);
    {
      asm volatile("" : "+v"(x01), "+v"(x23), "+v"(x4), "+v"(bs));
      const float bf = blo(bs);
      x01 = swap16(x01); x23 = swap16(x23);
      oc[0][0] += f32x4{blo(x01[0]), bhi(x01[0]), blo(x01[1]), bhi(x01[1])} + bf;
      oc[0][1] += f32x4{blo(x01[2]), bhi(x01[2]), blo(x01[3]), bhi(x01[3])} + bf;
      oc[0][2] += f32x4{blo(x23[0]), bhi(x23[0]), blo(x23[1]), bhi(x23[1])} + bf;
      oc[0][3] += f32x4{blo(x23[2]), bhi(x23[2]), blo(x23[3]), bhi(x23[3])} + bf;
      oc[0][4] += f32x4{blo(x4[0]), bhi(x4[0]), blo(x4[1]), bhi(x4[1])} + bf;
      if (late) publish_last(ws + WS_GFLAG + dt * 8192 + gi * 128, tag, lane, LCNT, 8);
      else if (roleA || narrow)
        publish(ws + (roleA ? WS_OFLAG + (2 * rt + hw) * 2048 + (ty * 8 + hg) * 128 : WS_GFLAG + (4 * (dt & 1) + wave) * 8192 + (16 + xc * 4 + (dt >> 1)) * 128), tag, lane);
      SB();
    }
    rounds(roleA ? R_JOIN : PUBR, 27, true);
    na3 = oa0 + sa3; nb3 = na3 ^ 8;
    static_assert(36 % NB3 < 2, "");
    u32 ew = L0 + L3_B + 20480 + wave * 5120 + lane * 16, er = L0 + L3_B + 20480 + (wave ^ 1) * 5120 + lane * 16, xo4 = xr + 128 + g * 8;
    asm volatile("" : "+v"(ew), "+v"(er), "+v"(xo4));
    rounds(27, 37, false);
    mma(fa0);
#pragma unroll
    for (int j = 0; j < 5; ++j) *(LAS f32x4*)(ew + j * 1024) = oc[1][j];
    SG(0x8, 6);
#pragma unroll
    for (int j = 0; j < 4; ++j) { SG(0x200, 1); SG(0x8, 1); }
    SG(0x200, 1);
    SB();
    BAR();
#pragma unroll
    for (int j = 0; j < 5; ++j) oc[1][j] = *(const LAS f32x4*)(er + j * 1024);
    SB();
#pragma unroll
    for (int j = 0; j < 5; ++j) oc[0][j] += oc[1][j];
    *(GAS u32x4*)(P.out + xo) = swap16(__builtin_bit_cast(u32x4, cat(pk4(oc[0][0]), pk4(oc[0][1]))));
    *(GAS u32x4*)(P.out + xo + 64) = swap16(__builtin_bit_cast(u32x4, cat(pk4(oc[0][2]), pk4(oc[0][3]))));
    *(GAS s16x4*)(P.out + xo4) = pk4(oc[0][4]);
    asm volatile(".Lcode_exit_b:" ::: "memory");
  }
}

extern "C" void kernel_launch(void* const* d_in, const int* in_sizes, int n_in, void* d_out, int out_size, void* d_ws,
                              size_t ws_size, hipStream_t stream) {
  Params p;
  p.x = (const char*)d_in[0];
  p.wqkv = (const char*)d_in[1];
  p.wo = (const char*)d_in[2];
  p.w1 = (const char*)d_in[4];
  p.b1 = (const char*)d_in[5];
  p.w2 = (const char*)d_in[6];
  p.b2 = (const char*)d_in[7];
  p.out = (char*)d_out;
  p.ws = (char*)d_ws;
  (void)hipFuncSetAttribute((const void*)fused_fwd, hipFuncAttributeMaxDynamicSharedMemorySize, L_BYTES);
  fused_fwd<<<256, 320, L_BYTES, stream>>>(p);
}
```

```cpp
#define STAMP
#define SGN -1
#include <hip/hip_runtime.h>
#include <hip/hip_bf16.h>
#pragma clang diagnostic ignored "-Wint-to-pointer-cast"

#define GAS __attribute__((address_space(1)))
#define LAS __attribute__((address_space(3)))
#define DI __device__ __forceinline__
typedef unsigned u32;
typedef unsigned long long u64;
typedef short s16x4 __attribute__((ext_vector_type(4)));
typedef short s16x8 __attribute__((ext_vector_type(8)));
typedef __bf16 b16x2 __attribute__((ext_vector_type(2)));
typedef __bf16 b16x8 __attribute__((ext_vector_type(8)));
typedef float f32x2 __attribute__((ext_vector_type(2)));
typedef float f32x4 __attribute__((ext_vector_type(4)));
typedef float f32x16 __attribute__((ext_vector_type(16)));
typedef unsigned u32x2 __attribute__((ext_vector_type(2)));
typedef unsigned u32x4 __attribute__((ext_vector_type(4)));

constexpr int WS_EPOCH = 0;
constexpr int WS_ALLFLAG = 1024;
constexpr int WS_LNFLAG = 196608;
constexpr int WS_GFLAG = 720896;
constexpr int WS_KVFLAG = 65536;
constexpr int WS_OFLAG = 131072;
constexpr int WS_PART = 262144;
constexpr int WS_SC = 8192;
constexpr int WS_H = 786432;
constexpr int WS_G = WS_H + 2621440;
constexpr int WS_O = WS_G + 2211840;
constexpr int WS_WO = WS_O + 2621440;
constexpr int WS_END = WS_WO + 819200;
constexpr int OOB = 0x40000000;
constexpr int L_BYTES = 163840;
constexpr int L_LN = 131072, LN_PITCH = 1296;
constexpr int NB3 = 5, NB1 = 4, NA1 = 7;
constexpr int LA3 = NB3 - 1, NA3 = 18 - NB3, H3 = 2 * (NA3 - LA3 - 2);
constexpr int LQ_B = 122880, L1_B = NA1 * 16384, L3_B = NA3 * 8192;
constexpr int L_KV = L3_B, L_FIN = L3_B + NB3 * 10240, L_SC = L_FIN + 3584;
constexpr int a3(int t) { return t < 1 || t > 35 - LA3 ? 0 : t <= H3 ? 4 : 8; }
constexpr int w3(int r) { int n = a3(r - LA3); for (int t = r - LA3 + 1; t < r; ++t) n += a3(t) + (t <= 36 - LA3 ? 10 : 0); return n; }
#define APRE 20
#define NPK_A 8
#define R_JOIN 9
#define LPK 22
#define EARLY 16
#define PUBR 3
#define TH_PA 10
#define TH_P3 8
#define SLP 2
#define MID_A 2
#define PRE_B 3
#define MID_B 3
constexpr int PRE_A = 2;
constexpr u32 SPIN_CAP = 1u << 16;

#define BAR() do { asm volatile("" ::: "memory"); __builtin_amdgcn_s_waitcnt(0xC07F); asm volatile("s_barrier" ::: "memory"); } while (0)
#define WAITV(n) asm volatile("s_waitcnt vmcnt(" #n ")" ::: "memory")
#define WAITX(n) asm volatile("s_waitcnt vmcnt(%0)" ::"i"((n) > 63 ? 63 : (n)) : "memory")
constexpr int NSTAMP = 32, WS_STAMP = 32 << 20;
#define STAMP_AT(k) do { u64 t_; asm volatile("s_memrealtime %0\n\ts_waitcnt lgkmcnt(0)" : "=s"(t_) :: "memory"); stamps[k] = (u32)t_; } while (0)
#define STAMP_DECL u32 stamps[16] = {}; u32 tl_ = 0
#define ACC_TO(k) do { u64 t_; asm volatile("s_memtime %0\n\ts_waitcnt lgkmcnt(0)" : "=s"(t_) :: "memory"); if (k >= 0) stamps[k >= 0 ? k : 0] += (u32)t_ - tl_; tl_ = (u32)t_; } while (0)
#define STAMP_OUT() do { if (lane == 0) { for (int k_ = 0; k_ < 16; ++k_) ((u64*)(ws + WS_STAMP))[(b * 8 + wave) * NSTAMP + k_] = stamps[k_]; } } while (0)
#define ACC0(k) ACC_TO(k)
#define ACC1(k)
#define ACC3(k)
#define LW1(n) do { ACC1(13); WAITX(n); ACC1(11); } while (0)
#define LBAR() asm volatile("s_barrier" ::: "memory")
#define LB1() do { LBAR(); ACC1(12); } while (0)
#define LW3(n) do { ACC3(13); WAITX(n); ACC3(11); } while (0)
#define LB3() do { LBAR(); ACC3(12); } while (0)
#define JIT(k)
#define RLX_AGENT __ATOMIC_RELAXED, __HIP_MEMORY_SCOPE_AGENT

DI f32x4 mfma32(s16x8 a, s16x8 b, f32x4 c) {
  return __builtin_amdgcn_mfma_f32_16x16x32_bf16(__builtin_bit_cast(b16x8, a), __builtin_bit_cast(b16x8, b), c, 0, 0, 0);
}
DI f32x16 mfmaw(s16x8 a, s16x8 b, f32x16 c) {
  return __builtin_amdgcn_mfma_f32_32x32x16_bf16(__builtin_bit_cast(b16x8, a), __builtin_bit_cast(b16x8, b), c, 0, 0, 0);
}
DI s16x4 ldtr(u32 a) { return __builtin_amdgcn_ds_read_tr16_b64_v4i16((LAS s16x4*)a); }
DI s16x8 ld128(u32 a) { return *(const LAS s16x8*)a; }
DI s16x4 ld64(u32 a) { return *(const LAS s16x4*)a; }
DI s16x8 cat(s16x4 a, s16x4 b) { return __builtin_shufflevector(a, b, 0, 1, 2, 3, 4, 5, 6, 7); }
DI u32 pk2(float a, float b) { f32x2 v = {a, b}; return __builtin_bit_cast(u32, __builtin_convertvector(v, b16x2)); }
DI s16x4 pk4(f32x4 v) { u32x2 r = {pk2(v[0], v[1]), pk2(v[2], v[3])}; return __builtin_bit_cast(s16x4, r); }
DI float blo(u32 u) { return __uint_as_float(u << 16); }
DI float bhi(u32 u) { return __uint_as_float(u & 0xffff0000u); }
template <int C> DI float dpp_add(float s) { return s + __int_as_float(__builtin_amdgcn_update_dpp(0, __float_as_int(s), C, 0xf, 0xf, true)); }
DI float sum32(float s) {
  s = dpp_add<0x121>(s); s = dpp_add<0x122>(s); s = dpp_add<0x124>(s); s = dpp_add<0x128>(s);
  auto r = __builtin_amdgcn_permlane16_swap(__float_as_uint(s), __float_as_uint(s), false, false);
  return __uint_as_float(r[0]) + __uint_as_float(r[1]);
}
#define SG(m, n) __builtin_amdgcn_sched_group_barrier(m, n, 0)
#define SB() __builtin_amdgcn_sched_barrier(0)
#define AL() do { asm volatile(".p2align 3"); SB(); } while (0)
template <int N> DI void phi(const f32x4 (&u)[N], const f32x4* sc, s16x4 (&o)[N]) {
  const float L = 1.4426950408889634f;
  f32x4 a[N], y[N];
  SB(); AL();
#pragma unroll
  for (int i = 0; i < N; ++i) a[i] = __builtin_elementwise_fma(u[i], sc[i], f32x4{1.f, 1.f, 1.f, 1.f});
  SB();
#pragma unroll
  for (int i = 0; i < N; ++i) y[i] = __builtin_elementwise_fma(a[i], f32x4{L, L, L, L}, f32x4{-L, -L, -L, -L});
  SB();
#pragma unroll
  for (int i = 0; i < 4 * N; ++i) y[i >> 2][i & 3] = __builtin_amdgcn_fmed3f(__builtin_amdgcn_exp2f(y[i >> 2][i & 3]), 0.f, 1.f);
  SB();
#pragma unroll
  for (int i = 0; i < 4 * N; ++i) y[i >> 2][i & 3] = __int_as_float(max(__float_as_int(a[i >> 2][i & 3]), __float_as_int(y[i >> 2][i & 3])));
  SB();
#pragma unroll
  for (int i = 0; i < N; ++i) y[i] += 1e-6f;
  SB();
#pragma unroll
  for (int i = 0; i < N; ++i) o[i] = pk4(y[i]);
}
DI s16x8 gelu8(f32x4 u0, f32x4 u1) {
  const float K = -2.f * 0.7978845608028654f * 1.4426950408889634f, K3 = K * 0.044715f;
  SB(); AL();
  f32x4 y0 = u0 * u0, y1 = u1 * u1;
  SB();
  y0 = __builtin_elementwise_fma(y0, f32x4{K3, K3, K3, K3}, f32x4{K, K, K, K});
  y1 = __builtin_elementwise_fma(y1, f32x4{K3, K3, K3, K3}, f32x4{K, K, K, K});
  SB();
  y0 *= u0; y1 *= u1;
  SB();
#pragma unroll
  for (int i = 0; i < 4; ++i) y0[i] = __builtin_amdgcn_exp2f(y0[i]);
#pragma unroll
  for (int i = 0; i < 4; ++i) y1[i] = __builtin_amdgcn_exp2f(y1[i]);
  SB();
  y0 += 1.f; y1 += 1.f;
  SB();
#pragma unroll
  for (int i = 0; i < 4; ++i) y0[i] = __builtin_amdgcn_rcpf(y0[i]);
#pragma unroll
  for (int i = 0; i < 4; ++i) y1[i] = __builtin_amdgcn_rcpf(y1[i]);
  SB();
  y0 *= u0; y1 *= u1;
  SB();
  return cat(pk4(y0), pk4(y1));
}
DI void st128(__amdgpu_buffer_rsrc_t rs, int off, s16x8 v) { __builtin_amdgcn_raw_buffer_store_b128(__builtin_bit_cast(u32x4, v), rs, off, 0, 16); }
DI void st64(__amdgpu_buffer_rsrc_t rs, int off, s16x4 v) { __builtin_amdgcn_raw_buffer_store_b64(__builtin_bit_cast(u32x2, v), rs, off, 0, 16); }
DI u64 mix(u64 e) {
  return (u64)((u32)(e >> 32) * 0x85EBCA6Bu + 0x165667B1u) << 32 | (u32)((u32)e * 0x9E3779B1u + 0x7F4A7C15u);
}

#define GL(v, off, sc) "global_load_lds_dwordx4 " v ", %0 offset:" #off sc "\n\t"
#define M0(bias) "s_add_u32 m0, %1, " #bias "\n\ts_nop 0\n\t"
#define RING m = m == HI - STEP ? LO : m + STEP
template <int STEP, int LO, int HI> DI void gl4(const char* base, u32& m, const u32 (&v)[4]) {
  asm volatile(M0(0) GL("%2", 0, "") GL("%3", 1024, "") GL("%4", 2048, "") GL("%5", 3072, "")
               ::"s"(base), "s"(m), "v"(v[0]), "v"(v[1]), "v"(v[2]), "v"(v[3]) : "memory", "scc");
  RING;
}
template <int STEP, int LO, int HI> DI void gl8(const char* base, u32& m, const u32 (&v)[8]) {
  asm volatile(M0(4096) GL("%2", -4096, "") GL("%3", -3072, "") GL("%4", -2048, "") GL("%5", -1024, "")
               GL("%6", 0, "") GL("%7", 1024, "") GL("%8", 2048, "") GL("%9", 3072, "")
               ::"s"(base), "s"(m), "v"(v[0]), "v"(v[1]), "v"(v[2]), "v"(v[3]), "v"(v[4]), "v"(v[5]), "v"(v[6]), "v"(v[7]) : "memory", "scc");
  RING;
}
template <int STEP, int LO, int HI> DI void gl8w(const char* base, u32& m, const u32 (&v)[8]) {
  asm volatile("s_add_u32 m0, %1, 3840\n\ts_mov_b32 exec_hi, 0x0fffffff\n\t" GL("%2", -3840, "") GL("%3", -2880, "") GL("%4", -1920, "") GL("%5", -960, "")
               GL("%6", 0, "") GL("%7", 960, "") GL("%8", 1920, "") GL("%9", 2880, "") "s_mov_b32 exec_hi, -1"
               ::"s"(base), "s"(m), "v"(v[0]), "v"(v[1]), "v"(v[2]), "v"(v[3]), "v"(v[4]), "v"(v[5]), "v"(v[6]), "v"(v[7]) : "memory", "scc");
  RING;
}
DI void gl5(const char* base, u32 m, const u32 (&v)[5]) {
  asm volatile(M0(4096) GL("%2", -4096, "") GL("%3", -3072, "") GL("%4", -2048, "") GL("%5", -1024, "") GL("%6", 0, "")
               ::"s"(base), "s"(m), "v"(v[0]), "v"(v[1]), "v"(v[2]), "v"(v[3]), "v"(v[4]) : "memory", "scc");
}
#define GL8S M0(4096) GL("%2", -4096, " sc1") GL("%2", -3072, " sc1") GL("%2", -2048, " sc1") GL("%2", -1024, " sc1") \
             GL("%2", 0, " sc1") GL("%2", 1024, " sc1") GL("%2", 2048, " sc1") GL("%2", 3072, " sc1")
template <int STEP, int LO, int HI> DI void glin8(const char* base, u32& m, u32 vl4) {
  asm volatile(GL8S ::"s"(base), "s"(m), "v"(vl4) : "memory", "scc");
  RING;
}
template <int STEP, int LO, int HI> DI void glin10(const char* base, u32& m, u32 vl4, u32 vl8) {
  asm volatile(GL8S M0(8192) GL("%3", 0, " sc1") GL("%3", 1024, " sc1")
               ::"s"(base), "s"(m), "v"(vl4), "v"(vl8) : "memory", "scc");
  RING;
}
template <int STEP, int LO, int HI> DI void glin55(const char* base, u32& m, u32 vl4, u32 vl4b) {
  asm volatile(M0(4096) GL("%2", -4096, " sc1") GL("%2", -3072, " sc1") GL("%2", -2048, " sc1") GL("%2", -1024, " sc1") GL("%2", 0, " sc1")
               M0(9216) GL("%3", -4096, " sc1") GL("%3", -3072, " sc1") GL("%3", -2048, " sc1") GL("%3", -1024, " sc1") GL("%3", 0, " sc1")
               ::"s"(base), "s"(m), "v"(vl4), "v"(vl4b) : "memory", "scc");
  RING;
}
template <int STEP, int LO, int HI> DI void glin2222(const char* base, u32& m, const u32 (&v)[4]) {
  asm volatile(M0(4096) GL("%2", -4096, " sc1") GL("%2", -3072, " sc1") GL("%3", -2048, " sc1") GL("%3", -1024, " sc1")
               GL("%4", 0, " sc1") GL("%4", 1024, " sc1") GL("%5", 2048, " sc1") GL("%5", 3072, " sc1")
               ::"s"(base), "s"(m), "v"(v[0]), "v"(v[1]), "v"(v[2]), "v"(v[3]) : "memory", "scc");
  RING;
}
DI void glin1(const char* base, u32 m, u32 v) { asm volatile(M0(0) GL("%2", 0, " sc1")::"s"(base), "s"(m), "v"(v) : "memory", "scc"); }
DI u32x4 swap16(u32x4 t) {
  const auto s0 = __builtin_amdgcn_permlane16_swap(t[0], t[2], false, false), s1 = __builtin_amdgcn_permlane16_swap(t[1], t[3], false, false);
  return u32x4{s0[0], s1[0], s0[1], s1[1]};
}
DI void touch(const char* base, u32 m, u32 v) {
  asm volatile("s_mov_b32 m0, %1\n\ts_nop 0\n\tglobal_load_lds_dword %2, %0" ::"s"(base), "s"(m), "v"(v) : "memory");
}
DI void poll(const char* f, int idx, u64 tag) {
  const GAS u64* p = (const GAS u64*)(f + (u32)(idx * 128));
#pragma unroll 1
  for (u32 sp = 0; sp < SPIN_CAP; ++sp) {
    const u64 v = __hip_atomic_load(p, RLX_AGENT);
    if (__all(v == tag)) break;
    __builtin_amdgcn_s_sleep(SLP);
  }
  asm volatile("" ::: "memory");
}
template <int R = 254> DI void peek(const char* f, int idx, u64 tag) {
  asm volatile("v_not_b32 v%c2, %4\n\tglobal_load_dwordx2 v[%c2:%c3], %0, %1 sc1" ::"v"(idx * 128), "s"(f), "i"(R), "i"(R + 1), "s"((u32)tag)
               : "memory", "v252", "v253", "v254", "v255");
}
template <int R = 254> DI bool seen(u64 tag) {
  u64 miss;
  asm volatile("v_cmp_ne_u64 %0, %1, v[%c2:%c3]" : "=s"(miss) : "s"(tag), "i"(R), "i"(R + 1) : "memory");
  return miss == 0;
}
template <int R = 254> DI void await(const char* f, int idx, u64 tag) { if (!seen<R>(tag)) poll(f, idx, tag); }
DI void publish(char* f, u64 tag, int lane) {
  JIT(20);
  WAITV(0);
  if (lane == 0) __hip_atomic_store((GAS u64*)f, tag, RLX_AGENT);
}
DI void publish_last(char* f, u64 tag, int lane, u32 cnt, u32 n) {
  JIT(21);
  WAITV(0);
  if (lane == 0 && __hip_atomic_fetch_add((LAS u32*)cnt, 1u, __ATOMIC_RELAXED, __HIP_MEMORY_SCOPE_WORKGROUP) == n - 1)
    __hip_atomic_store((GAS u64*)f, tag, RLX_AGENT);
}

struct Params {
  const char *x, *wqkv, *wo, *w1, *b1, *w2, *b2;
  char *out, *ws;
};
#define ROLES() \
  int b_ = b; asm volatile("" : "+s"(b_)); \
  const int xc = b_ & 7, jb = b_ >> 3, xm = 7 - xc; \
  const bool roleA = b_ < 128; \
  const int hg = 2 * (xc & 3) + (jb & 1), rt = 8 * (xc >> 2) + (jb >> 1); \
  const int dt = jb & 7, grp = (jb >> 3) & 1; \
  const bool late = !roleA && xm >= 5, narrow = !roleA && xm < 5 && grp; \
  const int fu = late ? 4 * xm - 5 + 2 * grp : grp ? xm : 5 + 2 * xm, gi = late ? 2 * xm + grp : grp ? xm : 5 + xm; \
  const int st = 4 * xc + (jb >> 3), dt3 = jb & 7; \
  const u32 d0 = dt ^ (late ? grp : 0), istep = narrow ? 2 : 1, rot = (5 * (2 * (rt & 7) + (rt >> 3))) >> 2; \
  (void)hg; (void)fu; (void)gi; (void)st; (void)dt3; (void)d0; (void)istep; (void)rot
#define EPOCH() *(const __attribute__((address_space(4))) u64*)(ws + WS_EPOCH)
__global__ __launch_bounds__(320, 2) void fused_fwd(Params P) {
  const int tid = threadIdx.x, lane = tid & 63;
  const int wave = __builtin_amdgcn_readfirstlane(tid >> 6);
  const int b = blockIdx.x;
  char* const ws = P.ws;
  constexpr u32 L0 = 0;
  const u32 LDUM = L0 + L_BYTES - 256, LCNT = L0 + L_BYTES - 512;
  const int c16 = lane & 15, g = lane >> 4, q4 = c16 >> 2, p4 = lane & 3;
  STAMP_DECL;
  STAMP_AT(0);
  {
    u64 pc; int left;
    asm volatile("s_getpc_b64 %0\n.Lcode_here:\n\ts_mov_b32 %1, .Lcode_exit_a-.Lcode_here\n\ts_max_i32 %1, %1, .Lcode_exit_b-.Lcode_here"
                 : "=s"(pc), "=&s"(left)::"scc");
    touch((const char*)pc, LDUM, min((((b >> 3) * 5 + wave) * 2 + (lane & 1)) * 128, left));
  }
  if (wave == 4) {
    __builtin_amdgcn_s_setprio(3);
    u32 v[8], sa;
    if (b >= 128) {
      ROLES();
      const u32 col = narrow ? ((lane & 7) ^ ((g & 1) << 1)) * 16 : (c16 ^ (g << 1)) * 16, stp = (narrow ? 8 : 4) * 3456 - 1024;
#pragma unroll
      for (int i = 0; i < 8; ++i)
        v[i] = (narrow ? lane >> 3 : g) * 3456 + (col ^ (narrow ? (i & 1) * 64 : ((i >> 1) & 1) * 128)) + i * stp + 4096;
      sa = narrow ? 10 * 8192 : 6 * 8192;
      STAMP_AT(1);
#pragma unroll 1
      for (int i = 0; i < PRE_B; ++i) gl8<8192, 0, L1_B>(P.w1 + fu * 128 + (8 * d0 + i * istep) * 110592, sa, v);
    } else {
      ROLES();
      const u32 hi = lane >= 30, cc = lane - 30 * hi, sg = (cc >= 10) + (cc >= 20);
      v[0] = (hi + 1) * 3840 + sg * 1120 + cc * 16;
#pragma unroll
      for (int i = 1; i < 8; ++i) v[i] = v[0] + i * 6720;
      sa = 0;
      STAMP_AT(1);
      const char* wp = P.wqkv + (hg * 160 + rot * 122880);
      gl8w<7680, 0, LQ_B>(wp, sa, v); gl8w<7680, 0, LQ_B>(wp + 61440, sa, v);
    }
    u64 e0 = EPOCH();
    ROLES();
    asm volatile("" : "+s"(e0));
    BAR(); LBAR(); STAMP_AT(14);
    const u64 tag = mix(e0);
    const u32 vl4 = lane * 16 + 4096, vl8 = vl4 + 4096;
    const u32 u0 = (lane >> 3) * 4096 + ((lane & 7) ^ (g << 1)) * 16;
    const u32 u[4] = {u0, u0 + 31744, u0 + 2 * 31744, u0 + 3 * 31744};
    const char* w2p = P.w2 + st * 128;
    u32 sa3 = 0;
    auto ahalf = [&]() { gl4<4096, 0, L3_B>(w2p, sa3, u); w2p += 131072; };
    const char* const gfl = ws + WS_GFLAG + dt3 * 8192;
    const char* bp = ws + WS_G + dt3 * 276480;
    u32 sb3 = L3_B;
    auto bunit3 = [&]() { glin10<10240, L3_B, L3_B + NB3 * 10240>(bp, sb3, vl4, vl8); bp += 10240; };
    auto start3 = [&](int done) {
#pragma unroll 1
      for (int i = 0; i < 4; ++i) bunit3();
#pragma unroll 1
      for (int i = done; i < 2 * NA3; i += 2) { ahalf(); ahalf(); }
    };
    if (!roleA) {
      const char* wb = P.w1 + fu * 128;
      const char* hb = ws + WS_H + dt * 5120;
      u32 ia = PRE_B * istep, ib2 = 0, sb = L1_B;
      static_assert(PRE_B <= 4, "");
      auto a8 = [&]() { gl8<8192, 0, L1_B>(wb + (ia ^ (8 * d0)) * 110592, sa, v); ia += istep; };
      auto bunit = [&]() { glin55<10240, L1_B, L1_B + NB1 * 10240>(hb + (ib2 ^ (4 * d0)) * 81920, sb, vl4, vl4 + 40960); ++ib2; };
#pragma unroll 1
      for (int i = 0; i < MID_B; ++i) a8();
      LBAR();
#pragma unroll 1
      for (int i = PRE_B + MID_B; i < 10; ++i) { WAITV(32); a8(); }
      STAMP_AT(2);
      peek(ws + WS_LNFLAG, (d0 & 6) * 32 + lane, tag);
      a8(); a8();
      WAITV(16);
      STAMP_AT(15);
      await(ws + WS_LNFLAG, (d0 & 6) * 32 + lane, tag);
      STAMP_AT(3); ACC_TO(-1);
      bunit(); bunit(); bunit();
      a8(); a8();
      LW1(36); LB1(); bunit();
      LW1(36);
      if (narrow) {
#pragma unroll 1
        for (int r = 1; r < 3; ++r) { LB1(); bunit(); LW1(20); }
        peek(ws + WS_ALLFLAG, 0, tag);
#pragma unroll 1
        for (int r = 3; r < 5; ++r) { LB1(); bunit(); LW1(20); }
      } else {
#pragma unroll 1
        for (int r = 1; r < 3; ++r) { LB1(); a8(); a8(); bunit(); LW1(52); }
        peek(ws + WS_ALLFLAG, 0, tag);
#pragma unroll 1
        for (int r = 3; r < 5; ++r) { LB1(); a8(); a8(); bunit(); LW1(52); }
      }
      STAMP_AT(5);
      await(ws + WS_ALLFLAG, 0, tag);
      STAMP_AT(6);
      if (narrow) {
#pragma unroll 1
        for (int r = 5; r < 29; ++r) { LB1(); if (r & 3) a8(); bunit(); LW1(28); }
      } else {
#pragma unroll 1
        for (int r = 5; r < 29; ++r) { LB1(); a8(); if (r & 3) a8(); bunit(); LW1(44); }
      }
      LB1();
      if (narrow) {
        u32 w5[5];
#pragma unroll
        for (int i = 0; i < 5; ++i) { const u32 n = 64 * i + lane; w5[i] = n / 40u * 640 + lane * 16 + 4096; }
        const char* wp = P.wo + (xm * 4 + (dt >> 1)) * (32 * 1280) + (dt & 1) * 640;
#pragma unroll 1
        for (int i = 0; i < 4; ++i) gl5(wp + i * (8 * 1280), i * 5120, w5);
        LW1(30); LB1(); LW1(20); LB1();
      } else {
        constexpr int E1 = 3 * EARLY / 8;
#pragma unroll 1
        for (int i = 0; i < E1; ++i) ahalf();
        LW1(18 + 4 * E1); LB1();
#pragma unroll 1
        for (int i = 0; i < E1; ++i) ahalf();
        LW1(8 * E1); LB1();
        if (!late) {
#pragma unroll 1
          for (int i = 2 * E1; i < EARLY; ++i) ahalf();
        }
      }
      if (!late) {
        LBAR();
        STAMP_AT(4); ACC0(11);
        if (narrow) { WAITV(0); LBAR(); LBAR(); }
#pragma unroll 1
        for (int i = narrow ? -16 : 0; i < 10; ++i) { ahalf(); WAITV(32); if (!narrow) WAITX(TH_P3); }
        peek(gfl, lane < 5 ? lane : 0, tag);
        WAITV(0);
        if (!narrow) LBAR();
      } else {
        ahalf(); ahalf(); ahalf(); ahalf();
        LBAR();
        STAMP_AT(4); ACC0(11);
#pragma unroll 1
        for (int i = 3 * EARLY / 8 * 2 + 4; i < LPK; ++i) ahalf();
        peek(gfl, lane < 5 ? lane : 0, tag);
        ahalf(); ahalf(); ahalf(); ahalf();
        WAITV(16);
      }
      await(gfl, lane < 5 ? lane : 0, tag);
      STAMP_AT(7); ACC_TO(-1);
      start3(late ? LPK + 4 : narrow ? 26 : EARLY + 10);
    } else {
      const char* wb = P.wqkv + hg * 160;
      const char* xb = ws + WS_H + rt * 163840;
      const u32 x0 = (lane ^ ((g & 1) << 3)) * 16 + 4096, xv[4] = {x0, x0 + 38912, x0 + 2 * 38912, x0 + 3 * 38912};
      u32 oa = (rot + 1) * 122880  , ob = rot * 2048, sb = LQ_B;
      auto a8 = [&]() { gl8w<7680, 0, LQ_B>(wb + oa, sa, v); oa = oa == 39 * 61440 ? 0 : oa + 61440; };
      auto bunit = [&]() { glin2222<8192, LQ_B, LQ_B + 4 * 8192>(xb + ob, sb, xv); ob = ob == 19 * 2048 ? 0 : ob + 2048; };
#pragma unroll 1
      for (int i = 0; i < MID_A; ++i) a8();
      LBAR();
#pragma unroll 1
      for (int i = PRE_A + MID_A; i < NPK_A; ++i) { WAITV(24); a8(); }
      STAMP_AT(2);
      peek(ws + WS_LNFLAG, rt * 16 + c16, tag);
      a8(); a8();
      WAITV(16);
      STAMP_AT(15);
      await(ws + WS_LNFLAG, rt * 16 + c16, tag);
      STAMP_AT(3); ACC_TO(-1);
      bunit(); bunit(); bunit();
      glin1(ws + WS_SC + rt * 512, L_SC, (lane & 31) * 16);
#pragma unroll 1
      for (int i = NPK_A + 2; i < 12; ++i) a8();
      LW1(16 + 8 * (10 - NPK_A));
#pragma unroll 1
      for (int r = 1; r < 17; ++r) { LB1(); a8(); if (r & 3) a8(); bunit(); LW1(40); }
      LB1(); bunit();
      const char* tb = P.w2 + xc * 512 + jb * (108 * 4096);
      const u32 tv = (lane >> 2) * 4096 + p4 * 128;
#pragma unroll 1
      for (int i = 0; i < 7; ++i) {
        if (i == 2) { LW1(26); LB1(); }
        touch(tb + (i == 6 ? 92 : 16 * i) * 4096, LDUM, tv);
      }
      LW1(15); LB1(); LW1(7); LB1();
      BAR();
      STAMP_AT(4); ACC0(11);
      const char* const kfl = ws + WS_KVFLAG + hg * 2048;
      int done = 0;
#pragma unroll 1
      for (; done < APRE; ++done) { ahalf(); WAITV(24); }
      LBAR();
      peek(gfl, lane < 5 ? lane : 0, tag);
      WAITV(0);
      LBAR();
      if (!seen(tag)) {
#pragma unroll 1
        for (; done < 2 * NA3; ++done) ahalf();
        poll(gfl, lane < 5 ? lane : 0, tag);
      }
      STAMP_AT(7); ACC_TO(-1);
      bunit3(); bunit3(); bunit3();
      WAITX(TH_PA);
      peek(kfl, 8 + (lane & 7), tag);
      bunit3(); bunit3();
      WAITV(41); LBAR(); WAITV(31); LBAR(); WAITV(20);
      STAMP_AT(8);
      await(kfl, 8 + (lane & 7), tag);
      STAMP_AT(9);
      peek(kfl, lane & 7, tag);
      LBAR(); WAITV(11); LBAR(); WAITV(0); LBAR();
      await(kfl, lane & 7, tag);
      LBAR();
      STAMP_AT(5);
      peek(gfl, lane < 5 ? 5 + lane : 5, tag);
      WAITV(0);
      const bool ready = seen(tag);
      if (!ready) { LBAR(); poll(gfl, lane < 5 ? 5 + lane : 5, tag); }
      bunit3(); bunit3();
      if (ready) LBAR();
      bunit3(); bunit3();
#pragma unroll 1
      for (; done < 2 * NA3 + 4; ++done) ahalf();
      WAITV(16);
    }
    {
      auto bunit = bunit3;
      if (!roleA) {
        LW3(30); LB3();
        peek(gfl, lane < 5 ? 5 + lane : 5, tag);
        bunit();
        LW3(30); LB3(); ahalf();
        LW3(25); LB3();
        WAITV(14);
        STAMP_AT(8);
        await(gfl, lane < 5 ? 5 + lane : 5, tag);
        STAMP_AT(9);
        bunit(); bunit(); ahalf();
      }
#pragma unroll 1
      for (int r = roleA ? 5 : 3; r < 7; ++r) { LB3(); bunit(); ahalf(); LW3(42); }
      peek(gfl, lane < 6 ? 10 + lane : 10, tag);
#pragma unroll 1
      for (int r = 7; r < 11; ++r) { LB3(); bunit(); ahalf(); LW3(46); }
      await(gfl, lane < 6 ? 10 + lane : 10, tag);
#pragma unroll 1
      for (int r = 11; r < 18; ++r) { LB3(); bunit(); ahalf(); if (r > H3) ahalf(); LW3(46); }
#pragma unroll 1
      for (int r = 18; r < 20; ++r) { LB3(); bunit(); ahalf(); ahalf(); LW3(62); }
      peek<252>(ws + WS_OFLAG + st * 2048, c16, tag);
      peek(gfl, lane < 20 ? 16 + lane : 16, tag);
#pragma unroll 1
      for (int r = 20; r < 22; ++r) { LB3(); bunit(); ahalf(); ahalf(); LW3(62); }
      LB3(); bunit(); LW3(54);
      LB3(); LW3(36);
      await<252>(ws + WS_OFLAG + st * 2048, c16, tag); await(gfl, lane < 20 ? 16 + lane : 16, tag);
      const char* ap = ws + WS_O + st * 81920 + dt3 * 8192;
      bp = ws + WS_WO + dt3 * 112640;
#pragma unroll 1
      for (int r = 24; r < 34; ++r) {
        LB3();
        glin8<8192, 0, L3_B>(ap, sa3, vl4);
        bunit();
        ap += 8192;
        if (r + dt3 == 33) { ap -= 81920; bp -= 102400; }
        LW3(36);
      }
      LB3(); LW3(18); LB3(); LW3(0); LB3();
    }
    STAMP_AT(10); ACC0(12);
    STAMP_OUT();
    asm volatile(".Lcode_exit_a:" ::: "memory");
    return;
  }

  const int hw = wave >> 1, ty = wave & 1;
  const __amdgpu_buffer_rsrc_t rs = __builtin_amdgcn_make_buffer_rsrc(ws, 0, WS_END, 0x00020000);
  u64 e0;
  {
    const int lb = (b & 4) * 32 + (b >> 3) * 4 + (b & 3);
    const int row = 2 * wave + (lane >> 5), hl = lane & 31;
    const GAS u32x2* xp = (const GAS u32x2*)(P.x + lb * (8 * 1280) + (u32)(row * 1280 + hl * 8));
    u32x2 raw[5];
#pragma unroll
    for (int i = 0; i < 5; ++i) raw[i] = xp[32 * i];
    SB();
    e0 = EPOCH();
    u32 ra = L0 + L_LN + q4 * LN_PITCH + (10 * wave + g) * 32 + p4 * 8, wa = L0 + L_LN + row * LN_PITCH + hl * 8;
    int go = WS_H + (lb >> 2) * 40960 + (10 * wave + g) * 1024 + (lb & 3) * 256 + c16 * 16, g1 = go + 4096, g2 = g < 2 ? go + 8192 : OOB;
    int gs = hl == 0 ? WS_SC + (8 * lb + row) * 4 : OOB;
    *(LAS u32*)LCNT = 0;
    asm volatile("" : "+v"(ra), "+v"(go), "+v"(g1), "+v"(g2), "+v"(gs), "+v"(wa), "+s"(e0));
    SB();
    f32x2 v[10];
#pragma unroll
    for (int i = 0; i < 5; ++i) { v[2 * i] = f32x2{blo(raw[i][0]), bhi(raw[i][0])}; v[2 * i + 1] = f32x2{blo(raw[i][1]), bhi(raw[i][1])}; }
    f32x2 s = v[0] + v[1], s2 = v[2] + v[3];
#pragma unroll
    for (int i = 4; i < 10; i += 2) { s += v[i]; s2 += v[i + 1]; }
    s += s2;
    const float m = sum32(s[0] + s[1]) * (1.f / 640.f);
#pragma unroll
    for (int i = 0; i < 10; ++i) v[i] -= m;
    f32x2 qq = v[0] * v[0], q2 = v[1] * v[1];
#pragma unroll
    for (int i = 2; i < 10; i += 2) { qq += v[i] * v[i]; q2 += v[i + 1] * v[i + 1]; }
    qq += q2;
    const float var = sum32(qq[0] + qq[1]) * (1.f / 640.f);
    const float r1 = __builtin_amdgcn_rsqf(var + 1e-5f), v2 = var * r1 * r1 + 1e-5f, r2 = __builtin_amdgcn_rsqf(v2), r12 = r1 * r2;
#pragma unroll
    for (int i = 0; i < 5; ++i) {
      const f32x2 a = v[2 * i] * r12, c = v[2 * i + 1] * r12;
      *(LAS u32x2*)(wa + i * 256) = u32x2{pk2(a[0], a[1]), pk2(c[0], c[1])};
    }
    __builtin_amdgcn_raw_buffer_store_b32(__float_as_uint(v2 * r2), rs, gs, 0, 16);
    STAMP_AT(1);
    BAR();
    s16x8 f[3];
#pragma unroll
    for (int i = 0; i < 3; ++i) f[i] = cat(ldtr(ra + i * 128), ldtr(ra + i * 128 + 4 * LN_PITCH));
    st128(rs, go, f[0]); st128(rs, g1, f[1]); st128(rs, g2, f[2]);
    BAR();
    publish_last(ws + WS_LNFLAG + lb * 128, mix(e0), lane, LCNT, 4);
    STAMP_AT(2);
    BAR();
  }
  const u64 tag = mix(e0);
  ROLES();
  const u32 xr = (st * 64 + wave * 16 + c16) * 1280 + dt3 * 160, xo = xr + (g & 1) * 32 + (g >> 1) * 16;
  u32x4 x01, x23; u32x2 x4; u32 bs;
  auto ldx = [&]() {
    x01 = *(const GAS u32x4*)(P.x + xo); x23 = *(const GAS u32x4*)(P.x + xo + 64);
    x4 = *(const GAS u32x2*)(P.x + (xr + 128 + g * 8));
    bs = *(const GAS unsigned short*)(P.b2 + st * 128 + wave * 32 + (u32)(c16 * 2));
  };
  f32x4 oc[2][5];
  const u32 a30 = L0 + (32 * ty + 4 * g + q4) * 128 + (wave ^ ((2 * g + (q4 >> 1)) & 3)) * 32 + p4 * 8;
  const u32 ga0 = L0 + L3_B + ty * 5120 + lane * 16;
  const u32 oa0 = L0 + ty * 4096 + hw * 1024 + lane * 16 + ty * 8;
  u32 sa3, sb3, na3, nb3, ng3;
  struct FA { s16x8 a[2], g[5]; };
  auto lda = [&](FA& f, bool tr) {
    const u32 aa = na3, ab = nb3, ga = ng3;
    BAR();
    if (tr) { f.a[1] = cat(ldtr(ab), ldtr(ab + 2048)); f.a[0] = cat(ldtr(aa), ldtr(aa + 2048)); }
    else { f.a[1] = cat(ld64(ab), ld64(ab + 2048)); f.a[0] = cat(ld64(aa), ld64(aa + 2048)); }
#pragma unroll
    for (int j = 0; j < 5; ++j) f.g[j] = ld128(ga + j * 1024);
    sa3 = sa3 == L3_B - 8192 ? 0 : sa3 + 8192;
    sb3 = sb3 == LA3 * 10240 ? 0 : sb3 + 10240;
    na3 = (tr ? a30 : oa0) + sa3; nb3 = na3 ^ (tr ? 32 : 8); ng3 = ga0 + sb3;
  };
  auto mma = [&](const FA& f) {
#pragma unroll
    for (int j = 0; j < 5; ++j) oc[1][j] = mfma32(f.g[j], f.a[1], oc[1][j]);
#pragma unroll
    for (int j = 0; j < 5; ++j) oc[0][j] = mfma32(f.g[j], f.a[0], oc[0][j]);
  };
  auto pat = [&]() {
    SG(0x100, 1);
#pragma unroll
    for (int j = 0; j < 4; ++j) { SG(0x8, 1); SG(0x100, 2); }
#pragma unroll
    for (int j = 0; j < 5; ++j) { SG(0x8, 1); SG(0x6, 2); }
    SG(0x8, 1);
    asm volatile("" : "+v"(na3), "+v"(nb3), "+v"(ng3));
    SB();
  };
  FA fa0, fa1;
  auto begin3 = [&]() {
    STAMP_AT(8);
#pragma unroll
    for (int j = 0; j < 5; ++j) oc[0][j] = oc[1][j] = f32x4{0.f, 0.f, 0.f, 0.f};
    sa3 = sb3 = 0; na3 = a30; nb3 = a30 ^ 32; ng3 = ga0;
    lda(fa0, true); SB();
  };
  auto rounds = [&](int r0, int r1, bool tr) {
#pragma unroll 1
    for (int r = r0; r < r1; r += 2) { lda(fa1, tr); mma(fa0); pat(); lda(fa0, tr); mma(fa1); pat(); }
  };
  if (roleA) {
    f32x4 aq[5][2] = {}, akv[5][4] = {};
    const u32 wa0 = L0 + (4 * g + q4) * 480 + p4 * 8;
    const u32 xa0 = L0 + LQ_B + wave * 2048 + (p4 >> 1) * 256 + ((4 * g + q4) ^ ((p4 >> 1) << 3)) * 16 + (p4 & 1) * 8;
    const int c32 = lane & 31, hh = lane >> 5;
    const int pb = WS_PART + (hg * 4 + wave) * (16 * 896);
    u32 vp0 = L0 + L_KV + (8 * hw + 4 * ty + g) * LN_PITCH + c16 * 16 + ty * 768, vp1 = vp0 + (1 - 2 * ty) * (4 * LN_PITCH);
    u32 ka = L0 + L_KV + hh * LN_PITCH + (20 * wave + (c32 < 20 ? c32 : 19)) * 16;
    u32 vb = L0 + L_KV + (16 + hh) * LN_PITCH + (c32 < 20 ? 20 * wave + c32 : 80) * 16;
    int so0 = c32 <= 20 ? pb + rt * 896 + (hh * 21 + c32) * 16 : OOB, so1 = lane <= 21 ? pb + rt * 896 + 672 + lane * 8 : OOB;
    u32 vp2 = vp0 - ty * 768 + 512, von = L0 + L_KV + (16 + 4 * wave + g) * LN_PITCH + 1280;
    asm volatile("" : "+v"(vp2), "+v"(von), "+v"(vp0), "+v"(vp1), "+v"(ka), "+v"(vb), "+v"(so0), "+v"(so1));
    u32 qa[5][3], za[5];
#pragma unroll
    for (int je = 0; je < 5; ++je) {
      const int e = 16 * je + c16, he = e / 20, el = e - 20 * he;
#pragma unroll
      for (int x = 0; x < 3; ++x) {
        const int dl = 4 * (je - 1 + x) + g - 5 * he;
        const int qi = dl < 4 ? ((dl & 1) * 21 + el) * 2 + (dl >> 1) : 84 + el;
        qa[je][x] = L0 + L_FIN + (dl >= 0 && dl < 5 ? he * 848 + qi * 8 : 3392);
        if (je - 1 + x >= 0 && je - 1 + x < 5) asm volatile("" : "+v"(qa[je][x]));
      }
      const int dl = 4 * je + g - 5 * p4;
      const int qi = dl < 4 ? ((dl & 1) * 21 + 20) * 2 + (dl >> 1) : 104;
      za[je] = L0 + L_FIN + (dl >= 0 && dl < 5 ? p4 * 848 + qi * 8 : 3392);
      asm volatile("" : "+v"(za[je]));
    }
    u32x4 sel;
    {
      const int r = (c32 & 3) + 4 * (c32 >> 3);
      const u32 one = c32 < 16 && ((c32 >> 2) & 1) == hh ? 0x3f80u << (16 * (r & 1)) : 0u;
#pragma unroll
      for (int i = 0; i < 4; ++i) sel[i] = (r >> 1) == i ? one : 0u;
    }
    int go = lane < 53 ? pb + lane * 16 : OOB;
    u32 fa = L0 + L_FIN + (lane < 53 ? wave * 848 + lane * 16 : 3392);
    int ob = WS_O + (2 * rt + hw) * 81920 + hg * 10240 + ty * 1024 + lane * 16;
    asm volatile("" : "+v"(sel), "+v"(go), "+v"(fa), "+v"(ob));
    s16x8 xA[4] = {}, xB[4], wq[5] = {};
    u32 nwa = wa0, nwk = wa0 + ty * 96, nxa = xa0, nxb = xa0 ^ 2048;
    auto round = [&](int r, s16x8(&xo)[4], s16x8(&xn)[4]) {
      const u32 wa = nwa, wk = nwk, xa = nxa, xb = nxb;
      BAR();
      s16x8 w[6];
      xn[0] = cat(ldtr(xa), ldtr(xa + 1024)); w[0] = cat(ldtr(wk + 160), ldtr(wk + 160 + 7680));
      xn[1] = cat(ldtr(xa + 512), ldtr(xa + 1536)); xn[2] = cat(ldtr(xb), ldtr(xb + 1024)); xn[3] = cat(ldtr(xb + 512), ldtr(xb + 1536));
      w[1] = cat(ldtr(wk + 192), ldtr(wk + 192 + 7680));
#pragma unroll
      for (int j = 0; j < 5; ++j) { aq[j][0] = mfma32(wq[j], xo[0], aq[j][0]); aq[j][1] = mfma32(wq[j], xo[1], aq[j][1]); }
#pragma unroll
      for (int j = 0; j < 2; ++j) { SG(0x8, 1); SG(0x100, 2); }
#pragma unroll
      for (int j = 0; j < 8; ++j) { SG(0x8, 1); SG(0x100, 1); }
      SB();
      w[3] = cat(ldtr(wk + 320), ldtr(wk + 320 + 7680));
      w[4] = cat(ldtr(wk + 352), ldtr(wk + 352 + 7680));
      w[2] = cat(ldtr(wa + 224), ldtr(wa + 224 + 7680));
      w[5] = cat(ldtr(wa + 384), ldtr(wa + 384 + 7680));
#pragma unroll
      for (int j = 0; j < 5; ++j) wq[j] = cat(ldtr(wa + j * 32), ldtr(wa + 7680 + j * 32));
      nwa = wa0 + ((r + 1) & 7) * 15360; nwk = nwa + ty * 96; nxa = xa0 + ((r + 1) & 3) * 8192; nxb = nxa ^ 2048;
#pragma unroll
      for (int j = 0; j < 5; ++j) {
        if (j == 2) continue;
#pragma unroll
        for (int i = 0; i < 4; ++i) akv[j][i] = mfma32(xn[i], w[j], akv[j][i]);
      }
#pragma unroll
      for (int i = 0; i < 4; ++i) akv[2][i] = mfma32(xn[i & 1], w[i < 2 ? 2 : 5], akv[2][i]);
#pragma unroll
      for (int j = 0; j < 3; ++j) { SG(0x8, 1); SG(0x100, 2); }
#pragma unroll
      for (int j = 0; j < 12; ++j) { SG(0x8, 1); SG(0x100, 1); }
#pragma unroll
      for (int j = 0; j < 4; ++j) { SG(0x8, 1); SG(0x6, 2); }
      SG(0x8, 1);
      asm volatile("" : "+v"(nwa), "+v"(nwk), "+v"(nxa), "+v"(nxb));
      SB();
    };
#pragma unroll 1
    for (int r = 0; r < 20; r += 2) { round(r, xA, xB); round(r + 1, xB, xA); }
    f32x4 sk[4], sq[2];
#pragma unroll
    for (int i = 0; i < 4; ++i) sk[i] = *(const LAS f32x4*)(L0 + L_SC + ((wave ^ (i >> 1)) * 128 + (i & 1) * 64 + g * 16));
#pragma unroll
    for (int i = 0; i < 2; ++i) { const float t = *(const LAS float*)(L0 + L_SC + (wave * 128 + i * 64 + c16 * 4)); sq[i] = f32x4{t, t, t, t}; }
    BAR();
    STAMP_AT(3);
    {
      SB();
      *(LAS s16x8*)(vp2 + 16 * LN_PITCH) = cat(pk4(akv[2][2] * sk[0]), pk4(akv[2][3] * sk[1]));
      *(LAS s16x8*)(von) = s16x8{0x3f80, 0x3f80, 0x3f80, 0x3f80, 0x3f80, 0x3f80, 0x3f80, 0x3f80};
#pragma unroll
      for (int j = 3; j < 5; ++j) {
        *(LAS s16x8*)(vp0 + 16 * LN_PITCH + (j - 3) * 256) = cat(pk4(akv[j][0] * sk[0]), pk4(akv[j][1] * sk[1]));
        *(LAS s16x8*)(vp1 + 16 * LN_PITCH + (j - 3) * 256) = cat(pk4(akv[j][2] * sk[2]), pk4(akv[j][3] * sk[3]));
      }
      SB();
#pragma unroll
      for (int j = 0; j < 2; ++j) {
        s16x4 o[4];
        phi<4>(akv[j], sk, o);
        *(LAS s16x8*)(vp0 + j * 256) = cat(o[0], o[1]);
        *(LAS s16x8*)(vp1 + j * 256) = cat(o[2], o[3]);
        SB();
      }
      const f32x4 k2[2] = {akv[2][0], akv[2][1]};
      s16x4 o[2];
      phi<2>(k2, sk, o);
      *(LAS s16x8*)(vp2) = cat(o[0], o[1]);
    }
    STAMP_AT(4);
    BAR();
    {
      s16x8 fk[8], fv[8];
#pragma unroll
      for (int i = 0; i < 8; ++i) { fk[i] = ld128(ka + i * 2 * LN_PITCH); fv[i] = ld128(vb + i * 2 * LN_PITCH); }
      SB();
      f32x16 d = {};
#pragma unroll
      for (int i = 0; i < 8; ++i) d = mfmaw(fk[i], fv[i], d);
      SB();
#pragma unroll
      for (int j = 0; j < 2; ++j) { aq[j][0] = mfma32(wq[j], xA[0], aq[j][0]); aq[j][1] = mfma32(wq[j], xA[1], aq[j][1]); }
      SB();
      const u32x4 lo = {pk2(d[0], d[1]), pk2(d[2], d[3]), pk2(d[4], d[5]), pk2(d[6], d[7])};
      const u32x2 hi = {pk2(d[8], d[9]), pk2(d[10], d[11])};
      st128(rs, so0, __builtin_bit_cast(s16x8, lo));
      st64(rs, so1, __builtin_bit_cast(s16x4, hi));
      SB();
    }
    STAMP_AT(13);
    BAR();
#pragma unroll
    for (int j = 2; j < 5; ++j) { aq[j][0] = mfma32(wq[j], xA[0], aq[j][0]); aq[j][1] = mfma32(wq[j], xA[1], aq[j][1]); }
    SB();
    s16x4 pq[5][2];
#pragma unroll
    for (int j = 0; j < 2; ++j) { phi<2>(aq[j], sq, pq[j]); asm volatile("" : "+v"(pq[j][0]), "+v"(pq[j][1])); }
    publish_last(ws + WS_KVFLAG + hg * 2048 + rt * 128, tag, lane, LCNT, 8);
    STAMP_AT(5);
    ldx();
    SB();
    s16x8 p01[2], p23[2], p34[2];
#pragma unroll
    for (int j = 2; j < 5; ++j) phi<2>(aq[j], sq, pq[j]);
#pragma unroll
    for (int i = 0; i < 2; ++i) {
      p01[i] = cat(pq[0][i], pq[1][i]); p23[i] = cat(pq[2][i], pq[3][i]); p34[i] = cat(pq[3][i], pq[4][i]);
      asm volatile("" : "+v"(p01[i]), "+v"(p23[i]), "+v"(p34[i]));
    }
    begin3();
    rounds(1, 3, true);
    {
      f32x16 d = {};
      u32x4 pr[16];
#pragma unroll
      for (int i = 0; i < 8; ++i) pr[i] = __builtin_amdgcn_raw_buffer_load_b128(rs, go + (i & 3) * 896, ((i ^ 8) >> 2) * 3584, 16);
      SB();
      rounds(3, 5, true);
      STAMP_AT(12);
      BAR();
      STAMP_AT(6);
#pragma unroll
      for (int i = 8; i < 16; ++i) pr[i] = __builtin_amdgcn_raw_buffer_load_b128(rs, go + (i & 3) * 896, ((i ^ 8) >> 2) * 3584, 16);
      SB();
#pragma unroll
      for (int i = 0; i < 16; ++i) d = mfmaw(__builtin_bit_cast(s16x8, sel), __builtin_bit_cast(s16x8, pr[i]), d);
      *(LAS u32x4*)fa = u32x4{pk2(d[0], d[1]), pk2(d[2], d[3]), pk2(d[4], d[5]), pk2(d[6], d[7])};
    }
    STAMP_AT(7);
    BAR();
    STAMP_AT(15);
    {
      const u32 z0 = L0 + L_FIN + 3392;
      const s16x8 z01 = cat(ld64(za[0]), ld64(za[1])), z23 = cat(ld64(za[2]), ld64(za[3])), z4 = cat(ld64(z0), ld64(za[4]));
      const s16x8 k0 = cat(ld64(qa[0][1]), ld64(qa[0][2])), k1a = cat(ld64(qa[1][0]), ld64(qa[1][1])), k2a = cat(ld64(qa[2][1]), ld64(qa[2][2]));
      const s16x8 k3a = cat(ld64(qa[3][0]), ld64(qa[3][1])), k4 = cat(ld64(qa[4][0]), ld64(qa[4][1]));
      const s16x4 zz = {0, 0, 0, 0};
      const s16x8 k1b = cat(ld64(qa[1][2]), zz), k2b = cat(zz, ld64(qa[2][0])), k3b = cat(zz, ld64(qa[3][2]));
      SB();
      f32x4 den[2], o[5][2];
      const f32x4 e4 = {1e-6f, 1e-6f, 1e-6f, 1e-6f}, n4 = {0.f, 0.f, 0.f, 0.f};
#pragma unroll
      for (int i = 0; i < 2; ++i) den[i] = mfma32(z01, p01[i], e4);
#pragma unroll
      for (int i = 0; i < 2; ++i) den[i] = mfma32(z23, p23[i], den[i]);
#pragma unroll
      for (int i = 0; i < 2; ++i) den[i] = mfma32(z4, p34[i], den[i]);
#pragma unroll
      for (int i = 0; i < 2; ++i) { o[0][i] = mfma32(k0, p01[i], n4); o[1][i] = mfma32(k1a, p01[i], n4); }
      SB();
      float rc[5][2];
#pragma unroll
      for (int i = 0; i < 2; ++i) {
        const float r0 = __builtin_amdgcn_rcpf(den[i][0]), r1 = __builtin_amdgcn_rcpf(den[i][1]), r2 = __builtin_amdgcn_rcpf(den[i][2]), r3 = __builtin_amdgcn_rcpf(den[i][3]);
        rc[0][i] = r0; rc[1][i] = g < 1 ? r0 : r1; rc[2][i] = g < 2 ? r1 : r2; rc[3][i] = g < 3 ? r2 : r3; rc[4][i] = r3;
      }
#pragma unroll
      for (int i = 0; i < 2; ++i) { o[1][i] = mfma32(k1b, p23[i], o[1][i]); o[2][i] = mfma32(k2a, p23[i], n4); o[3][i] = mfma32(k3a, p23[i], n4); }
#pragma unroll
      for (int i = 0; i < 2; ++i) { o[2][i] = mfma32(k2b, p01[i], o[2][i]); o[3][i] = mfma32(k3b, p34[i], o[3][i]); o[4][i] = mfma32(k4, p34[i], n4); }
#pragma unroll
      for (int i = 0; i < 12; ++i) { SG(0x8, 1); SG(0x402, 1); }
      SB();
#pragma unroll
      for (int je = 0; je < 5; ++je) { st128(rs, ob + je * 2048, cat(pk4(o[je][0] * rc[je][0]), pk4(o[je][1] * rc[je][1]))); SB(); }
    }
    STAMP_AT(14);
  } else {
    if (narrow) {
      if (xm == 0 && dt == 0 && wave == 2) {
        const GAS u64* fp = (const GAS u64*)(ws + WS_LNFLAG + (u32)(lane * 128));
#pragma unroll 1
        for (u32 sp = 0; sp < SPIN_CAP; ++sp) {
          const u64 f0 = __hip_atomic_load(fp, RLX_AGENT), f1 = __hip_atomic_load(fp + 1024, RLX_AGENT);
          const u64 f2 = __hip_atomic_load(fp + 2048, RLX_AGENT), f3 = __hip_atomic_load(fp + 3072, RLX_AGENT);
          if (__all(f0 == tag && f1 == tag && f2 == tag && f3 == tag)) break;
        }
        asm volatile("" ::: "memory");
        if (lane < 2) __hip_atomic_store((GAS u64*)(ws + (lane ? WS_EPOCH : WS_ALLFLAG)), tag, RLX_AGENT);
      }
      f32x4 acc[5];
      {
        const u32x2 bb = *(const GAS u32x2*)(P.b1 + fu * 128 + hw * 64 + ty * 16 + (u32)((g & 1) * 32 + (g >> 1) * 8));
#pragma unroll
        for (int j = 0; j < 5; ++j) acc[j] = f32x4{blo(bb[0]), bhi(bb[0]), blo(bb[1]), bhi(bb[1])};
      }
      const u32 aa0 = L0 + (8 * g + q4) * 128 + ((2 * hw + (p4 & 1)) ^ ((q4 >> 1) | ((g & 1) << 1))) * 32 + ty * 16 + (p4 >> 1) * 8;
      const u32 ha0 = L0 + L1_B + lane * 16;
      u32 as = 10 * 8192, hs = 0, naa = aa0 + as, nha = ha0;
      struct FN { s16x8 a[2], h[10]; };
      auto step = [&](FN& n, const FN& o) {
        const u32 aa = naa, ha = nha;
        BAR();
        n.a[0] = cat(ldtr(aa), ldtr(aa + 512)); n.a[1] = cat(ldtr(aa + 4096), ldtr(aa + 4608));
#pragma unroll
        for (int j = 0; j < 10; ++j) n.h[j] = ld128(ha + j * 1024);
        as = as == L1_B - 8192 ? 0 : as + 8192;
        hs = hs == (NB1 - 1) * 10240 ? 0 : hs + 10240;
        naa = aa0 + as; nha = ha0 + hs;
#pragma unroll
        for (int j = 0; j < 10; ++j) acc[j % 5] = mfma32(o.a[j / 5], o.h[j], acc[j % 5]);
        SG(0x100, 2);
#pragma unroll
        for (int j = 0; j < 6; ++j) { SG(0x8, 1); SG(0x100, 2); }
#pragma unroll
        for (int j = 0; j < 4; ++j) { SG(0x8, 1); SG(0x6, 2); }
        asm volatile("" : "+v"(naa), "+v"(nha));
        SB();
      };
      FN f0 = {}, f1;
#pragma unroll 1
      for (int r = 0; r < 32; r += 2) { step(f1, f0); step(f0, f1); }
      BAR();
      STAMP_AT(3);
#pragma unroll
      for (int j = 0; j < 10; ++j) acc[j % 5] = mfma32(f0.a[j / 5], f0.h[j], acc[j % 5]);
      const int go = WS_G + dt * 276480 + (fu * 2 + hw) * 5120 + (g & 1) * 1024 + (2 * ty + (g >> 1)) * 256 + c16 * 16;
#pragma unroll
      for (int j = 0; j < 5; j += 2) {
        const u32x4 o = swap16(__builtin_bit_cast(u32x4, gelu8(acc[j], acc[j < 4 ? j + 1 : j])));
        st128(rs, j < 4 || !(g & 1) ? go + j * 1024 : OOB, __builtin_bit_cast(s16x8, o));
      }
      STAMP_AT(4);
      BAR();
      const u32 ra = L0 + (4 * g + q4) * 640 + wave * 160 + p4 * 8;
      s16x8 f[5];
#pragma unroll
      for (int j = 0; j < 5; ++j) f[j] = cat(ldtr(ra + j * 32), ldtr(ra + j * 32 + 10240));
      publish_last(ws + WS_GFLAG + dt * 8192 + gi * 128, tag, lane, LCNT, 8);
      STAMP_AT(5);
      BAR();
      const int gw = WS_WO + (4 * (dt & 1) + wave) * 102400 + (xm * 4 + (dt >> 1)) * 5120 + lane * 16;
#pragma unroll
      for (int j = 0; j < 5; ++j) st128(rs, gw + j * 1024, f[j]);
      SB();
    } else {
      f32x4 acc[2][5];
#pragma unroll
      for (int t = 0; t < 2; ++t) {
        const u32x2 bb = *(const GAS u32x2*)(P.b1 + fu * 128 + wave * 64 + (u32)(t * 32 + g * 8));
        const f32x4 bi = {blo(bb[0]), bhi(bb[0]), blo(bb[1]), bhi(bb[1])};
#pragma unroll
        for (int j = 0; j < 5; ++j) acc[t][j] = bi;
      }
      s16x8 a1[2] = {}, h1[5] = {};
      constexpr int RP = 256;
      const u32 aa0 = L0 + (8 * g + q4) * RP + ((2 * wave) ^ (q4 | ((g & 1) << 2))) * 32 + p4 * 8;
      const u32 ha0 = L0 + L1_B + lane * 16;
      u32 as = 6 * 8192, hs = 0, naa = aa0 + as, nab = naa ^ 32, nha = ha0;
#pragma unroll 1
      for (int r = 0; r < 32; ++r) {
        const u32 aa = naa, ab = nab, ha = nha;
        BAR();
        s16x8 a0[2], h0[5];
        a0[0] = cat(ldtr(aa), ldtr(aa + 4 * RP)); h0[0] = ld128(ha); a0[1] = cat(ldtr(ab), ldtr(ab + 4 * RP));
#pragma unroll
        for (int j = 1; j < 5; ++j) h0[j] = ld128(ha + j * 1024);
#pragma unroll
        for (int j = 0; j < 5; ++j) { acc[0][j] = mfma32(a1[0], h1[j], acc[0][j]); acc[1][j] = mfma32(a1[1], h1[j], acc[1][j]); }
#pragma unroll
        for (int j = 0; j < 9; ++j) { SG(0x8, 1); SG(0x100, 1); }
        SG(0x8, 1);
        SB();
        a1[0] = cat(ldtr(aa + 32 * RP), ldtr(aa + 36 * RP)); a1[1] = cat(ldtr(ab + 32 * RP), ldtr(ab + 36 * RP));
#pragma unroll
        for (int j = 0; j < 5; ++j) h1[j] = ld128(ha + 5120 + j * 1024);
        as = as == L1_B - 64 * RP ? 0 : as + 64 * RP;
        hs = hs == (NB1 - 1) * 10240 ? 0 : hs + 10240;
        naa = aa0 + as; nab = naa ^ 32; nha = ha0 + hs;
#pragma unroll
        for (int j = 0; j < 5; ++j) { acc[0][j] = mfma32(a0[0], h0[j], acc[0][j]); acc[1][j] = mfma32(a0[1], h0[j], acc[1][j]); }
        SG(0x100, 1);
#pragma unroll
        for (int j = 0; j < 4; ++j) { SG(0x8, 1); SG(0x100, 2); }
#pragma unroll
        for (int j = 0; j < 5; ++j) { SG(0x8, 1); SG(0x6, 2); }
        SG(0x8, 1);
        asm volatile("" : "+v"(naa), "+v"(nab), "+v"(nha));
        SB();
      }
      BAR();
      STAMP_AT(3);
#pragma unroll
      for (int j = 0; j < 5; ++j) { acc[0][j] = mfma32(a1[0], h1[j], acc[0][j]); acc[1][j] = mfma32(a1[1], h1[j], acc[1][j]); }
      const int go = WS_G + dt * 276480 + (fu * 2 + wave) * 5120 + lane * 16;
#pragma unroll
      for (int j = 0; j < 5; ++j) st128(rs, go + j * 1024, gelu8(acc[0][j], acc[1][j]));
      STAMP_AT(4);
    }
    if (!late && !narrow) {
      publish_last(ws + WS_GFLAG + dt * 8192 + gi * 128, tag, lane, LCNT, 8);
      STAMP_AT(5);
      BAR();
    }
    ldx();
    begin3();
  }

  {
    rounds(roleA ? 5 : 1, roleA ? R_JOIN : PUBR, true);
    {
      asm volatile("" : "+v"(x01), "+v"(x23), "+v"(x4), "+v"(bs));
      const float bf = blo(bs);
      x01 = swap16(x01); x23 = swap16(x23);
      oc[0][0] += f32x4{blo(x01[0]), bhi(x01[0]), blo(x01[1]), bhi(x01[1])} + bf;
      oc[0][1] += f32x4{blo(x01[2]), bhi(x01[2]), blo(x01[3]), bhi(x01[3])} + bf;
      oc[0][2] += f32x4{blo(x23[0]), bhi(x23[0]), blo(x23[1]), bhi(x23[1])} + bf;
      oc[0][3] += f32x4{blo(x23[2]), bhi(x23[2]), blo(x23[3]), bhi(x23[3])} + bf;
      oc[0][4] += f32x4{blo(x4[0]), bhi(x4[0]), blo(x4[1]), bhi(x4[1])} + bf;
      if (late) publish_last(ws + WS_GFLAG + dt * 8192 + gi * 128, tag, lane, LCNT, 8);
      else if (roleA || narrow)
        publish(ws + (roleA ? WS_OFLAG + (2 * rt + hw) * 2048 + (ty * 8 + hg) * 128 : WS_GFLAG + (4 * (dt & 1) + wave) * 8192 + (16 + xm * 4 + (dt >> 1)) * 128), tag, lane);
      SB();
    }
    rounds(roleA ? R_JOIN : PUBR, 27, true);
    STAMP_AT(9);
    na3 = oa0 + sa3; nb3 = na3 ^ 8;
    static_assert(36 % NB3 < 2, "");
    u32 ew = L0 + L3_B + 20480 + wave * 5120 + lane * 16, er = L0 + L3_B + 20480 + (wave ^ 1) * 5120 + lane * 16, xo4 = xr + 128 + g * 8;
    asm volatile("" : "+v"(ew), "+v"(er), "+v"(xo4));
    rounds(27, 37, false);
    mma(fa0);
#pragma unroll
    for (int j = 0; j < 5; ++j) *(LAS f32x4*)(ew + j * 1024) = oc[1][j];
    SG(0x8, 6);
#pragma unroll
    for (int j = 0; j < 4; ++j) { SG(0x200, 1); SG(0x8, 1); }
    SG(0x200, 1);
    SB();
    BAR();
#pragma unroll
    for (int j = 0; j < 5; ++j) oc[1][j] = *(const LAS f32x4*)(er + j * 1024);
    SB();
#pragma unroll
    for (int j = 0; j < 5; ++j) oc[0][j] += oc[1][j];
    *(GAS u32x4*)(P.out + xo) = swap16(__builtin_bit_cast(u32x4, cat(pk4(oc[0][0]), pk4(oc[0][1]))));
    *(GAS u32x4*)(P.out + xo + 64) = swap16(__builtin_bit_cast(u32x4, cat(pk4(oc[0][2]), pk4(oc[0][3]))));
    *(GAS s16x4*)(P.out + xo4) = pk4(oc[0][4]);
    asm volatile(".Lcode_exit_b:" ::: "memory");
    STAMP_AT(10);
    WAITV(0);
    STAMP_AT(11);
    STAMP_OUT();
  }
}

__global__ void spin_k(const char* ws, int mul){
  u32 tstart=(u32)__builtin_amdgcn_s_memrealtime();
  __shared__ int acc, mn, mx;
  int b=threadIdx.x; const u64* s=(const u64*)(ws+WS_STAMP);
  if(b==0){ acc=0; mn=0x7fffffff; mx=-0x7fffffff; }
  __syncthreads();
  int t0=(int)((u32)s[(b*8+0)*NSTAMP]-tstart);
  atomicAdd(&acc, ((b&4)?1:-1)*t0*SGN); atomicMin(&mn,t0); atomicMax(&mx,t0);
  __syncthreads();
  if(b==0){ int v= SGN? acc/128 : mx-mn; if(v<0) v=0; u64 val=(u64)v*mul; if(val>100000) val=100000;
    u64 r0=__builtin_amdgcn_s_memrealtime(); while(__builtin_amdgcn_s_memrealtime()-r0<val) __builtin_amdgcn_s_sleep(2); }
}
extern "C" void kernel_launch(void* const* d_in, const int* in_sizes, int n_in, void* d_out, int out_size, void* d_ws,
                              size_t ws_size, hipStream_t stream) {
  Params p;
  p.x = (const char*)d_in[0];
  p.wqkv = (const char*)d_in[1];
  p.wo = (const char*)d_in[2];
  p.w1 = (const char*)d_in[4];
  p.b1 = (const char*)d_in[5];
  p.w2 = (const char*)d_in[6];
  p.b2 = (const char*)d_in[7];
  p.out = (char*)d_out;
  p.ws = (char*)d_ws;
  (void)hipFuncSetAttribute((const void*)fused_fwd, hipFuncAttributeMaxDynamicSharedMemorySize, L_BYTES);
  fused_fwd<<<256, 320, L_BYTES, stream>>>(p);
  spin_k<<<1,256,0,stream>>>(p.ws,20);
}
```

```cpp
#include <hip/hip_runtime.h>
#include <hip/hip_bf16.h>
#pragma clang diagnostic ignored "-Wint-to-pointer-cast"

#define GAS __attribute__((address_space(1)))
#define LAS __attribute__((address_space(3)))
#define DI __device__ __forceinline__
typedef unsigned u32;
typedef unsigned long long u64;
typedef short s16x4 __attribute__((ext_vector_type(4)));
typedef short s16x8 __attribute__((ext_vector_type(8)));
typedef __bf16 b16x2 __attribute__((ext_vector_type(2)));
typedef __bf16 b16x8 __attribute__((ext_vector_type(8)));
typedef float f32x2 __attribute__((ext_vector_type(2)));
typedef float f32x4 __attribute__((ext_vector_type(4)));
typedef float f32x16 __attribute__((ext_vector_type(16)));
typedef unsigned u32x2 __attribute__((ext_vector_type(2)));
typedef unsigned u32x4 __attribute__((ext_vector_type(4)));

constexpr int WS_EPOCH = 0;
constexpr int WS_ALLFLAG = 1024;
constexpr int WS_LNFLAG = 196608;
constexpr int WS_GFLAG = 720896;
constexpr int WS_KVFLAG = 65536;
constexpr int WS_OFLAG = 131072;
constexpr int WS_PART = 262144;
constexpr int WS_SC = 8192;
constexpr int WS_H = 786432;
constexpr int WS_G = WS_H + 2621440;
constexpr int WS_O = WS_G + 2211840;
constexpr int WS_WO = WS_O + 2621440;
constexpr int WS_END = WS_WO + 819200;
constexpr int OOB = 0x40000000;
constexpr int L_BYTES = 163840;
constexpr int L_LN = 131072, LN_PITCH = 1296;
constexpr int NB3 = 5, NB1 = 4, NA1 = 7;
constexpr int LA3 = NB3 - 1, NA3 = 18 - NB3, H3 = 2 * (NA3 - LA3 - 2);
constexpr int LQ_B = 122880, L1_B = NA1 * 16384, L3_B = NA3 * 8192;
constexpr int L_KV = L3_B, L_FIN = L3_B + NB3 * 10240, L_SC = L_FIN + 3584;
constexpr int PRE_A = 2;
constexpr int APRE = 20, NPK_A = 8, R_JOIN = 9, LPK = 22, EARLY = 16, PUBR = 3, TH_PA = 10, TH_P3 = 8, SLP = 2, MID_A = 2, PRE_B = 3, MID_B = 3;
constexpr u32 SPIN_CAP = 1u << 16;

#define BAR() do { asm volatile("" ::: "memory"); __builtin_amdgcn_s_waitcnt(0xC07F); asm volatile("s_barrier" ::: "memory"); } while (0)
#define WAITV(n) asm volatile("s_waitcnt vmcnt(" #n ")" ::: "memory")
#define WAITX(n) asm volatile("s_waitcnt vmcnt(%0)" ::"i"((n) > 63 ? 63 : (n)) : "memory")
#define LBAR() asm volatile("s_barrier" ::: "memory")
#define RLX_AGENT __ATOMIC_RELAXED, __HIP_MEMORY_SCOPE_AGENT

DI f32x4 mfma32(s16x8 a, s16x8 b, f32x4 c) {
  return __builtin_amdgcn_mfma_f32_16x16x32_bf16(__builtin_bit_cast(b16x8, a), __builtin_bit_cast(b16x8, b), c, 0, 0, 0);
}
DI f32x16 mfmaw(s16x8 a, s16x8 b, f32x16 c) {
  return __builtin_amdgcn_mfma_f32_32x32x16_bf16(__builtin_bit_cast(b16x8, a), __builtin_bit_cast(b16x8, b), c, 0, 0, 0);
}
DI s16x4 ldtr(u32 a) { return __builtin_amdgcn_ds_read_tr16_b64_v4i16((LAS s16x4*)a); }
DI s16x8 ld128(u32 a) { return *(const LAS s16x8*)a; }
DI s16x4 ld64(u32 a) { return *(const LAS s16x4*)a; }
DI s16x8 cat(s16x4 a, s16x4 b) { return __builtin_shufflevector(a, b, 0, 1, 2, 3, 4, 5, 6, 7); }
DI u32 pk2(float a, float b) { f32x2 v = {a, b}; return __builtin_bit_cast(u32, __builtin_convertvector(v, b16x2)); }
DI s16x4 pk4(f32x4 v) { u32x2 r = {pk2(v[0], v[1]), pk2(v[2], v[3])}; return __builtin_bit_cast(s16x4, r); }
DI float blo(u32 u) { return __uint_as_float(u << 16); }
DI float bhi(u32 u) { return __uint_as_float(u & 0xffff0000u); }
template <int C> DI float dpp_add(float s) { return s + __int_as_float(__builtin_amdgcn_update_dpp(0, __float_as_int(s), C, 0xf, 0xf, true)); }
DI float sum32(float s) {
  s = dpp_add<0x121>(s); s = dpp_add<0x122>(s); s = dpp_add<0x124>(s); s = dpp_add<0x128>(s);
  auto r = __builtin_amdgcn_permlane16_swap(__float_as_uint(s), __float_as_uint(s), false, false);
  return __uint_as_float(r[0]) + __uint_as_float(r[1]);
}
#define SG(m, n) __builtin_amdgcn_sched_group_barrier(m, n, 0)
#define SB() __builtin_amdgcn_sched_barrier(0)
#define AL() do { asm volatile(".p2align 3"); SB(); } while (0)
template <int N> DI void phi(const f32x4 (&u)[N], const f32x4* sc, s16x4 (&o)[N]) {
  const float L = 1.4426950408889634f;
  f32x4 a[N], y[N];
  SB(); AL();
#pragma unroll
  for (int i = 0; i < N; ++i) a[i] = __builtin_elementwise_fma(u[i], sc[i], f32x4{1.f, 1.f, 1.f, 1.f});
  SB();
#pragma unroll
  for (int i = 0; i < N; ++i) y[i] = __builtin_elementwise_fma(a[i], f32x4{L, L, L, L}, f32x4{-L, -L, -L, -L});
  SB();
#pragma unroll
  for (int i = 0; i < 4 * N; ++i) y[i >> 2][i & 3] = __builtin_amdgcn_fmed3f(__builtin_amdgcn_exp2f(y[i >> 2][i & 3]), 0.f, 1.f);
  SB();
#pragma unroll
  for (int i = 0; i < 4 * N; ++i) y[i >> 2][i & 3] = __int_as_float(max(__float_as_int(a[i >> 2][i & 3]), __float_as_int(y[i >> 2][i & 3])));
  SB();
#pragma unroll
  for (int i = 0; i < N; ++i) y[i] += 1e-6f;
  SB();
#pragma unroll
  for (int i = 0; i < N; ++i) o[i] = pk4(y[i]);
}
DI s16x8 gelu8(f32x4 u0, f32x4 u1) {
  const float K = -2.f * 0.7978845608028654f * 1.4426950408889634f, K3 = K * 0.044715f;
  SB(); AL();
  f32x4 y0 = u0 * u0, y1 = u1 * u1;
  SB();
  y0 = __builtin_elementwise_fma(y0, f32x4{K3, K3, K3, K3}, f32x4{K, K, K, K});
  y1 = __builtin_elementwise_fma(y1, f32x4{K3, K3, K3, K3}, f32x4{K, K, K, K});
  SB();
  y0 *= u0; y1 *= u1;
  SB();
#pragma unroll
  for (int i = 0; i < 4; ++i) y0[i] = __builtin_amdgcn_exp2f(y0[i]);
#pragma unroll
  for (int i = 0; i < 4; ++i) y1[i] = __builtin_amdgcn_exp2f(y1[i]);
  SB();
  y0 += 1.f; y1 += 1.f;
  SB();
#pragma unroll
  for (int i = 0; i < 4; ++i) y0[i] = __builtin_amdgcn_rcpf(y0[i]);
#pragma unroll
  for (int i = 0; i < 4; ++i) y1[i] = __builtin_amdgcn_rcpf(y1[i]);
  SB();
  y0 *= u0; y1 *= u1;
  SB();
  return cat(pk4(y0), pk4(y1));
}
DI void st128(__amdgpu_buffer_rsrc_t rs, int off, s16x8 v) { __builtin_amdgcn_raw_buffer_store_b128(__builtin_bit_cast(u32x4, v), rs, off, 0, 16); }
DI void st64(__amdgpu_buffer_rsrc_t rs, int off, s16x4 v) { __builtin_amdgcn_raw_buffer_store_b64(__builtin_bit_cast(u32x2, v), rs, off, 0, 16); }
DI u64 mix(u64 e) {
  return (u64)((u32)(e >> 32) * 0x85EBCA6Bu + 0x165667B1u) << 32 | (u32)((u32)e * 0x9E3779B1u + 0x7F4A7C15u);
}

#define GL(v, off, sc) "global_load_lds_dwordx4 " v ", %0 offset:" #off sc "\n\t"
#define M0(bias) "s_add_u32 m0, %1, " #bias "\n\ts_nop 0\n\t"
#define RING m = m == HI - STEP ? LO : m + STEP
template <int STEP, int LO, int HI> DI void gl4(const char* base, u32& m, const u32 (&v)[4]) {
  asm volatile(M0(0) GL("%2", 0, "") GL("%3", 1024, "") GL("%4", 2048, "") GL("%5", 3072, "")
               ::"s"(base), "s"(m), "v"(v[0]), "v"(v[1]), "v"(v[2]), "v"(v[3]) : "memory", "scc");
  RING;
}
template <int STEP, int LO, int HI> DI void gl8(const char* base, u32& m, const u32 (&v)[8]) {
  asm volatile(M0(4096) GL("%2", -4096, "") GL("%3", -3072, "") GL("%4", -2048, "") GL("%5", -1024, "")
               GL("%6", 0, "") GL("%7", 1024, "") GL("%8", 2048, "") GL("%9", 3072, "")
               ::"s"(base), "s"(m), "v"(v[0]), "v"(v[1]), "v"(v[2]), "v"(v[3]), "v"(v[4]), "v"(v[5]), "v"(v[6]), "v"(v[7]) : "memory", "scc");
  RING;
}
template <int STEP, int LO, int HI> DI void gl8w(const char* base, u32& m, const u32 (&v)[8]) {
  asm volatile("s_add_u32 m0, %1, 3840\n\ts_mov_b32 exec_hi, 0x0fffffff\n\t" GL("%2", -3840, "") GL("%3", -2880, "") GL("%4", -1920, "") GL("%5", -960, "")
               GL("%6", 0, "") GL("%7", 960, "") GL("%8", 1920, "") GL("%9", 2880, "") "s_mov_b32 exec_hi, -1"
               ::"s"(base), "s"(m), "v"(v[0]), "v"(v[1]), "v"(v[2]), "v"(v[3]), "v"(v[4]), "v"(v[5]), "v"(v[6]), "v"(v[7]) : "memory", "scc");
  RING;
}
DI void gl5(const char* base, u32 m, const u32 (&v)[5]) {
  asm volatile(M0(4096) GL("%2", -4096, "") GL("%3", -3072, "") GL("%4", -2048, "") GL("%5", -1024, "") GL("%6", 0, "")
               ::"s"(base), "s"(m), "v"(v[0]), "v"(v[1]), "v"(v[2]), "v"(v[3]), "v"(v[4]) : "memory", "scc");
}
#define GL8S M0(4096) GL("%2", -4096, " sc1") GL("%2", -3072, " sc1") GL("%2", -2048, " sc1") GL("%2", -1024, " sc1") \
             GL("%2", 0, " sc1") GL("%2", 1024, " sc1") GL("%2", 2048, " sc1") GL("%2", 3072, " sc1")
template <int STEP, int LO, int HI> DI void glin8(const char* base, u32& m, u32 vl4) {
  asm volatile(GL8S ::"s"(base), "s"(m), "v"(vl4) : "memory", "scc");
  RING;
}
template <int STEP, int LO, int HI> DI void glin10(const char* base, u32& m, u32 vl4, u32 vl8) {
  asm volatile(GL8S M0(8192) GL("%3", 0, " sc1") GL("%3", 1024, " sc1")
               ::"s"(base), "s"(m), "v"(vl4), "v"(vl8) : "memory", "scc");
  RING;
}
template <int STEP, int LO, int HI> DI void glin55(const char* base, u32& m, u32 vl4, u32 vl4b) {
  asm volatile(M0(4096) GL("%2", -4096, " sc1") GL("%2", -3072, " sc1") GL("%2", -2048, " sc1") GL("%2", -1024, " sc1") GL("%2", 0, " sc1")
               M0(9216) GL("%3", -4096, " sc1") GL("%3", -3072, " sc1") GL("%3", -2048, " sc1") GL("%3", -1024, " sc1") GL("%3", 0, " sc1")
               ::"s"(base), "s"(m), "v"(vl4), "v"(vl4b) : "memory", "scc");
  RING;
}
template <int STEP, int LO, int HI> DI void glin2222(const char* base, u32& m, const u32 (&v)[4]) {
  asm volatile(M0(4096) GL("%2", -4096, " sc1") GL("%2", -3072, " sc1") GL("%3", -2048, " sc1") GL("%3", -1024, " sc1")
               GL("%4", 0, " sc1") GL("%4", 1024, " sc1") GL("%5", 2048, " sc1") GL("%5", 3072, " sc1")
               ::"s"(base), "s"(m), "v"(v[0]), "v"(v[1]), "v"(v[2]), "v"(v[3]) : "memory", "scc");
  RING;
}
DI void glin1(const char* base, u32 m, u32 v) { asm volatile(M0(0) GL("%2", 0, " sc1")::"s"(base), "s"(m), "v"(v) : "memory", "scc"); }
DI u32x4 swap16(u32x4 t) {
  const auto s0 = __builtin_amdgcn_permlane16_swap(t[0], t[2], false, false), s1 = __builtin_amdgcn_permlane16_swap(t[1], t[3], false, false);
  return u32x4{s0[0], s1[0], s0[1], s1[1]};
}
DI void touch(const char* base, u32 m, u32 v) {
  asm volatile("s_mov_b32 m0, %1\n\ts_nop 0\n\tglobal_load_lds_dword %2, %0" ::"s"(base), "s"(m), "v"(v) : "memory");
}
DI void poll(const char* f, int idx, u64 tag) {
  const GAS u64* p = (const GAS u64*)(f + (u32)(idx * 128));
#pragma unroll 1
  for (u32 sp = 0; sp < SPIN_CAP; ++sp) {
    const u64 v = __hip_atomic_load(p, RLX_AGENT);
    if (__all(v == tag)) break;
    __builtin_amdgcn_s_sleep(SLP);
  }
  asm volatile("" ::: "memory");
}
template <int R = 254> DI void peek(const char* f, int idx, u64 tag) {
  asm volatile("v_not_b32 v%c2, %4\n\tglobal_load_dwordx2 v[%c2:%c3], %0, %1 sc1" ::"v"(idx * 128), "s"(f), "i"(R), "i"(R + 1), "s"((u32)tag)
               : "memory", "v252", "v253", "v254", "v255");
}
template <int R = 254> DI bool seen(u64 tag) {
  u64 miss;
  asm volatile("v_cmp_ne_u64 %0, %1, v[%c2:%c3]" : "=s"(miss) : "s"(tag), "i"(R), "i"(R + 1) : "memory");
  return miss == 0;
}
template <int R = 254> DI void await(const char* f, int idx, u64 tag) { if (!seen<R>(tag)) poll(f, idx, tag); }
DI void publish(char* f, u64 tag, int lane) {
  WAITV(0);
  if (lane == 0) __hip_atomic_store((GAS u64*)f, tag, RLX_AGENT);
}
DI void publish_last(char* f, u64 tag, int lane, u32 cnt, u32 n) {
  WAITV(0);
  if (lane == 0 && __hip_atomic_fetch_add((LAS u32*)cnt, 1u, __ATOMIC_RELAXED, __HIP_MEMORY_SCOPE_WORKGROUP) == n - 1)
    __hip_atomic_store((GAS u64*)f, tag, RLX_AGENT);
}

struct Params {
  const char *x, *wqkv, *wo, *w1, *b1, *w2, *b2;
  char *out, *ws;
};
#define ROLES() \
  int b_ = b; asm volatile("" : "+s"(b_)); \
  const int xc = b_ & 7, jb = b_ >> 3, xm = 7 - xc; \
  const bool roleA = b_ < 128; \
  const int hg = 2 * (xc & 3) + (jb & 1), rt = 8 * (xc >> 2) + (jb >> 1); \
  const int dt = jb & 7, grp = (jb >> 3) & 1; \
  const bool late = !roleA && xm >= 5, narrow = !roleA && xm < 5 && grp; \
  const int fu = late ? 4 * xm - 5 + 2 * grp : grp ? xm : 5 + 2 * xm, gi = late ? 2 * xm + grp : grp ? xm : 5 + xm; \
  const int st = 4 * xc + (jb >> 3), dt3 = jb & 7; \
  const u32 d0 = dt ^ (late ? grp : 0), istep = narrow ? 2 : 1, rot = (5 * (2 * (rt & 7) + (rt >> 3))) >> 2; \
  (void)hg; (void)fu; (void)gi; (void)st; (void)dt3; (void)d0; (void)istep; (void)rot
#define EPOCH() *(const __attribute__((address_space(4))) u64*)(ws + WS_EPOCH)
__global__ __launch_bounds__(320, 2) void fused_fwd(Params P) {
  const int tid = threadIdx.x, lane = tid & 63;
  const int wave = __builtin_amdgcn_readfirstlane(tid >> 6);
  const int b = blockIdx.x;
  char* const ws = P.ws;
  constexpr u32 L0 = 0;
  const u32 LDUM = L0 + L_BYTES - 256, LCNT = L0 + L_BYTES - 512;
  const int c16 = lane & 15, g = lane >> 4, q4 = c16 >> 2, p4 = lane & 3;
  {
    u64 pc; int left;
    asm volatile("s_getpc_b64 %0\n.Lcode_here:\n\ts_mov_b32 %1, .Lcode_exit_a-.Lcode_here\n\ts_max_i32 %1, %1, .Lcode_exit_b-.Lcode_here"
                 : "=s"(pc), "=&s"(left)::"scc");
    touch((const char*)pc, LDUM, min((((b >> 3) * 5 + wave) * 2 + (lane & 1)) * 128, left));
  }
  if (wave == 4) {
    __builtin_amdgcn_s_setprio(3);
    u32 v[8], sa;
    if (b >= 128) {
      ROLES();
      const u32 col = narrow ? ((lane & 7) ^ ((g & 1) << 1)) * 16 : (c16 ^ (g << 1)) * 16, stp = (narrow ? 8 : 4) * 3456 - 1024;
#pragma unroll
      for (int i = 0; i < 8; ++i)
        v[i] = (narrow ? lane >> 3 : g) * 3456 + (col ^ (narrow ? (i & 1) * 64 : ((i >> 1) & 1) * 128)) + i * stp + 4096;
      sa = narrow ? 10 * 8192 : 6 * 8192;
#pragma unroll 1
      for (int i = 0; i < PRE_B; ++i) gl8<8192, 0, L1_B>(P.w1 + fu * 128 + (8 * d0 + i * istep) * 110592, sa, v);
    } else {
      ROLES();
      const u32 hi = lane >= 30, cc = lane - 30 * hi, sg = (cc >= 10) + (cc >= 20);
      v[0] = (hi + 1) * 3840 + sg * 1120 + cc * 16;
#pragma unroll
      for (int i = 1; i < 8; ++i) v[i] = v[0] + i * 6720;
      sa = 0;
      const char* wp = P.wqkv + (hg * 160 + rot * 122880);
      gl8w<7680, 0, LQ_B>(wp, sa, v); gl8w<7680, 0, LQ_B>(wp + 61440, sa, v);
    }
    u64 e0 = EPOCH();
    ROLES();
    asm volatile("" : "+s"(e0));
    BAR(); LBAR();
    const u64 tag = mix(e0);
    const u32 vl4 = lane * 16 + 4096, vl8 = vl4 + 4096;
    const u32 u0 = (lane >> 3) * 4096 + ((lane & 7) ^ (g << 1)) * 16;
    const u32 u[4] = {u0, u0 + 31744, u0 + 2 * 31744, u0 + 3 * 31744};
    const char* w2p = P.w2 + st * 128;
    u32 sa3 = 0;
    auto ahalf = [&]() { gl4<4096, 0, L3_B>(w2p, sa3, u); w2p += 131072; };
    const char* const gfl = ws + WS_GFLAG + dt3 * 8192;
    const char* bp = ws + WS_G + dt3 * 276480;
    u32 sb3 = L3_B;
    auto bunit3 = [&]() { glin10<10240, L3_B, L3_B + NB3 * 10240>(bp, sb3, vl4, vl8); bp += 10240; };
    auto start3 = [&](int done) {
#pragma unroll 1
      for (int i = 0; i < 4; ++i) bunit3();
#pragma unroll 1
      for (int i = done; i < 2 * NA3; i += 2) { ahalf(); ahalf(); }
    };
    if (!roleA) {
      const char* wb = P.w1 + fu * 128;
      const char* hb = ws + WS_H + dt * 5120;
      u32 ia = PRE_B * istep, ib2 = 0, sb = L1_B;
      static_assert(PRE_B <= 4, "");
      auto a8 = [&]() { gl8<8192, 0, L1_B>(wb + (ia ^ (8 * d0)) * 110592, sa, v); ia += istep; };
      auto bunit = [&]() { glin55<10240, L1_B, L1_B + NB1 * 10240>(hb + (ib2 ^ (4 * d0)) * 81920, sb, vl4, vl4 + 40960); ++ib2; };
#pragma unroll 1
      for (int i = 0; i < MID_B; ++i) a8();
      LBAR();
#pragma unroll 1
      for (int i = PRE_B + MID_B; i < 10; ++i) { WAITV(32); a8(); }
      peek(ws + WS_LNFLAG, (d0 & 6) * 32 + lane, tag);
      a8(); a8();
      WAITV(16);
      await(ws + WS_LNFLAG, (d0 & 6) * 32 + lane, tag);
      bunit(); bunit(); bunit();
      a8(); a8();
      WAITX(36); LBAR(); bunit();
      WAITX(36);
      if (narrow) {
#pragma unroll 1
        for (int r = 1; r < 3; ++r) { LBAR(); bunit(); WAITX(20); }
        peek(ws + WS_ALLFLAG, 0, tag);
#pragma unroll 1
        for (int r = 3; r < 5; ++r) { LBAR(); bunit(); WAITX(20); }
      } else {
#pragma unroll 1
        for (int r = 1; r < 3; ++r) { LBAR(); a8(); a8(); bunit(); WAITX(52); }
        peek(ws + WS_ALLFLAG, 0, tag);
#pragma unroll 1
        for (int r = 3; r < 5; ++r) { LBAR(); a8(); a8(); bunit(); WAITX(52); }
      }
      await(ws + WS_ALLFLAG, 0, tag);
      if (narrow) {
#pragma unroll 1
        for (int r = 5; r < 29; ++r) { LBAR(); if (r & 3) a8(); bunit(); WAITX(28); }
      } else {
#pragma unroll 1
        for (int r = 5; r < 29; ++r) { LBAR(); a8(); if (r & 3) a8(); bunit(); WAITX(44); }
      }
      LBAR();
      if (narrow) {
        u32 w5[5];
#pragma unroll
        for (int i = 0; i < 5; ++i) { const u32 n = 64 * i + lane; w5[i] = n / 40u * 640 + lane * 16 + 4096; }
        const char* wp = P.wo + (xm * 4 + (dt >> 1)) * (32 * 1280) + (dt & 1) * 640;
#pragma unroll 1
        for (int i = 0; i < 4; ++i) gl5(wp + i * (8 * 1280), i * 5120, w5);
        WAITX(30); LBAR(); WAITX(20); LBAR();
      } else {
        constexpr int E1 = 3 * EARLY / 8;
#pragma unroll 1
        for (int i = 0; i < E1; ++i) ahalf();
        WAITX(18 + 4 * E1); LBAR();
#pragma unroll 1
        for (int i = 0; i < E1; ++i) ahalf();
        WAITX(8 * E1); LBAR();
        if (!late) {
#pragma unroll 1
          for (int i = 2 * E1; i < EARLY; ++i) ahalf();
        }
      }
      if (!late) {
        LBAR();
        if (narrow) { WAITV(0); LBAR(); LBAR(); }
#pragma unroll 1
        for (int i = narrow ? -16 : 0; i < 10; ++i) { ahalf(); WAITV(32); if (!narrow) WAITX(TH_P3); }
        peek(gfl, lane < 5 ? lane : 0, tag);
        WAITV(0);
        if (!narrow) LBAR();
      } else {
        ahalf(); ahalf(); ahalf(); ahalf();
        LBAR();
#pragma unroll 1
        for (int i = 3 * EARLY / 8 * 2 + 4; i < LPK; ++i) ahalf();
        peek(gfl, lane < 5 ? lane : 0, tag);
        ahalf(); ahalf(); ahalf(); ahalf();
        WAITV(16);
      }
      await(gfl, lane < 5 ? lane : 0, tag);
      start3(late ? LPK + 4 : narrow ? 26 : EARLY + 10);
    } else {
      const char* wb = P.wqkv + hg * 160;
      const char* xb = ws + WS_H + rt * 163840;
      const u32 x0 = (lane ^ ((g & 1) << 3)) * 16 + 4096, xv[4] = {x0, x0 + 38912, x0 + 2 * 38912, x0 + 3 * 38912};
      u32 oa = (rot + 1) * 122880  , ob = rot * 2048, sb = LQ_B;
      auto a8 = [&]() { gl8w<7680, 0, LQ_B>(wb + oa, sa, v); oa = oa == 39 * 61440 ? 0 : oa + 61440; };
      auto bunit = [&]() { glin2222<8192, LQ_B, LQ_B + 4 * 8192>(xb + ob, sb, xv); ob = ob == 19 * 2048 ? 0 : ob + 2048; };
#pragma unroll 1
      for (int i = 0; i < MID_A; ++i) a8();
      LBAR();
#pragma unroll 1
      for (int i = PRE_A + MID_A; i < NPK_A; ++i) { WAITV(24); a8(); }
      peek(ws + WS_LNFLAG, rt * 16 + c16, tag);
      a8(); a8();
      WAITV(16);
      await(ws + WS_LNFLAG, rt * 16 + c16, tag);
      bunit(); bunit(); bunit();
      glin1(ws + WS_SC + rt * 512, L_SC, (lane & 31) * 16);
#pragma unroll 1
      for (int i = NPK_A + 2; i < 12; ++i) a8();
      WAITX(16 + 8 * (10 - NPK_A));
#pragma unroll 1
      for (int r = 1; r < 17; ++r) { LBAR(); a8(); if (r & 3) a8(); bunit(); WAITX(40); }
      LBAR(); bunit();
      const char* tb = P.w2 + xc * 512 + jb * (108 * 4096);
      const u32 tv = (lane >> 2) * 4096 + p4 * 128;
#pragma unroll 1
      for (int i = 0; i < 7; ++i) {
        if (i == 2) { WAITX(26); LBAR(); }
        touch(tb + (i == 6 ? 92 : 16 * i) * 4096, LDUM, tv);
      }
      WAITX(15); LBAR(); WAITX(7); LBAR();
      BAR();
      const char* const kfl = ws + WS_KVFLAG + hg * 2048;
      int done = 0;
#pragma unroll 1
      for (; done < APRE; ++done) { ahalf(); WAITV(24); }
      LBAR();
      peek(gfl, lane < 5 ? lane : 0, tag);
      WAITV(0);
      LBAR();
      if (!seen(tag)) {
#pragma unroll 1
        for (; done < 2 * NA3; ++done) ahalf();
        poll(gfl, lane < 5 ? lane : 0, tag);
      }
      bunit3(); bunit3(); bunit3();
      WAITX(TH_PA);
      peek(kfl, 8 + (lane & 7), tag);
      bunit3(); bunit3();
      WAITV(41); LBAR(); WAITV(31); LBAR(); WAITV(20);
      await(kfl, 8 + (lane & 7), tag);
      peek(kfl, lane & 7, tag);
      LBAR(); WAITV(11); LBAR(); WAITV(0); LBAR();
      await(kfl, lane & 7, tag);
      LBAR();
      peek(gfl, lane < 5 ? 5 + lane : 5, tag);
      WAITV(0);
      const bool ready = seen(tag);
      if (!ready) { LBAR(); poll(gfl, lane < 5 ? 5 + lane : 5, tag); }
      bunit3(); bunit3();
      if (ready) LBAR();
      bunit3(); bunit3();
#pragma unroll 1
      for (; done < 2 * NA3 + 4; ++done) ahalf();
      WAITV(46);
    }
    {
      auto bunit = bunit3;
      if (!roleA) {
        WAITX(30); LBAR();
        peek(gfl, lane < 5 ? 5 + lane : 5, tag);
        bunit();
        WAITX(30); LBAR(); ahalf();
        WAITX(25); LBAR();
        WAITV(14);
        await(gfl, lane < 5 ? 5 + lane : 5, tag);
        bunit(); bunit(); ahalf();
      }
#pragma unroll 1
      for (int r = roleA ? 5 : 3; r < 7; ++r) { LBAR(); bunit(); ahalf(); WAITX(42); }
      peek(gfl, lane < 6 ? 10 + lane : 10, tag);
#pragma unroll 1
      for (int r = 7; r < 11; ++r) { LBAR(); bunit(); ahalf(); WAITX(46); }
      await(gfl, lane < 6 ? 10 + lane : 10, tag);
#pragma unroll 1
      for (int r = 11; r < 18; ++r) { LBAR(); bunit(); ahalf(); if (r > H3) ahalf(); WAITX(46); }
#pragma unroll 1
      for (int r = 18; r < 20; ++r) { LBAR(); bunit(); ahalf(); ahalf(); WAITX(62); }
      peek<252>(ws + WS_OFLAG + st * 2048, c16, tag);
      peek(gfl, lane < 20 ? 16 + lane : 16, tag);
#pragma unroll 1
      for (int r = 20; r < 22; ++r) { LBAR(); bunit(); ahalf(); ahalf(); WAITX(62); }
      LBAR(); bunit(); WAITX(54);
      LBAR(); WAITX(36);
      await<252>(ws + WS_OFLAG + st * 2048, c16, tag); await(gfl, lane < 20 ? 16 + lane : 16, tag);
      const char* ap = ws + WS_O + st * 81920 + dt3 * 8192;
      bp = ws + WS_WO + dt3 * 112640;
#pragma unroll 1
      for (int r = 24; r < 34; ++r) {
        LBAR();
        glin8<8192, 0, L3_B>(ap, sa3, vl4);
        bunit();
        ap += 8192;
        if (r + dt3 == 33) { ap -= 81920; bp -= 102400; }
        WAITX(36);
      }
      LBAR(); WAITX(18); LBAR(); WAITX(0); LBAR();
    }
    asm volatile(".Lcode_exit_a:" ::: "memory");
    return;
  }

  const int hw = wave >> 1, ty = wave & 1;
  const __amdgpu_buffer_rsrc_t rs = __builtin_amdgcn_make_buffer_rsrc(ws, 0, WS_END, 0x00020000);
  u64 e0;
  {
    const int lb = (b & 4) * 32 + (b >> 3) * 4 + (b & 3);
    const int row = 2 * wave + (lane >> 5), hl = lane & 31;
    const GAS u32x2* xp = (const GAS u32x2*)(P.x + lb * (8 * 1280) + (u32)(row * 1280 + hl * 8));
    u32x2 raw[5];
#pragma unroll
    for (int i = 0; i < 5; ++i) raw[i] = xp[32 * i];
    SB();
    e0 = EPOCH();
    u32 ra = L0 + L_LN + q4 * LN_PITCH + (10 * wave + g) * 32 + p4 * 8, wa = L0 + L_LN + row * LN_PITCH + hl * 8;
    int go = WS_H + (lb >> 2) * 40960 + (10 * wave + g) * 1024 + (lb & 3) * 256 + c16 * 16, g1 = go + 4096, g2 = g < 2 ? go + 8192 : OOB;
    int gs = hl == 0 ? WS_SC + (8 * lb + row) * 4 : OOB;
    *(LAS u32*)LCNT = 0;
    asm volatile("" : "+v"(ra), "+v"(go), "+v"(g1), "+v"(g2), "+v"(gs), "+v"(wa), "+s"(e0));
    SB();
    f32x2 v[10];
#pragma unroll
    for (int i = 0; i < 5; ++i) { v[2 * i] = f32x2{blo(raw[i][0]), bhi(raw[i][0])}; v[2 * i + 1] = f32x2{blo(raw[i][1]), bhi(raw[i][1])}; }
    f32x2 s = v[0] + v[1], s2 = v[2] + v[3];
#pragma unroll
    for (int i = 4; i < 10; i += 2) { s += v[i]; s2 += v[i + 1]; }
    s += s2;
    const float m = sum32(s[0] + s[1]) * (1.f / 640.f);
#pragma unroll
    for (int i = 0; i < 10; ++i) v[i] -= m;
    f32x2 qq = v[0] * v[0], q2 = v[1] * v[1];
#pragma unroll
    for (int i = 2; i < 10; i += 2) { qq += v[i] * v[i]; q2 += v[i + 1] * v[i + 1]; }
    qq += q2;
    const float var = sum32(qq[0] + qq[1]) * (1.f / 640.f);
    const float r1 = __builtin_amdgcn_rsqf(var + 1e-5f), v2 = var * r1 * r1 + 1e-5f, r2 = __builtin_amdgcn_rsqf(v2), r12 = r1 * r2;
#pragma unroll
    for (int i = 0; i < 5; ++i) {
      const f32x2 a = v[2 * i] * r12, c = v[2 * i + 1] * r12;
      *(LAS u32x2*)(wa + i * 256) = u32x2{pk2(a[0], a[1]), pk2(c[0], c[1])};
    }
    __builtin_amdgcn_raw_buffer_store_b32(__float_as_uint(v2 * r2), rs, gs, 0, 16);
    BAR();
    s16x8 f[3];
#pragma unroll
    for (int i = 0; i < 3; ++i) f[i] = cat(ldtr(ra + i * 128), ldtr(ra + i * 128 + 4 * LN_PITCH));
    st128(rs, go, f[0]); st128(rs, g1, f[1]); st128(rs, g2, f[2]);
    BAR();
    publish_last(ws + WS_LNFLAG + lb * 128, mix(e0), lane, LCNT, 4);
    BAR();
  }
  const u64 tag = mix(e0);
  ROLES();
  const u32 xr = (st * 64 + wave * 16 + c16) * 1280 + dt3 * 160, xo = xr + (g & 1) * 32 + (g >> 1) * 16;
  u32x4 x01, x23; u32x2 x4; u32 bs;
  auto ldx = [&]() {
    x01 = *(const GAS u32x4*)(P.x + xo); x23 = *(const GAS u32x4*)(P.x + xo + 64);
    x4 = *(const GAS u32x2*)(P.x + (xr + 128 + g * 8));
    bs = *(const GAS unsigned short*)(P.b2 + st * 128 + wave * 32 + (u32)(c16 * 2));
  };
  f32x4 oc[2][5];
  const u32 a30 = L0 + (32 * ty + 4 * g + q4) * 128 + (wave ^ ((2 * g + (q4 >> 1)) & 3)) * 32 + p4 * 8;
  const u32 ga0 = L0 + L3_B + ty * 5120 + lane * 16;
  const u32 oa0 = L0 + ty * 4096 + hw * 1024 + lane * 16 + ty * 8;
  u32 sa3, sb3, na3, nb3, ng3;
  struct FA { s16x8 a[2], g[5]; };
  auto lda = [&](FA& f, bool tr) {
    const u32 aa = na3, ab = nb3, ga = ng3;
    BAR();
    if (tr) { f.a[1] = cat(ldtr(ab), ldtr(ab + 2048)); f.a[0] = cat(ldtr(aa), ldtr(aa + 2048)); }
    else { f.a[1] = cat(ld64(ab), ld64(ab + 2048)); f.a[0] = cat(ld64(aa), ld64(aa + 2048)); }
#pragma unroll
    for (int j = 0; j < 5; ++j) f.g[j] = ld128(ga + j * 1024);
    sa3 = sa3 == L3_B - 8192 ? 0 : sa3 + 8192;
    sb3 = sb3 == LA3 * 10240 ? 0 : sb3 + 10240;
    na3 = (tr ? a30 : oa0) + sa3; nb3 = na3 ^ (tr ? 32 : 8); ng3 = ga0 + sb3;
  };
  auto mma = [&](const FA& f) {
#pragma unroll
    for (int j = 0; j < 5; ++j) oc[1][j] = mfma32(f.g[j], f.a[1], oc[1][j]);
#pragma unroll
    for (int j = 0; j < 5; ++j) oc[0][j] = mfma32(f.g[j], f.a[0], oc[0][j]);
  };
  auto pat = [&]() {
    SG(0x100, 1);
#pragma unroll
    for (int j = 0; j < 4; ++j) { SG(0x8, 1); SG(0x100, 2); }
#pragma unroll
    for (int j = 0; j < 5; ++j) { SG(0x8, 1); SG(0x6, 2); }
    SG(0x8, 1);
    asm volatile("" : "+v"(na3), "+v"(nb3), "+v"(ng3));
    SB();
  };
  FA fa0, fa1;
  auto begin3 = [&]() {
#pragma unroll
    for (int j = 0; j < 5; ++j) oc[0][j] = oc[1][j] = f32x4{0.f, 0.f, 0.f, 0.f};
    sa3 = sb3 = 0; na3 = a30; nb3 = a30 ^ 32; ng3 = ga0;
    lda(fa0, true); SB();
  };
  auto rounds = [&](int r0, int r1, bool tr) {
#pragma unroll 1
    for (int r = r0; r < r1; r += 2) { lda(fa1, tr); mma(fa0); pat(); lda(fa0, tr); mma(fa1); pat(); }
  };
  if (roleA) {
    f32x4 aq[5][2] = {}, akv[5][4] = {};
    const u32 wa0 = L0 + (4 * g + q4) * 480 + p4 * 8;
    const u32 xa0 = L0 + LQ_B + wave * 2048 + (p4 >> 1) * 256 + ((4 * g + q4) ^ ((p4 >> 1) << 3)) * 16 + (p4 & 1) * 8;
    const int c32 = lane & 31, hh = lane >> 5;
    const int pb = WS_PART + (hg * 4 + wave) * (16 * 896);
    u32 vp0 = L0 + L_KV + (8 * hw + 4 * ty + g) * LN_PITCH + c16 * 16 + ty * 768, vp1 = vp0 + (1 - 2 * ty) * (4 * LN_PITCH);
    u32 ka = L0 + L_KV + hh * LN_PITCH + (20 * wave + (c32 < 20 ? c32 : 19)) * 16;
    u32 vb = L0 + L_KV + (16 + hh) * LN_PITCH + (c32 < 20 ? 20 * wave + c32 : 80) * 16;
    int so0 = c32 <= 20 ? pb + rt * 896 + (hh * 21 + c32) * 16 : OOB, so1 = lane <= 21 ? pb + rt * 896 + 672 + lane * 8 : OOB;
    u32 vp2 = vp0 - ty * 768 + 512, von = L0 + L_KV + (16 + 4 * wave + g) * LN_PITCH + 1280;
    asm volatile("" : "+v"(vp2), "+v"(von), "+v"(vp0), "+v"(vp1), "+v"(ka), "+v"(vb), "+v"(so0), "+v"(so1));
    u32 qa[5][3], za[5];
#pragma unroll
    for (int je = 0; je < 5; ++je) {
      const int e = 16 * je + c16, he = e / 20, el = e - 20 * he;
#pragma unroll
      for (int x = 0; x < 3; ++x) {
        const int dl = 4 * (je - 1 + x) + g - 5 * he;
        const int qi = dl < 4 ? ((dl & 1) * 21 + el) * 2 + (dl >> 1) : 84 + el;
        qa[je][x] = L0 + L_FIN + (dl >= 0 && dl < 5 ? he * 848 + qi * 8 : 3392);
        if (je - 1 + x >= 0 && je - 1 + x < 5) asm volatile("" : "+v"(qa[je][x]));
      }
      const int dl = 4 * je + g - 5 * p4;
      const int qi = dl < 4 ? ((dl & 1) * 21 + 20) * 2 + (dl >> 1) : 104;
      za[je] = L0 + L_FIN + (dl >= 0 && dl < 5 ? p4 * 848 + qi * 8 : 3392);
      asm volatile("" : "+v"(za[je]));
    }
    u32x4 sel;
    {
      const int r = (c32 & 3) + 4 * (c32 >> 3);
      const u32 one = c32 < 16 && ((c32 >> 2) & 1) == hh ? 0x3f80u << (16 * (r & 1)) : 0u;
#pragma unroll
      for (int i = 0; i < 4; ++i) sel[i] = (r >> 1) == i ? one : 0u;
    }
    int go = lane < 53 ? pb + lane * 16 : OOB;
    u32 fa = L0 + L_FIN + (lane < 53 ? wave * 848 + lane * 16 : 3392);
    int ob = WS_O + (2 * rt + hw) * 81920 + hg * 10240 + ty * 1024 + lane * 16;
    asm volatile("" : "+v"(sel), "+v"(go), "+v"(fa), "+v"(ob));
    s16x8 xA[4] = {}, xB[4], wq[5] = {};
    u32 nwa = wa0, nwk = wa0 + ty * 96, nxa = xa0, nxb = xa0 ^ 2048;
    auto round = [&](int r, s16x8(&xo)[4], s16x8(&xn)[4]) {
      const u32 wa = nwa, wk = nwk, xa = nxa, xb = nxb;
      BAR();
      s16x8 w[6];
      xn[0] = cat(ldtr(xa), ldtr(xa + 1024)); w[0] = cat(ldtr(wk + 160), ldtr(wk + 160 + 7680));
      xn[1] = cat(ldtr(xa + 512), ldtr(xa + 1536)); xn[2] = cat(ldtr(xb), ldtr(xb + 1024)); xn[3] = cat(ldtr(xb + 512), ldtr(xb + 1536));
      w[1] = cat(ldtr(wk + 192), ldtr(wk + 192 + 7680));
#pragma unroll
      for (int j = 0; j < 5; ++j) { aq[j][0] = mfma32(wq[j], xo[0], aq[j][0]); aq[j][1] = mfma32(wq[j], xo[1], aq[j][1]); }
#pragma unroll
      for (int j = 0; j < 2; ++j) { SG(0x8, 1); SG(0x100, 2); }
#pragma unroll
      for (int j = 0; j < 8; ++j) { SG(0x8, 1); SG(0x100, 1); }
      SB();
      w[3] = cat(ldtr(wk + 320), ldtr(wk + 320 + 7680));
      w[4] = cat(ldtr(wk + 352), ldtr(wk + 352 + 7680));
      w[2] = cat(ldtr(wa + 224), ldtr(wa + 224 + 7680));
      w[5] = cat(ldtr(wa + 384), ldtr(wa + 384 + 7680));
#pragma unroll
      for (int j = 0; j < 5; ++j) wq[j] = cat(ldtr(wa + j * 32), ldtr(wa + 7680 + j * 32));
      nwa = wa0 + ((r + 1) & 7) * 15360; nwk = nwa + ty * 96; nxa = xa0 + ((r + 1) & 3) * 8192; nxb = nxa ^ 2048;
#pragma unroll
      for (int j = 0; j < 5; ++j) {
        if (j == 2) continue;
#pragma unroll
        for (int i = 0; i < 4; ++i) akv[j][i] = mfma32(xn[i], w[j], akv[j][i]);
      }
#pragma unroll
      for (int i = 0; i < 4; ++i) akv[2][i] = mfma32(xn[i & 1], w[i < 2 ? 2 : 5], akv[2][i]);
#pragma unroll
      for (int j = 0; j < 3; ++j) { SG(0x8, 1); SG(0x100, 2); }
#pragma unroll
      for (int j = 0; j < 12; ++j) { SG(0x8, 1); SG(0x100, 1); }
#pragma unroll
      for (int j = 0; j < 4; ++j) { SG(0x8, 1); SG(0x6, 2); }
      SG(0x8, 1);
      asm volatile("" : "+v"(nwa), "+v"(nwk), "+v"(nxa), "+v"(nxb));
      SB();
    };
#pragma unroll 1
    for (int r = 0; r < 20; r += 2) { round(r, xA, xB); round(r + 1, xB, xA); }
    f32x4 sk[4], sq[2];
#pragma unroll
    for (int i = 0; i < 4; ++i) sk[i] = *(const LAS f32x4*)(L0 + L_SC + ((wave ^ (i >> 1)) * 128 + (i & 1) * 64 + g * 16));
#pragma unroll
    for (int i = 0; i < 2; ++i) { const float t = *(const LAS float*)(L0 + L_SC + (wave * 128 + i * 64 + c16 * 4)); sq[i] = f32x4{t, t, t, t}; }
    BAR();
    {
      SB();
      *(LAS s16x8*)(vp2 + 16 * LN_PITCH) = cat(pk4(akv[2][2] * sk[0]), pk4(akv[2][3] * sk[1]));
      *(LAS s16x8*)(von) = s16x8{0x3f80, 0x3f80, 0x3f80, 0x3f80, 0x3f80, 0x3f80, 0x3f80, 0x3f80};
#pragma unroll
      for (int j = 3; j < 5; ++j) {
        *(LAS s16x8*)(vp0 + 16 * LN_PITCH + (j - 3) * 256) = cat(pk4(akv[j][0] * sk[0]), pk4(akv[j][1] * sk[1]));
        *(LAS s16x8*)(vp1 + 16 * LN_PITCH + (j - 3) * 256) = cat(pk4(akv[j][2] * sk[2]), pk4(akv[j][3] * sk[3]));
      }
      SB();
#pragma unroll
      for (int j = 0; j < 2; ++j) {
        s16x4 o[4];
        phi<4>(akv[j], sk, o);
        *(LAS s16x8*)(vp0 + j * 256) = cat(o[0], o[1]);
        *(LAS s16x8*)(vp1 + j * 256) = cat(o[2], o[3]);
        SB();
      }
      const f32x4 k2[2] = {akv[2][0], akv[2][1]};
      s16x4 o[2];
      phi<2>(k2, sk, o);
      *(LAS s16x8*)(vp2) = cat(o[0], o[1]);
    }
    BAR();
    {
      s16x8 fk[8], fv[8];
#pragma unroll
      for (int i = 0; i < 8; ++i) { fk[i] = ld128(ka + i * 2 * LN_PITCH); fv[i] = ld128(vb + i * 2 * LN_PITCH); }
      SB();
      f32x16 d = {};
#pragma unroll
      for (int i = 0; i < 8; ++i) d = mfmaw(fk[i], fv[i], d);
      SB();
#pragma unroll
      for (int j = 0; j < 2; ++j) { aq[j][0] = mfma32(wq[j], xA[0], aq[j][0]); aq[j][1] = mfma32(wq[j], xA[1], aq[j][1]); }
      SB();
      const u32x4 lo = {pk2(d[0], d[1]), pk2(d[2], d[3]), pk2(d[4], d[5]), pk2(d[6], d[7])};
      const u32x2 hi = {pk2(d[8], d[9]), pk2(d[10], d[11])};
      st128(rs, so0, __builtin_bit_cast(s16x8, lo));
      st64(rs, so1, __builtin_bit_cast(s16x4, hi));
      SB();
    }
    BAR();
#pragma unroll
    for (int j = 2; j < 5; ++j) { aq[j][0] = mfma32(wq[j], xA[0], aq[j][0]); aq[j][1] = mfma32(wq[j], xA[1], aq[j][1]); }
    SB();
    s16x4 pq[5][2];
#pragma unroll
    for (int j = 0; j < 2; ++j) { phi<2>(aq[j], sq, pq[j]); asm volatile("" : "+v"(pq[j][0]), "+v"(pq[j][1])); }
    publish_last(ws + WS_KVFLAG + hg * 2048 + rt * 128, tag, lane, LCNT, 8);
    ldx();
    SB();
    s16x8 p01[2], p23[2], p34[2];
#pragma unroll
    for (int j = 2; j < 5; ++j) phi<2>(aq[j], sq, pq[j]);
#pragma unroll
    for (int i = 0; i < 2; ++i) {
      p01[i] = cat(pq[0][i], pq[1][i]); p23[i] = cat(pq[2][i], pq[3][i]); p34[i] = cat(pq[3][i], pq[4][i]);
      asm volatile("" : "+v"(p01[i]), "+v"(p23[i]), "+v"(p34[i]));
    }
    begin3();
    rounds(1, 3, true);
    {
      f32x16 d = {};
      u32x4 pr[16];
#pragma unroll
      for (int i = 0; i < 8; ++i) pr[i] = __builtin_amdgcn_raw_buffer_load_b128(rs, go + (i & 3) * 896, ((i ^ 8) >> 2) * 3584, 16);
      SB();
      rounds(3, 5, true);
      BAR();
#pragma unroll
      for (int i = 8; i < 16; ++i) pr[i] = __builtin_amdgcn_raw_buffer_load_b128(rs, go + (i & 3) * 896, ((i ^ 8) >> 2) * 3584, 16);
      SB();
#pragma unroll
      for (int i = 0; i < 16; ++i) d = mfmaw(__builtin_bit_cast(s16x8, sel), __builtin_bit_cast(s16x8, pr[i]), d);
      *(LAS u32x4*)fa = u32x4{pk2(d[0], d[1]), pk2(d[2], d[3]), pk2(d[4], d[5]), pk2(d[6], d[7])};
    }
    BAR();
    {
      const u32 z0 = L0 + L_FIN + 3392;
      const s16x8 z01 = cat(ld64(za[0]), ld64(za[1])), z23 = cat(ld64(za[2]), ld64(za[3])), z4 = cat(ld64(z0), ld64(za[4]));
      const s16x8 k0 = cat(ld64(qa[0][1]), ld64(qa[0][2])), k1a = cat(ld64(qa[1][0]), ld64(qa[1][1])), k2a = cat(ld64(qa[2][1]), ld64(qa[2][2]));
      const s16x8 k3a = cat(ld64(qa[3][0]), ld64(qa[3][1])), k4 = cat(ld64(qa[4][0]), ld64(qa[4][1]));
      const s16x4 zz = {0, 0, 0, 0};
      const s16x8 k1b = cat(ld64(qa[1][2]), zz), k2b = cat(zz, ld64(qa[2][0])), k3b = cat(zz, ld64(qa[3][2]));
      SB();
      f32x4 den[2], o[5][2];
      const f32x4 e4 = {1e-6f, 1e-6f, 1e-6f, 1e-6f}, n4 = {0.f, 0.f, 0.f, 0.f};
#pragma unroll
      for (int i = 0; i < 2; ++i) den[i] = mfma32(z01, p01[i], e4);
#pragma unroll
      for (int i = 0; i < 2; ++i) den[i] = mfma32(z23, p23[i], den[i]);
#pragma unroll
      for (int i = 0; i < 2; ++i) den[i] = mfma32(z4, p34[i], den[i]);
#pragma unroll
      for (int i = 0; i < 2; ++i) { o[0][i] = mfma32(k0, p01[i], n4); o[1][i] = mfma32(k1a, p01[i], n4); }
      SB();
      float rc[5][2];
#pragma unroll
      for (int i = 0; i < 2; ++i) {
        const float r0 = __builtin_amdgcn_rcpf(den[i][0]), r1 = __builtin_amdgcn_rcpf(den[i][1]), r2 = __builtin_amdgcn_rcpf(den[i][2]), r3 = __builtin_amdgcn_rcpf(den[i][3]);
        rc[0][i] = r0; rc[1][i] = g < 1 ? r0 : r1; rc[2][i] = g < 2 ? r1 : r2; rc[3][i] = g < 3 ? r2 : r3; rc[4][i] = r3;
      }
#pragma unroll
      for (int i = 0; i < 2; ++i) { o[1][i] = mfma32(k1b, p23[i], o[1][i]); o[2][i] = mfma32(k2a, p23[i], n4); o[3][i] = mfma32(k3a, p23[i], n4); }
#pragma unroll
      for (int i = 0; i < 2; ++i) { o[2][i] = mfma32(k2b, p01[i], o[2][i]); o[3][i] = mfma32(k3b, p34[i], o[3][i]); o[4][i] = mfma32(k4, p34[i], n4); }
#pragma unroll
      for (int i = 0; i < 12; ++i) { SG(0x8, 1); SG(0x402, 1); }
      SB();
#pragma unroll
      for (int je = 0; je < 5; ++je) { st128(rs, ob + je * 2048, cat(pk4(o[je][0] * rc[je][0]), pk4(o[je][1] * rc[je][1]))); SB(); }
    }
  } else {
    if (narrow) {
      if (xm == 0 && dt == 0 && wave == 2) {
        const GAS u64* fp = (const GAS u64*)(ws + WS_LNFLAG + (u32)(lane * 128));
#pragma unroll 1
        for (u32 sp = 0; sp < SPIN_CAP; ++sp) {
          const u64 f0 = __hip_atomic_load(fp, RLX_AGENT), f1 = __hip_atomic_load(fp + 1024, RLX_AGENT);
          const u64 f2 = __hip_atomic_load(fp + 2048, RLX_AGENT), f3 = __hip_atomic_load(fp + 3072, RLX_AGENT);
          if (__all(f0 == tag && f1 == tag && f2 == tag && f3 == tag)) break;
        }
        asm volatile("" ::: "memory");
        if (lane < 2) __hip_atomic_store((GAS u64*)(ws + (lane ? WS_EPOCH : WS_ALLFLAG)), tag, RLX_AGENT);
      }
      f32x4 acc[5];
      {
        const u32x2 bb = *(const GAS u32x2*)(P.b1 + fu * 128 + hw * 64 + ty * 16 + (u32)((g & 1) * 32 + (g >> 1) * 8));
#pragma unroll
        for (int j = 0; j < 5; ++j) acc[j] = f32x4{blo(bb[0]), bhi(bb[0]), blo(bb[1]), bhi(bb[1])};
      }
      const u32 aa0 = L0 + (8 * g + q4) * 128 + ((2 * hw + (p4 & 1)) ^ ((q4 >> 1) | ((g & 1) << 1))) * 32 + ty * 16 + (p4 >> 1) * 8;
      const u32 ha0 = L0 + L1_B + lane * 16;
      u32 as = 10 * 8192, hs = 0, naa = aa0 + as, nha = ha0;
      struct FN { s16x8 a[2], h[10]; };
      auto step = [&](FN& n, const FN& o) {
        const u32 aa = naa, ha = nha;
        BAR();
        n.a[0] = cat(ldtr(aa), ldtr(aa + 512)); n.a[1] = cat(ldtr(aa + 4096), ldtr(aa + 4608));
#pragma unroll
        for (int j = 0; j < 10; ++j) n.h[j] = ld128(ha + j * 1024);
        as = as == L1_B - 8192 ? 0 : as + 8192;
        hs = hs == (NB1 - 1) * 10240 ? 0 : hs + 10240;
        naa = aa0 + as; nha = ha0 + hs;
#pragma unroll
        for (int j = 0; j < 10; ++j) acc[j % 5] = mfma32(o.a[j / 5], o.h[j], acc[j % 5]);
        SG(0x100, 2);
#pragma unroll
        for (int j = 0; j < 6; ++j) { SG(0x8, 1); SG(0x100, 2); }
#pragma unroll
        for (int j = 0; j < 4; ++j) { SG(0x8, 1); SG(0x6, 2); }
        asm volatile("" : "+v"(naa), "+v"(nha));
        SB();
      };
      FN f0 = {}, f1;
#pragma unroll 1
      for (int r = 0; r < 32; r += 2) { step(f1, f0); step(f0, f1); }
      BAR();
#pragma unroll
      for (int j = 0; j < 10; ++j) acc[j % 5] = mfma32(f0.a[j / 5], f0.h[j], acc[j % 5]);
      const int go = WS_G + dt * 276480 + (fu * 2 + hw) * 5120 + (g & 1) * 1024 + (2 * ty + (g >> 1)) * 256 + c16 * 16;
#pragma unroll
      for (int j = 0; j < 5; j += 2) {
        const u32x4 o = swap16(__builtin_bit_cast(u32x4, gelu8(acc[j], acc[j < 4 ? j + 1 : j])));
        st128(rs, j < 4 || !(g & 1) ? go + j * 1024 : OOB, __builtin_bit_cast(s16x8, o));
      }
      BAR();
      const u32 ra = L0 + (4 * g + q4) * 640 + wave * 160 + p4 * 8;
      s16x8 f[5];
#pragma unroll
      for (int j = 0; j < 5; ++j) f[j] = cat(ldtr(ra + j * 32), ldtr(ra + j * 32 + 10240));
      publish_last(ws + WS_GFLAG + dt * 8192 + gi * 128, tag, lane, LCNT, 8);
      BAR();
      const int gw = WS_WO + (4 * (dt & 1) + wave) * 102400 + (xm * 4 + (dt >> 1)) * 5120 + lane * 16;
#pragma unroll
      for (int j = 0; j < 5; ++j) st128(rs, gw + j * 1024, f[j]);
      SB();
    } else {
      f32x4 acc[2][5];
#pragma unroll
      for (int t = 0; t < 2; ++t) {
        const u32x2 bb = *(const GAS u32x2*)(P.b1 + fu * 128 + wave * 64 + (u32)(t * 32 + g * 8));
        const f32x4 bi = {blo(bb[0]), bhi(bb[0]), blo(bb[1]), bhi(bb[1])};
#pragma unroll
        for (int j = 0; j < 5; ++j) acc[t][j] = bi;
      }
      s16x8 a1[2] = {}, h1[5] = {};
      constexpr int RP = 256;
      const u32 aa0 = L0 + (8 * g + q4) * RP + ((2 * wave) ^ (q4 | ((g & 1) << 2))) * 32 + p4 * 8;
      const u32 ha0 = L0 + L1_B + lane * 16;
      u32 as = 6 * 8192, hs = 0, naa = aa0 + as, nab = naa ^ 32, nha = ha0;
#pragma unroll 1
      for (int r = 0; r < 32; ++r) {
        const u32 aa = naa, ab = nab, ha = nha;
        BAR();
        s16x8 a0[2], h0[5];
        a0[0] = cat(ldtr(aa), ldtr(aa + 4 * RP)); h0[0] = ld128(ha); a0[1] = cat(ldtr(ab), ldtr(ab + 4 * RP));
#pragma unroll
        for (int j = 1; j < 5; ++j) h0[j] = ld128(ha + j * 1024);
#pragma unroll
        for (int j = 0; j < 5; ++j) { acc[0][j] = mfma32(a1[0], h1[j], acc[0][j]); acc[1][j] = mfma32(a1[1], h1[j], acc[1][j]); }
#pragma unroll
        for (int j = 0; j < 9; ++j) { SG(0x8, 1); SG(0x100, 1); }
        SG(0x8, 1);
        SB();
        a1[0] = cat(ldtr(aa + 32 * RP), ldtr(aa + 36 * RP)); a1[1] = cat(ldtr(ab + 32 * RP), ldtr(ab + 36 * RP));
#pragma unroll
        for (int j = 0; j < 5; ++j) h1[j] = ld128(ha + 5120 + j * 1024);
        as = as == L1_B - 64 * RP ? 0 : as + 64 * RP;
        hs = hs == (NB1 - 1) * 10240 ? 0 : hs + 10240;
        naa = aa0 + as; nab = naa ^ 32; nha = ha0 + hs;
#pragma unroll
        for (int j = 0; j < 5; ++j) { acc[0][j] = mfma32(a0[0], h0[j], acc[0][j]); acc[1][j] = mfma32(a0[1], h0[j], acc[1][j]); }
        SG(0x100, 1);
#pragma unroll
        for (int j = 0; j < 4; ++j) { SG(0x8, 1); SG(0x100, 2); }
#pragma unroll
        for (int j = 0; j < 5; ++j) { SG(0x8, 1); SG(0x6, 2); }
        SG(0x8, 1);
        asm volatile("" : "+v"(naa), "+v"(nab), "+v"(nha));
        SB();
      }
      BAR();
#pragma unroll
      for (int j = 0; j < 5; ++j) { acc[0][j] = mfma32(a1[0], h1[j], acc[0][j]); acc[1][j] = mfma32(a1[1], h1[j], acc[1][j]); }
      const int go = WS_G + dt * 276480 + (fu * 2 + wave) * 5120 + lane * 16;
#pragma unroll
      for (int j = 0; j < 5; ++j) st128(rs, go + j * 1024, gelu8(acc[0][j], acc[1][j]));
    }
    if (!late && !narrow) {
      publish_last(ws + WS_GFLAG + dt * 8192 + gi * 128, tag, lane, LCNT, 8);
      BAR();
    }
    ldx();
    begin3();
  }

  {
    rounds(roleA ? 5 : 1, roleA ? R_JOIN : PUBR, true);
    {
      asm volatile("" : "+v"(x01), "+v"(x23), "+v"(x4), "+v"(bs));
      const float bf = blo(bs);
      x01 = swap16(x01); x23 = swap16(x23);
      oc[0][0] += f32x4{blo(x01[0]), bhi(x01[0]), blo(x01[1]), bhi(x01[1])} + bf;
      oc[0][1] += f32x4{blo(x01[2]), bhi(x01[2]), blo(x01[3]), bhi(x01[3])} + bf;
      oc[0][2] += f32x4{blo(x23[0]), bhi(x23[0]), blo(x23[1]), bhi(x23[1])} + bf;
      oc[0][3] += f32x4{blo(x23[2]), bhi(x23[2]), blo(x23[3]), bhi(x23[3])} + bf;
      oc[0][4] += f32x4{blo(x4[0]), bhi(x4[0]), blo(x4[1]), bhi(x4[1])} + bf;
      if (late) publish_last(ws + WS_GFLAG + dt * 8192 + gi * 128, tag, lane, LCNT, 8);
      else if (roleA || narrow)
        publish(ws + (roleA ? WS_OFLAG + (2 * rt + hw) * 2048 + (ty * 8 + hg) * 128 : WS_GFLAG + (4 * (dt & 1) + wave) * 8192 + (16 + xm * 4 + (dt >> 1)) * 128), tag, lane);
      SB();
    }
    rounds(roleA ? R_JOIN : PUBR, 27, true);
    na3 = oa0 + sa3; nb3 = na3 ^ 8;
    static_assert(36 % NB3 < 2, "");
    u32 ew = L0 + L3_B + 20480 + wave * 5120 + lane * 16, er = L0 + L3_B + 20480 + (wave ^ 1) * 5120 + lane * 16, xo4 = xr + 128 + g * 8;
    asm volatile("" : "+v"(ew), "+v"(er), "+v"(xo4));
    rounds(27, 37, false);
    mma(fa0);
#pragma unroll
    for (int j = 0; j < 5; ++j) *(LAS f32x4*)(ew + j * 1024) = oc[1][j];
    SG(0x8, 6);
#pragma unroll
    for (int j = 0; j < 4; ++j) { SG(0x200, 1); SG(0x8, 1); }
    SG(0x200, 1);
    SB();
    BAR();
#pragma unroll
    for (int j = 0; j < 5; ++j) oc[1][j] = *(const LAS f32x4*)(er + j * 1024);
    SB();
#pragma unroll
    for (int j = 0; j < 5; ++j) oc[0][j] += oc[1][j];
    *(GAS u32x4*)(P.out + xo) = swap16(__builtin_bit_cast(u32x4, cat(pk4(oc[0][0]), pk4(oc[0][1]))));
    *(GAS u32x4*)(P.out + xo + 64) = swap16(__builtin_bit_cast(u32x4, cat(pk4(oc[0][2]), pk4(oc[0][3]))));
    *(GAS s16x4*)(P.out + xo4) = pk4(oc[0][4]);
    asm volatile(".Lcode_exit_b:" ::: "memory");
  }
}

extern "C" void kernel_launch(void* const* d_in, const int* in_sizes, int n_in, void* d_out, int out_size, void* d_ws,
                              size_t ws_size, hipStream_t stream) {
  Params p;
  p.x = (const char*)d_in[0];
  p.wqkv = (const char*)d_in[1];
  p.wo = (const char*)d_in[2];
  p.w1 = (const char*)d_in[4];
  p.b1 = (const char*)d_in[5];
  p.w2 = (const char*)d_in[6];
  p.b2 = (const char*)d_in[7];
  p.out = (char*)d_out;
  p.ws = (char*)d_ws;
  (void)hipFuncSetAttribute((const void*)fused_fwd, hipFuncAttributeMaxDynamicSharedMemorySize, L_BYTES);
  fused_fwd<<<256, 320, L_BYTES, stream>>>(p);
}
```
